```python
import torch

def _softmax(x, axis=-1):
    return torch.softmax(x, dim=axis)
def _gelu(x):
    return torch.nn.functional.gelu(x, approximate="tanh")
def _silu(x):
    return torch.nn.functional.silu(x)
def _sigmoid(x):
    return torch.sigmoid(x)
def _layernorm(x, axis=-1, eps=1e-5):
    m = x.mean(dim=axis, keepdim=True)
    v = x.var(dim=axis, keepdim=True, unbiased=False)
    return (x - m) / torch.sqrt(v + eps)
def _scatter_add(tgt, idx, src, axis=0):
    return tgt.clone().index_add_(axis, idx, src)
def _segment_sum(src, seg, n):
    out = torch.zeros((n,) + src.shape[1:], dtype=src.dtype)
    return out.index_add_(0, seg, src)
def _pad(x, pw):
    flat = tuple(v for pair in reversed(pw) for v in pair)
    return torch.nn.functional.pad(x, flat)

kernel_name = 'gated_mix__linear_attn_block__mixer_channel_mlp__prompt'

def setup_inputs():

    _w = (lambda a: (lambda t: t.to(torch.bfloat16) if t.is_floating_point() else t)(torch.as_tensor(a)))
    def _A_setup():
        B, S, D = 1, 2048, 576
        return {
            "x": _w((lambda *s: torch.randn(*s, dtype=torch.bfloat16) * 0.05)(B, S, D)),
            "wqkv": _w((lambda *s: torch.randn(*s, dtype=torch.bfloat16) * 0.8)(D, 3 * D)),
            "wo": _w((lambda *s: torch.randn(*s, dtype=torch.bfloat16) * 0.2)(D, D)),
        }
    _A_in = _A_setup()

    def _B_setup():
        B, S, D, F = 1, 2048, 576, 4608
        return {
            "x": _w((lambda *s: torch.randn(*s, dtype=torch.bfloat16) * 0.05)(B, S, D)),
            "w1": _w((lambda *s: torch.randn(*s, dtype=torch.bfloat16) * 1.6)(D, F) * (D ** -0.5)),
            "b1": _w((lambda *s: torch.randn(*s, dtype=torch.bfloat16) * 0.4)(F)),
            "w2": _w((lambda *s: torch.randn(*s, dtype=torch.bfloat16) * 0.05)(F, D) * (F ** -0.5)),
            "b2": _w((lambda *s: torch.randn(*s, dtype=torch.bfloat16) * 0.05)(D)),
        }
    _B_in = _B_setup()

    return {
        "A_x": _A_in["x"],
        "A_wqkv": _A_in["wqkv"],
        "A_wo": _A_in["wo"],
        "B_x": _B_in["x"],
        "B_w1": _B_in["w1"],
        "B_b1": _B_in["b1"],
        "B_w2": _B_in["w2"],
        "B_b2": _B_in["b2"],
        "gate_w": _w((lambda *s: torch.randn(*s, dtype=torch.bfloat16) * 0.05)(576) * 16.0),
    }

def reference(A_x, A_wqkv, A_wo, B_x, B_w1, B_b1, B_w2, B_b2, gate_w):
    def _A_ref(x, wqkv, wo):
        B, S, D = x.shape
        H = 32; Dh = D // H
        qkv = (lambda x, *d: x.permute(*d))((x @ wqkv).reshape(B, S, 3, H, Dh), 2, 0, 3, 1, 4)
        q, k, v = qkv[0], qkv[1], qkv[2]
        phi = lambda t: torch.where(t > 0, t + 1, torch.exp(t)) + 1e-6
        pq, pk = phi(q), phi(k)
        kv = torch.einsum("bhsd,bhse->bhde", pk, v)
        z = torch.einsum("bhsd->bhd", pk)
        num = torch.einsum("bhqd,bhde->bhqe", pq, kv)
        den = torch.einsum("bhqd,bhd->bhq", pq, z)[..., None] + 1e-6
        o = num / den
        return (lambda x, *d: x.permute(*d))(o, 0, 2, 1, 3).reshape(B, S, D) @ wo

    def _B_ref(x, w1, b1, w2, b2):
        y = _layernorm(x, axis=-1)
        h = _gelu(torch.einsum("bsd,df->bsf", y, w1) + b1)
        return torch.einsum("bsf,fd->bsd", h, w2) + b2

    x = A_x
    g = _sigmoid((x * gate_w).sum(-1, keepdims=True))
    a = _A_ref(x, A_wqkv, A_wo)
    b = _B_ref(x, B_w1, B_b1, B_w2, B_b2)
    return g * a + (1 - g) * b
```

```cpp
#include <hip/hip_runtime.h>
#include <hip/hip_bf16.h>
typedef unsigned short u16;
__device__ __forceinline__ float bf2f(u16 v){ return __uint_as_float(((unsigned)v)<<16); }
__device__ __forceinline__ u16 f2bf(float f){ unsigned u=__float_as_uint(f); u += 0x7fffu + ((u>>16)&1u); return (u16)(u>>16); }
constexpr int S=2048,D=576,F=4608,H=32,DH=18;

__global__ void k_stats(const u16* x,const u16* gw,float* y,float* g){
  int row=blockIdx.x; int t=threadIdx.x;
  float v[9]; float s=0,gs=0;
  for(int i=0;i<9;i++){ v[i]=bf2f(x[row*D+t+64*i]); s+=v[i]; gs+=v[i]*bf2f(gw[t+64*i]); }
  for(int o=32;o;o>>=1){ s+=__shfl_xor(s,o); gs+=__shfl_xor(gs,o);}
  float m=s/D; float q=0; for(int i=0;i<9;i++){ q+=(v[i]-m)*(v[i]-m);}
  for(int o=32;o;o>>=1) q+=__shfl_xor(q,o);
  float r=1.f/sqrtf(q/D+1e-5f);
  for(int i=0;i<9;i++) y[row*D+t+64*i]=bf2f(f2bf((v[i]-m)*r));
  if(t==0) g[row]=1.f/(1.f+expf(-gs));
}
template<int MODE,bool ABF>
__global__ void k_gemm(const void* A,const u16* B,const u16* bias,float* C,int M,int N,int K){
  __shared__ float sa[16][17], sb[16][17];
  int tx=threadIdx.x&15, ty=threadIdx.x>>4; int row=blockIdx.y*16+ty, col=blockIdx.x*16+tx; float acc=0;
  for(int k0=0;k0<K;k0+=16){
    sa[ty][tx]= ABF? bf2f(((const u16*)A)[(size_t)row*K+k0+tx]) : ((const float*)A)[(size_t)row*K+k0+tx];
    sb[ty][tx]= bf2f(B[(size_t)(k0+ty)*N+col]);
    __syncthreads();
    for(int k=0;k<16;k++) acc+=sa[ty][k]*sb[k][tx];
    __syncthreads();
  }
  if(MODE==1){ float u=acc+bf2f(bias[col]); float t=tanhf(0.7978845608f*(u+0.044715f*u*u*u)); acc=bf2f(f2bf(0.5f*u*(1.f+t))); }
  if(MODE==2){ acc+=bf2f(bias[col]); }
  C[(size_t)row*N+col]=acc;
}
__device__ __forceinline__ float phi(float t){ return (t>0? t+1.f: expf(t))+1e-6f; }
__global__ void k_kv(const float* qkv,float* kv){
  int h=blockIdx.x,t=threadIdx.x; if(t>=342) return; int d=t/19,e=t%19; float acc=0;
  for(int s=0;s<S;s++){ float pk=phi(qkv[(size_t)s*1728+576+h*18+d]); float v= e<18? qkv[(size_t)s*1728+1152+h*18+e]:1.f; acc+=pk*v; }
  kv[h*342+t]=acc;
}
__global__ void k_o(const float* qkv,const float* kv,float* o){ int s=blockIdx.x; int t=threadIdx.x;
  int h=t/18,e=t%18; float num=0,den=0; for(int d=0;d<18;d++){ float pq=phi(qkv[(size_t)s*1728+h*18+d]); num+=pq*kv[h*342+d*19+e]; den+=pq*kv[h*342+d*19+18]; }
  o[s*D+t]=bf2f(f2bf(num/(den+1e-6f)));
}
__global__ void k_fin(const float* a,const float* b,const float* g,u16* out){ int i=blockIdx.x*256+threadIdx.x; float gg=g[i/D]; out[i]=f2bf(gg*a[i]+(1.f-gg)*b[i]); }

extern "C" void kernel_launch(void* const* d_in, const int* in_sizes, int n_in, void* d_out, int out_size, void* d_ws, size_t ws_size, hipStream_t stream){
  const u16* x=(const u16*)d_in[0]; const u16* wqkv=(const u16*)d_in[1]; const u16* wo=(const u16*)d_in[2];
  const u16* w1=(const u16*)d_in[4]; const u16* b1=(const u16*)d_in[5]; const u16* w2=(const u16*)d_in[6]; const u16* b2=(const u16*)d_in[7]; const u16* gw=(const u16*)d_in[8];
  float* ws=(float*)d_ws; float* y=ws; float* g=y+S*D; float* qkv=g+S; float* h=qkv+S*1728; float* kv=h+(size_t)S*F; float* o=kv+32*342+64; float* a=o+S*D; float* b=a+S*D;
  k_stats<<<S,64,0,stream>>>(x,gw,y,g);
  k_gemm<0,true><<<dim3(1728/16,S/16),256,0,stream>>>(x,wqkv,nullptr,qkv,S,1728,D);
  k_gemm<1,false><<<dim3(F/16,S/16),256,0,stream>>>(y,w1,b1,h,S,F,D);
  k_kv<<<32,384,0,stream>>>(qkv,kv);
  k_o<<<S,576,0,stream>>>(qkv,kv,o);
  k_gemm<0,false><<<dim3(D/16,S/16),256,0,stream>>>(o,wo,nullptr,a,S,D,D);
  k_gemm<2,false><<<dim3(D/16,S/16),256,0,stream>>>(h,w2,b2,b,S,D,F);
  k_fin<<<S*D/256,256,0,stream>>>(a,b,g,(u16*)d_out);
}
```

```cpp
#include <hip/hip_runtime.h>
typedef unsigned u32;
__global__ __launch_bounds__(256, 1) void gm_fused(
    const void* x, u32 xn, u32 xf, const void* w1, u32 w1n, u32 w1f, const void* wq, u32 wqn, u32 wqf, void* ws, u32 wsn, u32 wsf,
    const void* w2, u32 w2n, u32 w2f, const void* wo, u32 won, u32 wof, void* out, u32 on, u32 of, const void* gw, u32 gn, u32 gf,
    const void* b1, u32 b1n, u32 b1f, const void* b2, u32 b2n, u32 b2f) {
  __shared__ __attribute__((aligned(16))) char lds[163840];
  u32 tid = threadIdx.x;
  asm volatile(
    "PC0_%=:\n"
    "s_load_dwordx16 s[8:23], %1, 0\n"
    "s_load_dwordx16 s[24:39], %1, 64\n"
    "s_load_dwordx8 s[40:47], %1, 128\n"
    "v_and_b32 v255, 63, v0\n"
    "v_lshrrev_b32 v224, 6, v0\n"
    "s_and_b32 s4, %2, 7\n"
    "s_bfe_u32 s5, %2, 0x20003\n"
    "s_lshr_b32 s6, %2, 5\n"
    "v_readfirstlane_b32 s7, v224\n"
    "v_lshrrev_b32 v224, 3, v255\n"
    "v_and_b32 v225, 7, v255\n"
    "v_lshrrev_b32 v226, 4, v255\n"
    "v_xor_b32 v225, v225, v226\n"
    "v_mul_u32_u24 v224, 1152, v224\n"
    "v_lshl_add_u32 v247, v225, 4, v224\n"
    "v_xor_b32 v225, 4, v225\n"
    "v_lshl_add_u32 v248, v225, 4, v224\n"
    "v_add_u32 v248, 8192, v248\n"
    "v_add_u32 v249, 16384, v247\n"
    "v_add_u32 v250, 16384, v248\n"
    "s_lshl_b32 s87, s6, 2\n"
    "s_add_u32 s87, s87, s7\n"
    "s_mul_i32 s57, s87, 73728\n"
    "s_mul_i32 s56, s6, 1\n"
    "s_mul_i32 s83, s4, 1\n"
    "s_add_u32 s56, s56, s83\n"
    "s_sub_u32 s83, s56, 9\n"
    "s_cmp_ge_u32 s56, 9\n"
    "s_cselect_b32 s56, s83, s56\n"
    "s_lshl_b32 s65, s7, 14\n"
    "s_lshl_b32 s60, s56, 7\n"
    "s_cmp_eq_u32 s5, 3\n"
    "s_cbranch_scc1 ROLEB_%=\n"
    "s_and_b32 s85, s7, 1\n"
    "s_lshl_b32 s80, s85, 4\n"
    "s_lshr_b32 s81, s7, 1\n"
    "s_lshl_b32 s81, s81, 2\n"
    "s_add_u32 s80, s80, s81\n"
    "s_mul_i32 s80, s80, 9216\n"
    "s_mul_i32 s81, s4, 1152\n"
    "s_mul_i32 s82, s5, 384\n"
    "s_add_u32 s81, s81, s82\n"
    "s_add_u32 s80, s80, s81\n"
    "v_bfe_u32 v224, v255, 3, 2\n"
    "v_lshrrev_b32 v225, 5, v255\n"
    "v_lshl_add_u32 v224, v225, 3, v224\n"
    "v_mul_u32_u24 v224, 9216, v224\n"
    "v_lshrrev_b32 v225, 3, v255\n"
    "v_and_b32 v225, 6, v225\n"
    "v_and_b32 v226, 7, v255\n"
    "v_xor_b32 v227, v225, v226\n"
    "v_lshl_add_u32 v224, v227, 4, v224\n"
    "v_add_u32 v242, s80, v224\n"
    "v_add_u32 v243, 128, v242\n"
    "v_add_u32 v244, 256, v242\n"
    "s_mul_i32 s61, s7, 3072\n"
    "s_add_u32 s61, s61, 65536\n"
    "v_lshrrev_b32 v225, 3, v255\n"
    "v_lshlrev_b32 v227, 4, v227\n"
    "v_lshl_add_u32 v227, v225, 7, v227\n"
    "v_add_u32 v237, s61, v227\n"
    "s_mul_i32 s58, s56, 589824\n"
    "s_waitcnt lgkmcnt(0)\n"
    "s_add_u32 s8, s8, s57\n"
    "s_addc_u32 s9, s9, 0\n"
    "s_mov_b32 s10, 73728\n"
    "s_add_u32 m0, s61, 0\n"
    "s_nop 0\n"
    "buffer_load_dwordx4 v242, s[12:15], s58 offen lds\n"
    "s_add_u32 m0, s61, 1024\n"
    "s_nop 0\n"
    "buffer_load_dwordx4 v243, s[12:15], s58 offen lds\n"
    "s_add_u32 m0, s61, 2048\n"
    "s_nop 0\n"
    "buffer_load_dwordx4 v244, s[12:15], s58 offen lds\n"
    "s_add_u32 m0, s61, 12288\n"
    "s_add_u32 s58, s58, 294912\n"
    "buffer_load_dwordx4 v242, s[12:15], s58 offen lds\n"
    "s_add_u32 m0, s61, 13312\n"
    "s_nop 0\n"
    "buffer_load_dwordx4 v243, s[12:15], s58 offen lds\n"
    "s_add_u32 m0, s61, 14336\n"
    "s_nop 0\n"
    "buffer_load_dwordx4 v244, s[12:15], s58 offen lds\n"
    "s_add_u32 m0, s65, 0\n"
    "s_nop 0\n"
    "buffer_load_dwordx4 v247, s[8:11], s60 offen offset:0 lds\n"
    "buffer_load_dwordx4 v248, s[8:11], s60 offen offset:1024 lds\n"
    "buffer_load_dwordx4 v249, s[8:11], s60 offen offset:2048 lds\n"
    "buffer_load_dwordx4 v250, s[8:11], s60 offen offset:3072 lds\n"
    "s_add_u32 m0, s65, 4096\n"
    "s_add_u32 s60, s60, 36864\n"
    "buffer_load_dwordx4 v247, s[8:11], s60 offen offset:0 lds\n"
    "buffer_load_dwordx4 v248, s[8:11], s60 offen offset:1024 lds\n"
    "buffer_load_dwordx4 v249, s[8:11], s60 offen offset:2048 lds\n"
    "buffer_load_dwordx4 v250, s[8:11], s60 offen offset:3072 lds\n"
    "v_and_b32 v224, 15, v255\n"
    "v_lshrrev_b32 v225, 4, v255\n"
    "v_lshrrev_b32 v226, 1, v224\n"
    "v_xor_b32 v226, v225, v226\n"
    "v_lshlrev_b32 v226, 4, v226\n"
    "v_lshl_add_u32 v226, v224, 7, v226\n"
    "v_add_u32 v245, s65, v226\n"
    "v_xor_b32 v246, 64, v245\n"
    "v_lshrrev_b32 v224, 5, v255\n"
    "v_mul_u32_u24 v224, 3072, v224\n"
    "v_bfe_u32 v225, v255, 2, 3\n"
    "v_lshl_add_u32 v224, v225, 7, v224\n"
    "v_and_b32 v225, 3, v255\n"
    "v_lshl_add_u32 v224, v225, 3, v224\n"
    "v_bfe_u32 v225, v255, 3, 2\n"
    "v_lshl_add_u32 v224, v225, 5, v224\n"
    "v_add_u32 v238, 65536, v224\n"
    "v_xor_b32 v239, 32, v238\n"
    "v_xor_b32 v240, 64, v238\n"
    "v_xor_b32 v241, 96, v238\n"
    "v_and_b32 v224, 1, v255\n"
    "v_lshlrev_b32 v224, 4, v224\n"
    "v_mov_b32 v225, 0x3f80\n"
    "v_lshlrev_b32 v224, v224, v225\n"
    "v_bfe_u32 v225, v255, 1, 3\n"
    "v_cmp_eq_u32 vcc, 0, v225\n"
    "s_nop 1\n"
    "v_cndmask_b32 v226, 0, v224, vcc\n"
    "v_accvgpr_write_b32 a168, v226\n"
    "v_cmp_eq_u32 vcc, 1, v225\n"
    "s_nop 1\n"
    "v_cndmask_b32 v226, 0, v224, vcc\n"
    "v_accvgpr_write_b32 a169, v226\n"
    "v_cmp_eq_u32 vcc, 2, v225\n"
    "s_nop 1\n"
    "v_cndmask_b32 v226, 0, v224, vcc\n"
    "v_accvgpr_write_b32 a170, v226\n"
    "v_cmp_eq_u32 vcc, 3, v225\n"
    "s_nop 1\n"
    "v_cndmask_b32 v226, 0, v224, vcc\n"
    "v_accvgpr_write_b32 a171, v226\n"
    "v_cmp_eq_u32 vcc, 4, v225\n"
    "s_nop 1\n"
    "v_cndmask_b32 v226, 0, v224, vcc\n"
    "v_accvgpr_write_b32 a172, v226\n"
    "v_cmp_eq_u32 vcc, 5, v225\n"
    "s_nop 1\n"
    "v_cndmask_b32 v226, 0, v224, vcc\n"
    "v_accvgpr_write_b32 a173, v226\n"
    "v_cmp_eq_u32 vcc, 6, v225\n"
    "s_nop 1\n"
    "v_cndmask_b32 v226, 0, v224, vcc\n"
    "v_accvgpr_write_b32 a174, v226\n"
    "v_cmp_eq_u32 vcc, 7, v225\n"
    "s_nop 1\n"
    "v_cndmask_b32 v226, 0, v224, vcc\n"
    "v_accvgpr_write_b32 a175, v226\n"
    "s_mov_b32 s85, s81\n"
    "v_lshlrev_b32 v188, 3, v255\n"
    "v_and_b32 v187, 31, v255\n"
    "v_lshrrev_b32 v224, 5, v255\n"
    "v_lshlrev_b32 v186, 4, v187\n"
    "v_lshlrev_b32 v187, 2, v187\n"
    "v_mul_u32_u24 v185, 36864, v224\n"
    "v_add_u32 v184, v185, v186\n"
    "v_add_u32 v185, v185, v187\n"
    "v_lshl_add_u32 v182, v224, 14, v186\n"
    "v_lshlrev_b32 v254, 4, v255\n"
    "v_lshlrev_b32 v224, 3, v255\n"
    "s_lshl_b32 s81, s4, 2\n"
    "s_add_u32 s81, s81, s5\n"
    "s_mul_i32 s84, s81, 1152\n"
    "s_lshl_b32 s86, s87, 6\n"
    "s_add_u32 s86, s86, s81\n"
    "s_lshl_b32 s86, s86, 9\n"
    "s_add_u32 s86, s86, 1179648\n"
    "s_add_u32 s56, s56, 1\n"
    "s_cmp_eq_u32 s56, 9\n"
    "s_cselect_b32 s56, 0, s56\n"
    "s_lshl_b32 s60, s56, 7\n"
    "s_mul_i32 s58, s56, 589824\n"
    "s_mov_b32 s57, 0\n"
    "s_mov_b32 s67, 200000\n"
    "s_getpc_b64 s[82:83]\n"
    "PCXA_%=:\n"
    "s_lshr_b32 s81, %2, 3\n"
    "s_lshl_b32 s81, s81, 2\n"
    "s_add_u32 s81, s81, s7\n"
    "s_lshl_b32 s81, s81, 9\n"
    "s_min_u32 s81, s81, PCE_%=-PC0_%=-512\n"
    "s_sub_u32 s82, s82, PCXA_%=-PC0_%=\n"
    "s_subb_u32 s83, s83, 0\n"
    "s_add_u32 s82, s82, s81\n"
    "s_addc_u32 s83, s83, 0\n"
    "global_load_dwordx2 v[190:191], v224, s[82:83]\n"
    "buffer_load_dwordx4 v[160:163], v184, s[8:11], s84 offen\n"
    "buffer_load_dwordx4 v[164:167], v184, s[8:11], s84 offen offset:512\n"
    "buffer_load_dword v168, v185, s[8:11], s84 offen offset:1024\n"
    "buffer_load_dwordx4 v[172:175], v186, s[36:39], 0 offen\n"
    "buffer_load_dwordx4 v[176:179], v186, s[36:39], 0 offen offset:512\n"
    "buffer_load_dword v180, v187, s[36:39], 0 offen offset:1024\n"
    "buffer_load_dword v181, off, s[20:23], 0 sc1\n"
    "buffer_load_dwordx2 v[234:235], v188, s[40:43], s85 offen\n"
    "s_add_u32 m0, s65, 8192\n"
    "s_nop 0\n"
    "buffer_load_dwordx4 v247, s[8:11], s60 offen offset:0 lds\n"
    "buffer_load_dwordx4 v248, s[8:11], s60 offen offset:1024 lds\n"
    "buffer_load_dwordx4 v249, s[8:11], s60 offen offset:2048 lds\n"
    "buffer_load_dwordx4 v250, s[8:11], s60 offen offset:3072 lds\n"
    "s_waitcnt vmcnt(13)\n"
    "s_barrier\n"
    "ds_read_b64_tr_b16 a[64:65], v238 offset:0\n"
    "ds_read_b64_tr_b16 a[66:67], v238 offset:6144\n"
    "ds_read_b128 a[0:3], v245 offset:0\n"
    "ds_read_b64_tr_b16 a[68:69], v238 offset:12288\n"
    "ds_read_b64_tr_b16 a[70:71], v238 offset:18432\n"
    "ds_read_b128 a[16:19], v246 offset:0\n"
    "ds_read_b128 a[20:23], v246 offset:2048\n"
    "ds_read_b128 a[4:7], v245 offset:2048\n"
    "ds_read_b128 a[24:27], v246 offset:4096\n"
    "ds_read_b128 a[8:11], v245 offset:4096\n"
    "ds_read_b128 a[28:31], v246 offset:6144\n"
    "ds_read_b128 a[12:15], v245 offset:6144\n"
    "ds_read_b64_tr_b16 a[72:73], v239 offset:0\n"
    "ds_read_b64_tr_b16 a[74:75], v239 offset:6144\n"
    "ds_read_b64_tr_b16 a[76:77], v239 offset:12288\n"
    "ds_read_b64_tr_b16 a[78:79], v239 offset:18432\n"
    "s_waitcnt lgkmcnt(13)\n"
    "v_mfma_f32_16x16x32_bf16 v[0:3], a[64:67], a[0:3], 0\n"
    "s_add_u32 m0, s65, 12288\n"
    "ds_read_b64_tr_b16 a[80:81], v240 offset:0\n"
    "s_waitcnt lgkmcnt(11)\n"
    "v_mfma_f32_16x16x32_bf16 v[0:3], a[68:71], a[16:19], v[0:3]\n"
    "s_add_u32 s60, s60, 36864\n"
    "buffer_load_dwordx4 v247, s[8:11], s60 offen offset:0 lds\n"
    "s_waitcnt lgkmcnt(10)\n"
    "v_mfma_f32_16x16x32_bf16 v[4:7], a[68:71], a[20:23], 0\n"
    "s_lshl_b32 s48, s5, 8\n"
    "ds_read_b64_tr_b16 a[82:83], v240 offset:6144\n"
    "s_waitcnt lgkmcnt(10)\n"
    "v_mfma_f32_16x16x32_bf16 v[4:7], a[64:67], a[4:7], v[4:7]\n"
    "s_lshl_b32 s49, s5, 5\n"
    "ds_read_b64_tr_b16 a[84:85], v240 offset:12288\n"
    "s_waitcnt lgkmcnt(9)\n"
    "v_mfma_f32_16x16x32_bf16 v[8:11], a[64:67], a[8:11], 0\n"
    "s_add_u32 s49, s49, 1024\n"
    "ds_read_b64_tr_b16 a[86:87], v240 offset:18432\n"
    "v_mfma_f32_16x16x32_bf16 v[8:11], a[68:71], a[24:27], v[8:11]\n"
    "s_lshl_b32 s50, s7, 7\n"
    "buffer_load_dwordx4 v248, s[8:11], s60 offen offset:1024 lds\n"
    "s_waitcnt lgkmcnt(9)\n"
    "v_mfma_f32_16x16x32_bf16 v[12:15], a[68:71], a[28:31], 0\n"
    "s_add_u32 s51, s50, 64\n"
    "ds_read_b64_tr_b16 a[88:89], v241 offset:0\n"
    "s_waitcnt lgkmcnt(9)\n"
    "v_mfma_f32_16x16x32_bf16 v[12:15], a[64:67], a[12:15], v[12:15]\n"
    "s_waitcnt lgkmcnt(5)\n"
    "ds_read_b128 a[160:163], v237 offset:0\n"
    "v_mfma_f32_16x16x32_bf16 v[28:31], a[72:75], a[12:15], 0\n"
    "s_sub_u32 s52, s48, 1024\n"
    "ds_read_b64_tr_b16 a[90:91], v241 offset:6144\n"
    "v_mfma_f32_16x16x32_bf16 v[28:31], a[76:79], a[28:31], v[28:31]\n"
    "s_sub_u32 s53, s49, 1024\n"
    "ds_read_b128 a[164:167], v237 offset:12288\n"
    "v_mfma_f32_16x16x32_bf16 v[24:27], a[76:79], a[24:27], 0\n"
    "s_and_b32 s54, s7, 1\n"
    "ds_read_b64_tr_b16 a[92:93], v241 offset:12288\n"
    "v_mfma_f32_16x16x32_bf16 v[24:27], a[72:75], a[8:11], v[24:27]\n"
    "s_mul_i32 s55, s54, 36864\n"
    "buffer_load_dwordx4 v249, s[8:11], s60 offen offset:2048 lds\n"
    "v_mfma_f32_16x16x32_bf16 v[20:23], a[72:75], a[4:7], 0\n"
    "s_add_u32 s54, s55, s48\n"
    "ds_read_b64_tr_b16 a[94:95], v241 offset:18432\n"
    "v_mfma_f32_16x16x32_bf16 v[20:23], a[76:79], a[20:23], v[20:23]\n"
    "s_add_u32 s55, s55, s49\n"
    "v_mfma_f32_16x16x32_bf16 v[16:19], a[76:79], a[16:19], 0\n"
    "ds_read_b64_tr_b16 a[96:97], v238 offset:1024\n"
    "v_add_u32 v224, s50, v255\n"
    "v_mfma_f32_16x16x32_bf16 v[16:19], a[72:75], a[0:3], v[16:19]\n"
    "s_waitcnt lgkmcnt(7)\n"
    "buffer_load_dwordx4 v250, s[8:11], s60 offen offset:3072 lds\n"
    "v_mfma_f32_16x16x32_bf16 v[32:35], a[80:83], a[0:3], 0\n"
    "s_add_u32 m0, s61, 24576\n"
    "ds_read_b64_tr_b16 a[98:99], v238 offset:7168\n"
    "v_mul_u32_u24 v225, 3641, v224\n"
    "v_mfma_f32_16x16x32_bf16 v[32:35], a[84:87], a[16:19], v[32:35]\n"
    "s_cmp_eq_u32 s5, 3\n"
    "v_lshrrev_b32 v225, 16, v225\n"
    "v_mul_u32_u24 v226, 18, v225\n"
    "v_mfma_f32_16x16x32_bf16 v[36:39], a[84:87], a[20:23], 0\n"
    "s_cselect_b32 s81, 0, s5\n"
    "ds_read_b64_tr_b16 a[100:101], v238 offset:13312\n"
    "v_sub_u32 v224, v224, v226\n"
    "v_mfma_f32_16x16x32_bf16 v[36:39], a[80:83], a[4:7], v[36:39]\n"
    "s_mul_i32 s36, s4, 82944\n"
    "buffer_load_dwordx4 v242, s[12:15], s58 offen lds\n"
    "v_mfma_f32_16x16x32_bf16 v[40:43], a[80:83], a[8:11], 0\n"
    "s_add_u32 m0, s61, 25600\n"
    "ds_read_b64_tr_b16 a[102:103], v238 offset:19456\n"
    "v_lshrrev_b32 v226, 1, v224\n"
    "v_mfma_f32_16x16x32_bf16 v[40:43], a[84:87], a[24:27], v[40:43]\n"
    "s_mul_i32 s83, s4, 663552\n"
    "v_cmp_gt_u32 vcc, 2, v224\n"
    "v_add_u32 v226, s4, v226\n"
    "v_mfma_f32_16x16x32_bf16 v[44:47], a[84:87], a[28:31], 0\n"
    "s_and_b32 s77, %2, 0xe7\n"
    "ds_read_b64_tr_b16 a[104:105], v239 offset:1024\n"
    "v_and_b32 v226, 7, v226\n"
    "v_mfma_f32_16x16x32_bf16 v[44:47], a[80:83], a[12:15], v[44:47]\n"
    "s_waitcnt lgkmcnt(7)\n"
    "v_lshl_add_u32 v226, v226, 5, s48\n"
    "v_mov_b32 v227, s49\n"
    "v_mfma_f32_16x16x32_bf16 v[192:195], a[168:171], a[160:163], 0\n"
    "s_lshl_b32 s77, s77, 2\n"
    "ds_read_b64_tr_b16 a[106:107], v239 offset:7168\n"
    "v_cndmask_b32 v226, v226, v227, vcc\n"
    "v_mfma_f32_16x16x32_bf16 v[192:195], a[168:171], a[164:167], v[192:195]\n"
    "s_waitcnt lgkmcnt(6)\n"
    "buffer_load_dwordx4 v243, s[12:15], s58 offen lds\n"
    "v_mfma_f32_16x16x32_bf16 v[60:63], a[88:91], a[12:15], 0\n"
    "s_add_u32 m0, s61, 26624\n"
    "ds_read_b64_tr_b16 a[108:109], v239 offset:13312\n"
    "v_and_b32 v224, 1, v224\n"
    "v_mfma_f32_16x16x32_bf16 v[60:63], a[92:95], a[28:31], v[60:63]\n"
    "s_add_u32 s77, s77, s7\n"
    "ds_read_b128 a[160:163], v237 offset:1024\n"
    "v_mul_u32_u24 v225, 1152, v225\n"
    "v_mfma_f32_16x16x32_bf16 v[56:59], a[92:95], a[24:27], 0\n"
    "s_mul_i32 s75, s77, 24576\n"
    "ds_read_b64_tr_b16 a[110:111], v239 offset:19456\n"
    "v_lshl_add_u32 v224, v224, 4, v225\n"
    "v_mfma_f32_16x16x32_bf16 v[56:59], a[88:91], a[8:11], v[56:59]\n"
    "s_add_u32 s75, s75, 4194304\n"
    "buffer_load_dwordx4 v244, s[12:15], s58 offen lds\n"
    "v_mfma_f32_16x16x32_bf16 v[52:55], a[88:91], a[4:7], 0\n"
    "s_add_u32 m0, s61, 36864\n"
    "ds_read_b64_tr_b16 a[112:113], v240 offset:1024\n"
    "v_add_u32 v251, v224, v226\n"
    "v_mfma_f32_16x16x32_bf16 v[52:55], a[92:95], a[20:23], v[52:55]\n"
    "s_add_u32 s58, s58, 294912\n"
    "ds_read_b128 a[164:167], v237 offset:13312\n"
    "v_add_u32 v224, s51, v255\n"
    "v_mfma_f32_16x16x32_bf16 v[48:51], a[92:95], a[16:19], 0\n"
    "s_mul_i32 s82, s81, 221184\n"
    "ds_read_b64_tr_b16 a[114:115], v240 offset:7168\n"
    "v_mul_u32_u24 v225, 3641, v224\n"
    "v_mfma_f32_16x16x32_bf16 v[48:51], a[88:91], a[0:3], v[48:51]\n"
    "s_waitcnt lgkmcnt(8)\n"
    "buffer_load_dwordx4 v242, s[12:15], s58 offen lds\n"
    "v_mfma_f32_16x16x32_bf16 v[64:67], a[96:99], a[0:3], 0\n"
    "s_add_u32 m0, s61, 37888\n"
    "ds_read_b64_tr_b16 a[116:117], v240 offset:13312\n"
    "v_lshrrev_b32 v225, 16, v225\n"
    "v_mfma_f32_16x16x32_bf16 v[64:67], a[100:103], a[16:19], v[64:67]\n"
    "s_add_u32 s68, s83, s82\n"
    "v_mul_u32_u24 v226, 18, v225\n"
    "v_sub_u32 v224, v224, v226\n"
    "v_mfma_f32_16x16x32_bf16 v[68:71], a[100:103], a[20:23], 0\n"
    "s_mul_i32 s82, s81, 786432\n"
    "ds_read_b64_tr_b16 a[118:119], v240 offset:19456\n"
    "v_lshrrev_b32 v226, 1, v224\n"
    "v_mfma_f32_16x16x32_bf16 v[68:71], a[96:99], a[4:7], v[68:71]\n"
    "s_add_u32 s71, s75, s82\n"
    "v_cmp_gt_u32 vcc, 2, v224\n"
    "v_add_u32 v226, s4, v226\n"
    "v_mfma_f32_16x16x32_bf16 v[72:75], a[96:99], a[8:11], 0\n"
    "s_add_u32 s81, s81, 1\n"
    "ds_read_b64_tr_b16 a[120:121], v241 offset:1024\n"
    "v_and_b32 v226, 7, v226\n"
    "v_mfma_f32_16x16x32_bf16 v[72:75], a[100:103], a[24:27], v[72:75]\n"
    "buffer_load_dwordx4 v243, s[12:15], s58 offen lds\n"
    "v_mfma_f32_16x16x32_bf16 v[76:79], a[100:103], a[28:31], 0\n"
    "s_add_u32 m0, s61, 38912\n"
    "ds_read_b64_tr_b16 a[122:123], v241 offset:7168\n"
    "v_lshl_add_u32 v226, v226, 5, s52\n"
    "v_mfma_f32_16x16x32_bf16 v[76:79], a[96:99], a[12:15], v[76:79]\n"
    "s_waitcnt lgkmcnt(7)\n"
    "v_mov_b32 v227, s53\n"
    "v_cndmask_b32 v226, v226, v227, vcc\n"
    "v_mfma_f32_16x16x32_bf16 v[92:95], a[104:107], a[12:15], 0\n"
    "s_cmp_eq_u32 s81, 3\n"
    "ds_read_b64_tr_b16 a[124:125], v241 offset:13312\n"
    "v_and_b32 v224, 1, v224\n"
    "v_mfma_f32_16x16x32_bf16 v[92:95], a[108:111], a[28:31], v[92:95]\n"
    "s_cselect_b32 s81, 0, s81\n"
    "buffer_load_dwordx4 v244, s[12:15], s58 offen lds\n"
    "v_mfma_f32_16x16x32_bf16 v[88:91], a[108:111], a[24:27], 0\n"
    "s_add_u32 s56, s56, 1\n"
    "s_cmp_eq_u32 s56, 9\n"
    "s_cselect_b32 s56, 0, s56\n"
    "s_lshl_b32 s60, s56, 7\n"
    "ds_read_b64_tr_b16 a[126:127], v241 offset:19456\n"
    "v_mul_u32_u24 v225, 1152, v225\n"
    "v_mfma_f32_16x16x32_bf16 v[88:91], a[104:107], a[8:11], v[88:91]\n"
    "s_add_u32 m0, s65, 0\n"
    "v_lshl_add_u32 v224, v224, 4, v225\n"
    "v_add_u32 v252, v224, v226\n"
    "v_mfma_f32_16x16x32_bf16 v[84:87], a[104:107], a[4:7], 0\n"
    "s_mul_i32 s82, s81, 221184\n"
    "ds_read_b64_tr_b16 a[128:129], v238 offset:2048\n"
    "v_add_u32 v224, 512, v255\n"
    "v_mfma_f32_16x16x32_bf16 v[84:87], a[108:111], a[20:23], v[84:87]\n"
    "s_add_u32 s69, s83, s82\n"
    "v_mul_u32_u24 v225, 3641, v224\n"
    "v_lshrrev_b32 v225, 16, v225\n"
    "v_mfma_f32_16x16x32_bf16 v[80:83], a[108:111], a[16:19], 0\n"
    "s_mul_i32 s82, s81, 786432\n"
    "ds_read_b64_tr_b16 a[130:131], v238 offset:8192\n"
    "v_mul_u32_u24 v226, 18, v225\n"
    "v_mfma_f32_16x16x32_bf16 v[80:83], a[104:107], a[0:3], v[80:83]\n"
    "s_waitcnt lgkmcnt(9)\n"
    "buffer_load_dwordx4 v247, s[8:11], s60 offen offset:0 lds\n"
    "v_mfma_f32_16x16x32_bf16 v[192:195], a[172:175], a[160:163], v[192:195]\n"
    "s_mul_i32 s58, s56, 589824\n"
    "ds_read_b64_tr_b16 a[132:133], v238 offset:14336\n"
    "v_sub_u32 v224, v224, v226\n"
    "v_mfma_f32_16x16x32_bf16 v[192:195], a[172:175], a[164:167], v[192:195]\n"
    "s_waitcnt lgkmcnt(7)\n"
    "v_lshrrev_b32 v226, 1, v224\n"
    "v_cmp_gt_u32 vcc, 2, v224\n"
    "v_mfma_f32_16x16x32_bf16 v[96:99], a[112:115], a[0:3], 0\n"
    "s_add_u32 s57, s57, 1\n"
    "ds_read_b64_tr_b16 a[134:135], v238 offset:20480\n"
    "v_add_u32 v226, s4, v226\n"
    "v_mfma_f32_16x16x32_bf16 v[96:99], a[116:119], a[16:19], v[96:99]\n"
    "s_add_u32 s72, s75, s82\n"
    "buffer_load_dwordx4 v248, s[8:11], s60 offen offset:1024 lds\n"
    "v_mfma_f32_16x16x32_bf16 v[100:103], a[116:119], a[20:23], 0\n"
    "s_add_u32 s81, s81, 1\n"
    "ds_read_b64_tr_b16 a[136:137], v239 offset:2048\n"
    "v_and_b32 v226, 7, v226\n"
    "v_mfma_f32_16x16x32_bf16 v[100:103], a[112:115], a[4:7], v[100:103]\n"
    "s_cmp_eq_u32 s81, 3\n"
    "ds_read_b128 a[160:163], v237 offset:2048\n"
    "v_lshl_add_u32 v226, v226, 5, s54\n"
    "v_mfma_f32_16x16x32_bf16 v[104:107], a[112:115], a[8:11], 0\n"
    "s_cselect_b32 s81, 0, s81\n"
    "ds_read_b64_tr_b16 a[138:139], v239 offset:8192\n"
    "v_mov_b32 v227, s55\n"
    "v_mfma_f32_16x16x32_bf16 v[104:107], a[116:119], a[24:27], v[104:107]\n"
    "s_mul_i32 s82, s81, 221184\n"
    "buffer_load_dwordx4 v249, s[8:11], s60 offen offset:2048 lds\n"
    "v_mfma_f32_16x16x32_bf16 v[108:111], a[116:119], a[28:31], 0\n"
    "s_add_u32 s70, s83, s82\n"
    "ds_read_b64_tr_b16 a[140:141], v239 offset:14336\n"
    "v_cndmask_b32 v226, v226, v227, vcc\n"
    "v_mfma_f32_16x16x32_bf16 v[108:111], a[112:115], a[12:15], v[108:111]\n"
    "s_waitcnt lgkmcnt(8)\n"
    "ds_read_b128 a[164:167], v237 offset:14336\n"
    "v_and_b32 v224, 1, v224\n"
    "v_mfma_f32_16x16x32_bf16 v[124:127], a[120:123], a[12:15], 0\n"
    "s_mul_i32 s82, s81, 786432\n"
    "ds_read_b64_tr_b16 a[142:143], v239 offset:20480\n"
    "v_mul_u32_u24 v225, 1152, v225\n"
    "v_mfma_f32_16x16x32_bf16 v[124:127], a[124:127], a[28:31], v[124:127]\n"
    "s_add_u32 s73, s75, s82\n"
    "v_lshl_add_u32 v224, v224, 4, v225\n"
    "v_add_u32 v253, v224, v226\n"
    "v_mfma_f32_16x16x32_bf16 v[120:123], a[124:127], a[24:27], 0\n"
    "ds_read_b64_tr_b16 a[144:145], v240 offset:2048\n"
    "v_add_u32 v228, 122880, v254\n"
    "v_mfma_f32_16x16x32_bf16 v[120:123], a[120:123], a[8:11], v[120:123]\n"
    "buffer_load_dwordx4 v250, s[8:11], s60 offen offset:3072 lds\n"
    "v_mfma_f32_16x16x32_bf16 v[116:119], a[120:123], a[4:7], 0\n"
    "s_add_u32 m0, s65, 4096\n"
    "ds_read_b64_tr_b16 a[146:147], v240 offset:8192\n"
    "v_mov_b32 v224, 1536\n"
    "v_mfma_f32_16x16x32_bf16 v[116:119], a[124:127], a[20:23], v[116:119]\n"
    "s_add_u32 s60, s60, 36864\n"
    "v_mul_u32_u24 v224, s7, v224\n"
    "v_add_u32 v229, v224, v228\n"
    "v_mfma_f32_16x16x32_bf16 v[112:115], a[124:127], a[16:19], 0\n"
    "ds_read_b64_tr_b16 a[148:149], v240 offset:14336\n"
    "v_and_b32 v225, 48, v255\n"
    "v_mfma_f32_16x16x32_bf16 v[112:115], a[120:123], a[0:3], v[112:115]\n"
    "s_waitcnt lgkmcnt(9)\n"
    "buffer_load_dwordx4 v247, s[8:11], s60 offen offset:0 lds\n"
    "v_mfma_f32_16x16x32_bf16 v[128:131], a[128:131], a[0:3], 0\n"
    "ds_read_b64_tr_b16 a[150:151], v240 offset:20480\n"
    "v_add_u32 v225, v225, v224\n"
    "v_mfma_f32_16x16x32_bf16 v[128:131], a[132:135], a[16:19], v[128:131]\n"
    "v_add_u32 v230, 156672, v225\n"
    "v_and_b32 v225, 0xf0, v254\n"
    "s_mov_b32 s80, 0x3f803f80\n"
    "s_waitcnt vmcnt(19)\n"
    "v_readfirstlane_b32 s66, v181\n"
    "v_dot2_f32_bf16 v169, v160, s80, 0\n"
    "v_dot2_f32_bf16 v170, v160, v160, 0\n"
    "v_dot2_f32_bf16 v171, v160, v172, 0\n"
    "v_dot2_f32_bf16 v169, v161, s80, v169\n"
    "v_dot2_f32_bf16 v170, v161, v161, v170\n"
    "v_dot2_f32_bf16 v171, v161, v173, v171\n"
    "v_dot2_f32_bf16 v169, v162, s80, v169\n"
    "v_dot2_f32_bf16 v170, v162, v162, v170\n"
    "v_dot2_f32_bf16 v171, v162, v174, v171\n"
    "v_dot2_f32_bf16 v169, v163, s80, v169\n"
    "v_dot2_f32_bf16 v170, v163, v163, v170\n"
    "v_dot2_f32_bf16 v171, v163, v175, v171\n"
    "v_dot2_f32_bf16 v169, v164, s80, v169\n"
    "v_dot2_f32_bf16 v170, v164, v164, v170\n"
    "v_dot2_f32_bf16 v171, v164, v176, v171\n"
    "v_dot2_f32_bf16 v169, v165, s80, v169\n"
    "v_dot2_f32_bf16 v170, v165, v165, v170\n"
    "v_dot2_f32_bf16 v171, v165, v177, v171\n"
    "v_dot2_f32_bf16 v169, v166, s80, v169\n"
    "v_dot2_f32_bf16 v170, v166, v166, v170\n"
    "v_dot2_f32_bf16 v171, v166, v178, v171\n"
    "v_dot2_f32_bf16 v169, v167, s80, v169\n"
    "v_dot2_f32_bf16 v170, v167, v167, v170\n"
    "v_dot2_f32_bf16 v171, v167, v179, v171\n"
    "v_dot2_f32_bf16 v169, v168, s80, v169\n"
    "v_dot2_f32_bf16 v170, v168, v168, v170\n"
    "v_dot2_f32_bf16 v171, v168, v180, v171\n"
    "s_mul_i32 s66, s66, 1664525\n"
    "s_add_u32 s66, s66, 1013904223\n"
    "v_add_f32_dpp v169, v169, v169 quad_perm:[1,0,3,2] row_mask:0xf bank_mask:0xf\n"
    "v_add_f32_dpp v170, v170, v170 quad_perm:[1,0,3,2] row_mask:0xf bank_mask:0xf\n"
    "v_add_f32_dpp v171, v171, v171 quad_perm:[1,0,3,2] row_mask:0xf bank_mask:0xf\n"
    "v_add_f32_dpp v169, v169, v169 quad_perm:[2,3,0,1] row_mask:0xf bank_mask:0xf\n"
    "v_add_f32_dpp v170, v170, v170 quad_perm:[2,3,0,1] row_mask:0xf bank_mask:0xf\n"
    "v_add_f32_dpp v171, v171, v171 quad_perm:[2,3,0,1] row_mask:0xf bank_mask:0xf\n"
    "v_add_f32_dpp v169, v169, v169 row_half_mirror row_mask:0xf bank_mask:0xf\n"
    "v_add_f32_dpp v170, v170, v170 row_half_mirror row_mask:0xf bank_mask:0xf\n"
    "v_add_f32_dpp v171, v171, v171 row_half_mirror row_mask:0xf bank_mask:0xf\n"
    "v_add_f32_dpp v169, v169, v169 row_mirror row_mask:0xf bank_mask:0xf\n"
    "v_add_f32_dpp v170, v170, v170 row_mirror row_mask:0xf bank_mask:0xf\n"
    "v_add_f32_dpp v171, v171, v171 row_mirror row_mask:0xf bank_mask:0xf\n"
    "v_mov_b32 v172, v169\n"
    "v_mov_b32 v173, v170\n"
    "v_mov_b32 v174, v171\n"
    "v_permlane16_swap_b32 v169, v172\n"
    "v_permlane16_swap_b32 v170, v173\n"
    "v_permlane16_swap_b32 v171, v174\n"
    "v_add_f32 v169, v169, v172\n"
    "v_add_f32 v170, v170, v173\n"
    "v_add_f32 v162, v171, v174\n"
    "v_mul_f32 v169, 0xbae38e39, v169\n"
    "v_mul_f32 v170, 0x3ae38e39, v170\n"
    "v_fma_f32 v170, -v169, v169, v170\n"
    "v_max_f32 v170, 0, v170\n"
    "v_add_f32 v170, 0x3727c5ac, v170\n"
    "v_rsq_f32 v160, v170\n"
    "v_mov_b32 v163, s66\n"
    "v_mul_f32 v161, v169, v160\n"
    "buffer_store_dwordx4 v[160:163], v182, s[20:23], s86 offen sc1\n"
    "s_mul_i32 s82, s5, 786432\n"
    "s_add_u32 s74, s75, s82\n"
    "s_add_u32 s75, s75, 2359296\n"
    "s_lshl_b32 s77, s77, 10\n"
    "s_add_u32 s77, s77, 65536\n"
    "s_lshl_b32 s82, s5, 15\n"
    "s_add_u32 s76, s77, s82\n"
    "s_lshl_b32 s78, s7, 11\n"
    "s_and_b32 s79, s7, 1\n"
    "s_mul_i32 s79, s79, 9216\n"
    "s_add_u32 s79, s79, 8192\n"
    "s_lshl_b32 s37, s87, 15\n"
    "s_add_u32 s37, s37, 1179648\n"
    "v_mfma_f32_16x16x32_bf16 v[132:135], a[132:135], a[20:23], 0\n"
    "s_waitcnt vmcnt(12)\n"
    "ds_read_b64_tr_b16 a[152:153], v241 offset:2048\n"
    "v_add_u32 v225, v225, v224\n"
    "v_mfma_f32_16x16x32_bf16 v[132:135], a[128:131], a[4:7], v[132:135]\n"
    "s_lshr_b32 s81, %2, 3\n"
    "buffer_load_dwordx4 v248, s[8:11], s60 offen offset:1024 lds\n"
    "v_mfma_f32_16x16x32_bf16 v[136:139], a[128:131], a[8:11], 0\n"
    "s_lshl_b32 s81, s81, 2\n"
    "ds_read_b64_tr_b16 a[154:155], v241 offset:8192\n"
    "v_add_u32 v231, 122880, v225\n"
    "v_mfma_f32_16x16x32_bf16 v[136:139], a[132:135], a[24:27], v[136:139]\n"
    "s_add_u32 s81, s81, s7\n"
    "ds_read_b128 a[32:35], v245 offset:8192\n"
    "v_lshrrev_b32 v224, 5, v255\n"
    "v_mfma_f32_16x16x32_bf16 v[140:143], a[132:135], a[28:31], 0\n"
    "s_mul_i32 s82, s81, 7282\n"
    "ds_read_b64_tr_b16 a[156:157], v241 offset:14336\n"
    "v_and_b32 v225, 7, v255\n"
    "v_mfma_f32_16x16x32_bf16 v[140:143], a[128:131], a[12:15], v[140:143]\n"
    "s_waitcnt lgkmcnt(9)\n"
    "ds_read_b128 a[48:51], v246 offset:8192\n"
    "v_lshlrev_b32 v225, 5, v225\n"
    "v_mfma_f32_16x16x32_bf16 v[196:199], a[168:171], a[160:163], 0\n"
    "s_lshr_b32 s82, s82, 16\n"
    "ds_read_b64_tr_b16 a[158:159], v241 offset:20480\n"
    "v_lshl_add_u32 v224, v224, 8, v225\n"
    "v_mfma_f32_16x16x32_bf16 v[196:199], a[168:171], a[164:167], v[196:199]\n"
    "s_waitcnt lgkmcnt(10)\n"
    "buffer_load_dwordx4 v249, s[8:11], s60 offen offset:2048 lds\n"
    "v_mfma_f32_16x16x32_bf16 v[156:159], a[136:139], a[12:15], 0\n"
    "s_mul_i32 s83, s82, 9\n"
    "ds_read_b128 a[36:39], v245 offset:10240\n"
    "v_and_b32 v225, 16, v255\n"
    "v_mfma_f32_16x16x32_bf16 v[156:159], a[140:143], a[28:31], v[156:159]\n"
    "s_sub_u32 s81, s81, s83\n"
    "ds_read_b128 a[52:55], v246 offset:10240\n"
    "v_add_u32 v224, v224, v225\n"
    "v_mfma_f32_16x16x32_bf16 v[152:155], a[140:143], a[24:27], 0\n"
    "s_lshl_b32 s81, s81, 7\n"
    "ds_read_b128 a[40:43], v245 offset:12288\n"
    "v_lshl_add_u32 v224, s7, 10, v224\n"
    "v_mfma_f32_16x16x32_bf16 v[152:155], a[136:139], a[8:11], v[152:155]\n"
    "s_mul_i32 s83, s82, 73728\n"
    "buffer_load_dwordx4 v250, s[8:11], s60 offen offset:3072 lds\n"
    "v_mfma_f32_16x16x32_bf16 v[148:151], a[136:139], a[4:7], 0\n"
    "s_add_u32 m0, s61, 0\n"
    "ds_read_b128 a[56:59], v246 offset:12288\n"
    "v_add_u32 v232, 131072, v224\n"
    "v_mfma_f32_16x16x32_bf16 v[148:151], a[140:143], a[20:23], v[148:151]\n"
    "s_waitcnt vmcnt(9)\n"
    "ds_read_b128 a[44:47], v245 offset:14336\n"
    "v_lshlrev_b32 v200, 9, v255\n"
    "v_mfma_f32_16x16x32_bf16 v[144:147], a[140:143], a[16:19], 0\n"
    "s_waitcnt lgkmcnt(5)\n"
    "ds_read_b128 a[60:63], v246 offset:14336\n"
    "v_mfma_f32_16x16x32_bf16 v[144:147], a[136:139], a[0:3], v[144:147]\n"
    "v_mfma_f32_16x16x32_bf16 v[160:163], a[144:147], a[0:3], 0\n"
    "s_barrier\n"
    "ds_read_b64_tr_b16 a[64:65], v238 offset:24576\n"
    "v_mfma_f32_16x16x32_bf16 v[160:163], a[148:151], a[16:19], v[160:163]\n"
    "s_add_u32 s81, s81, s83\n"
    "buffer_load_dwordx4 v242, s[12:15], s58 offen lds\n"
    "v_mfma_f32_16x16x32_bf16 v[164:167], a[148:151], a[20:23], 0\n"
    "s_add_u32 m0, s61, 1024\n"
    "ds_read_b64_tr_b16 a[66:67], v238 offset:30720\n"
    "v_mfma_f32_16x16x32_bf16 v[164:167], a[144:147], a[4:7], v[164:167]\n"
    "s_mul_i32 s83, s4, 663552\n"
    "v_mfma_f32_16x16x32_bf16 v[168:171], a[144:147], a[8:11], 0\n"
    "s_add_u32 s50, s81, s83\n"
    "ds_read_b64_tr_b16 a[68:69], v238 offset:36864\n"
    "v_mfma_f32_16x16x32_bf16 v[168:171], a[148:151], a[24:27], v[168:171]\n"
    "s_add_u32 s81, s81, s36\n"
    "v_mfma_f32_16x16x32_bf16 v[172:175], a[148:151], a[28:31], 0\n"
    "s_sub_u32 s81, s81, 663552\n"
    "ds_read_b64_tr_b16 a[70:71], v238 offset:43008\n"
    "v_mfma_f32_16x16x32_bf16 v[172:175], a[144:147], a[12:15], v[172:175]\n"
    "buffer_load_dwordx4 v243, s[12:15], s58 offen lds\n"
    "v_mfma_f32_16x16x32_bf16 v[188:191], a[152:155], a[12:15], 0\n"
    "s_add_u32 m0, s61, 2048\n"
    "ds_read_b64_tr_b16 a[72:73], v239 offset:24576\n"
    "v_mfma_f32_16x16x32_bf16 v[188:191], a[156:159], a[28:31], v[188:191]\n"
    "s_cmp_lt_u32 s82, 9\n"
    "v_mfma_f32_16x16x32_bf16 v[184:187], a[156:159], a[24:27], 0\n"
    "s_cselect_b32 s50, s50, s81\n"
    "ds_read_b64_tr_b16 a[74:75], v239 offset:30720\n"
    "v_mfma_f32_16x16x32_bf16 v[184:187], a[152:155], a[8:11], v[184:187]\n"
    "s_cselect_b64 s[48:49], s[24:25], s[28:29]\n"
    "v_mfma_f32_16x16x32_bf16 v[180:183], a[152:155], a[4:7], 0\n"
    "s_cselect_b32 s51, s26, s30\n"
    "ds_read_b64_tr_b16 a[76:77], v239 offset:36864\n"
    "v_mfma_f32_16x16x32_bf16 v[180:183], a[156:159], a[20:23], v[180:183]\n"
    "s_cmp_lt_u32 s82, 11\n"
    "s_cselect_b32 s51, s51, 0\n"
    "buffer_load_dwordx4 v244, s[12:15], s58 offen lds\n"
    "v_mfma_f32_16x16x32_bf16 v[176:179], a[156:159], a[16:19], 0\n"
    "s_waitcnt lgkmcnt(3)\n"
    "ds_read_b64_tr_b16 a[78:79], v239 offset:43008\n"
    "v_mfma_f32_16x16x32_bf16 v[176:179], a[152:155], a[0:3], v[176:179]\n"
    "LP1A_%=:\n"
    "v_mfma_f32_16x16x32_bf16 v[0:3], a[64:67], a[32:35], v[0:3]\n"
    "s_add_u32 m0, s61, 12288\n"
    "ds_read_b64_tr_b16 a[80:81], v240 offset:24576\n"
    "v_mfma_f32_16x16x32_bf16 v[0:3], a[68:71], a[48:51], v[0:3]\n"
    "s_add_u32 s58, s58, 294912\n"
    "ds_read_b64_tr_b16 a[82:83], v240 offset:30720\n"
    "v_mfma_f32_16x16x32_bf16 v[4:7], a[68:71], a[52:55], v[4:7]\n"
    "ds_read_b64_tr_b16 a[84:85], v240 offset:36864\n"
    "v_mfma_f32_16x16x32_bf16 v[4:7], a[64:67], a[36:39], v[4:7]\n"
    "buffer_load_dwordx4 v242, s[12:15], s58 offen lds\n"
    "v_mfma_f32_16x16x32_bf16 v[8:11], a[64:67], a[40:43], v[8:11]\n"
    "s_add_u32 m0, s61, 13312\n"
    "ds_read_b64_tr_b16 a[86:87], v240 offset:43008\n"
    "v_mfma_f32_16x16x32_bf16 v[8:11], a[68:71], a[56:59], v[8:11]\n"
    "ds_read_b128 a[160:163], v237 offset:24576\n"
    "v_mfma_f32_16x16x32_bf16 v[12:15], a[68:71], a[60:63], v[12:15]\n"
    "ds_read_b64_tr_b16 a[88:89], v241 offset:24576\n"
    "v_mfma_f32_16x16x32_bf16 v[12:15], a[64:67], a[44:47], v[12:15]\n"
    "s_waitcnt lgkmcnt(6)\n"
    "ds_read_b128 a[164:167], v237 offset:36864\n"
    "v_mfma_f32_16x16x32_bf16 v[28:31], a[72:75], a[44:47], v[28:31]\n"
    "s_add_u32 s56, s56, 1\n"
    "ds_read_b64_tr_b16 a[90:91], v241 offset:30720\n"
    "v_mfma_f32_16x16x32_bf16 v[28:31], a[76:79], a[60:63], v[28:31]\n"
    "s_cmp_eq_u32 s56, 9\n"
    "v_mfma_f32_16x16x32_bf16 v[24:27], a[76:79], a[56:59], v[24:27]\n"
    "s_cselect_b32 s56, 0, s56\n"
    "ds_read_b64_tr_b16 a[92:93], v241 offset:36864\n"
    "v_mfma_f32_16x16x32_bf16 v[24:27], a[72:75], a[40:43], v[24:27]\n"
    "buffer_load_dwordx4 v243, s[12:15], s58 offen lds\n"
    "v_mfma_f32_16x16x32_bf16 v[20:23], a[72:75], a[36:39], v[20:23]\n"
    "s_add_u32 m0, s61, 14336\n"
    "ds_read_b64_tr_b16 a[94:95], v241 offset:43008\n"
    "v_mfma_f32_16x16x32_bf16 v[20:23], a[76:79], a[52:55], v[20:23]\n"
    "s_cmp_ge_u32 s57, 7\n"
    "v_mfma_f32_16x16x32_bf16 v[16:19], a[76:79], a[48:51], v[16:19]\n"
    "s_cselect_b32 s56, 7000, s56\n"
    "ds_read_b64_tr_b16 a[96:97], v238 offset:25600\n"
    "v_mfma_f32_16x16x32_bf16 v[16:19], a[72:75], a[32:35], v[16:19]\n"
    "s_waitcnt lgkmcnt(7)\n"
    "v_mfma_f32_16x16x32_bf16 v[32:35], a[80:83], a[32:35], v[32:35]\n"
    "s_lshl_b32 s60, s56, 7\n"
    "ds_read_b64_tr_b16 a[98:99], v238 offset:31744\n"
    "v_mfma_f32_16x16x32_bf16 v[32:35], a[84:87], a[48:51], v[32:35]\n"
    "s_cmp_eq_u32 s57, 7\n"
    "buffer_load_dwordx4 v244, s[12:15], s58 offen lds\n"
    "v_mfma_f32_16x16x32_bf16 v[36:39], a[84:87], a[52:55], v[36:39]\n"
    "s_cselect_b32 s60, s50, s60\n"
    "ds_read_b64_tr_b16 a[100:101], v238 offset:37888\n"
    "v_mfma_f32_16x16x32_bf16 v[36:39], a[80:83], a[36:39], v[36:39]\n"
    "s_cselect_b64 s[8:9], s[48:49], s[8:9]\n"
    "v_mfma_f32_16x16x32_bf16 v[40:43], a[80:83], a[40:43], v[40:43]\n"
    "s_cselect_b32 s10, s51, s10\n"
    "ds_read_b64_tr_b16 a[102:103], v238 offset:44032\n"
    "v_mfma_f32_16x16x32_bf16 v[40:43], a[84:87], a[56:59], v[40:43]\n"
    "s_add_u32 m0, s65, 8192\n"
    "v_mfma_f32_16x16x32_bf16 v[44:47], a[84:87], a[60:63], v[44:47]\n"
    "ds_read_b64_tr_b16 a[104:105], v239 offset:25600\n"
    "v_mfma_f32_16x16x32_bf16 v[44:47], a[80:83], a[44:47], v[44:47]\n"
    "s_waitcnt lgkmcnt(8)\n"
    "v_mfma_f32_16x16x32_bf16 v[192:195], a[168:171], a[160:163], v[192:195]\n"
    "ds_read_b64_tr_b16 a[106:107], v239 offset:31744\n"
    "v_mfma_f32_16x16x32_bf16 v[192:195], a[168:171], a[164:167], v[192:195]\n"
    "s_waitcnt lgkmcnt(6)\n"
    "buffer_load_dwordx4 v247, s[8:11], s60 offen offset:0 lds\n"
    "v_mfma_f32_16x16x32_bf16 v[60:63], a[88:91], a[44:47], v[60:63]\n"
    "s_mul_i32 s58, s56, 589824\n"
    "ds_read_b64_tr_b16 a[108:109], v239 offset:37888\n"
    "v_mfma_f32_16x16x32_bf16 v[60:63], a[92:95], a[60:63], v[60:63]\n"
    "s_add_u32 s57, s57, 1\n"
    "ds_read_b128 a[160:163], v237 offset:25600\n"
    "v_mfma_f32_16x16x32_bf16 v[56:59], a[92:95], a[56:59], v[56:59]\n"
    "ds_read_b64_tr_b16 a[110:111], v239 offset:44032\n"
    "v_mfma_f32_16x16x32_bf16 v[56:59], a[88:91], a[40:43], v[56:59]\n"
    "ds_read_b128 a[164:167], v237 offset:37888\n"
    "v_mfma_f32_16x16x32_bf16 v[52:55], a[88:91], a[36:39], v[52:55]\n"
    "ds_read_b64_tr_b16 a[112:113], v240 offset:25600\n"
    "v_mfma_f32_16x16x32_bf16 v[52:55], a[92:95], a[52:55], v[52:55]\n"
    "v_mfma_f32_16x16x32_bf16 v[48:51], a[92:95], a[48:51], v[48:51]\n"
    "ds_read_b64_tr_b16 a[114:115], v240 offset:31744\n"
    "v_mfma_f32_16x16x32_bf16 v[48:51], a[88:91], a[32:35], v[48:51]\n"
    "s_waitcnt lgkmcnt(8)\n"
    "buffer_load_dwordx4 v248, s[8:11], s60 offen offset:1024 lds\n"
    "v_mfma_f32_16x16x32_bf16 v[64:67], a[96:99], a[32:35], v[64:67]\n"
    "ds_read_b64_tr_b16 a[116:117], v240 offset:37888\n"
    "v_mfma_f32_16x16x32_bf16 v[64:67], a[100:103], a[48:51], v[64:67]\n"
    "v_mfma_f32_16x16x32_bf16 v[68:71], a[100:103], a[52:55], v[68:71]\n"
    "ds_read_b64_tr_b16 a[118:119], v240 offset:44032\n"
    "v_mfma_f32_16x16x32_bf16 v[68:71], a[96:99], a[36:39], v[68:71]\n"
    "v_mfma_f32_16x16x32_bf16 v[72:75], a[96:99], a[40:43], v[72:75]\n"
    "ds_read_b64_tr_b16 a[120:121], v241 offset:25600\n"
    "v_mfma_f32_16x16x32_bf16 v[72:75], a[100:103], a[56:59], v[72:75]\n"
    "v_mfma_f32_16x16x32_bf16 v[76:79], a[100:103], a[60:63], v[76:79]\n"
    "ds_read_b64_tr_b16 a[122:123], v241 offset:31744\n"
    "v_mfma_f32_16x16x32_bf16 v[76:79], a[96:99], a[44:47], v[76:79]\n"
    "s_waitcnt lgkmcnt(7)\n"
    "buffer_load_dwordx4 v249, s[8:11], s60 offen offset:2048 lds\n"
    "v_mfma_f32_16x16x32_bf16 v[92:95], a[104:107], a[44:47], v[92:95]\n"
    "ds_read_b64_tr_b16 a[124:125], v241 offset:37888\n"
    "v_mfma_f32_16x16x32_bf16 v[92:95], a[108:111], a[60:63], v[92:95]\n"
    "v_mfma_f32_16x16x32_bf16 v[88:91], a[108:111], a[56:59], v[88:91]\n"
    "ds_read_b64_tr_b16 a[126:127], v241 offset:44032\n"
    "v_mfma_f32_16x16x32_bf16 v[88:91], a[104:107], a[40:43], v[88:91]\n"
    "v_mfma_f32_16x16x32_bf16 v[84:87], a[104:107], a[36:39], v[84:87]\n"
    "ds_read_b64_tr_b16 a[128:129], v238 offset:26624\n"
    "v_mfma_f32_16x16x32_bf16 v[84:87], a[108:111], a[52:55], v[84:87]\n"
    "buffer_load_dwordx4 v250, s[8:11], s60 offen offset:3072 lds\n"
    "v_mfma_f32_16x16x32_bf16 v[80:83], a[108:111], a[48:51], v[80:83]\n"
    "s_add_u32 m0, s65, 12288\n"
    "ds_read_b64_tr_b16 a[130:131], v238 offset:32768\n"
    "v_mfma_f32_16x16x32_bf16 v[80:83], a[104:107], a[32:35], v[80:83]\n"
    "s_waitcnt lgkmcnt(10)\n"
    "v_mfma_f32_16x16x32_bf16 v[192:195], a[172:175], a[160:163], v[192:195]\n"
    "s_add_u32 s60, s60, 36864\n"
    "ds_read_b64_tr_b16 a[132:133], v238 offset:38912\n"
    "v_mfma_f32_16x16x32_bf16 v[192:195], a[172:175], a[164:167], v[192:195]\n"
    "s_waitcnt lgkmcnt(7)\n"
    "v_mfma_f32_16x16x32_bf16 v[96:99], a[112:115], a[32:35], v[96:99]\n"
    "ds_read_b64_tr_b16 a[134:135], v238 offset:45056\n"
    "v_mfma_f32_16x16x32_bf16 v[96:99], a[116:119], a[48:51], v[96:99]\n"
    "ds_read_b128 a[160:163], v237 offset:26624\n"
    "v_mfma_f32_16x16x32_bf16 v[100:103], a[116:119], a[52:55], v[100:103]\n"
    "ds_read_b64_tr_b16 a[136:137], v239 offset:26624\n"
    "v_mfma_f32_16x16x32_bf16 v[100:103], a[112:115], a[36:39], v[100:103]\n"
    "buffer_load_dwordx4 v247, s[8:11], s60 offen offset:0 lds\n"
    "v_mfma_f32_16x16x32_bf16 v[104:107], a[112:115], a[40:43], v[104:107]\n"
    "ds_read_b64_tr_b16 a[138:139], v239 offset:32768\n"
    "v_mfma_f32_16x16x32_bf16 v[104:107], a[116:119], a[56:59], v[104:107]\n"
    "ds_read_b128 a[164:167], v237 offset:38912\n"
    "v_mfma_f32_16x16x32_bf16 v[108:111], a[116:119], a[60:63], v[108:111]\n"
    "ds_read_b64_tr_b16 a[140:141], v239 offset:38912\n"
    "v_mfma_f32_16x16x32_bf16 v[108:111], a[112:115], a[44:47], v[108:111]\n"
    "s_waitcnt lgkmcnt(9)\n"
    "v_mfma_f32_16x16x32_bf16 v[124:127], a[120:123], a[44:47], v[124:127]\n"
    "ds_read_b64_tr_b16 a[142:143], v239 offset:45056\n"
    "v_mfma_f32_16x16x32_bf16 v[124:127], a[124:127], a[60:63], v[124:127]\n"
    "v_mfma_f32_16x16x32_bf16 v[120:123], a[124:127], a[56:59], v[120:123]\n"
    "ds_read_b64_tr_b16 a[144:145], v240 offset:26624\n"
    "v_mfma_f32_16x16x32_bf16 v[120:123], a[120:123], a[40:43], v[120:123]\n"
    "buffer_load_dwordx4 v248, s[8:11], s60 offen offset:1024 lds\n"
    "v_mfma_f32_16x16x32_bf16 v[116:119], a[120:123], a[36:39], v[116:119]\n"
    "ds_read_b64_tr_b16 a[146:147], v240 offset:32768\n"
    "v_mfma_f32_16x16x32_bf16 v[116:119], a[124:127], a[52:55], v[116:119]\n"
    "v_mfma_f32_16x16x32_bf16 v[112:115], a[124:127], a[48:51], v[112:115]\n"
    "ds_read_b64_tr_b16 a[148:149], v240 offset:38912\n"
    "v_mfma_f32_16x16x32_bf16 v[112:115], a[120:123], a[32:35], v[112:115]\n"
    "s_waitcnt lgkmcnt(9)\n"
    "v_mfma_f32_16x16x32_bf16 v[128:131], a[128:131], a[32:35], v[128:131]\n"
    "ds_read_b64_tr_b16 a[150:151], v240 offset:45056\n"
    "v_mfma_f32_16x16x32_bf16 v[128:131], a[132:135], a[48:51], v[128:131]\n"
    "buffer_load_dwordx4 v249, s[8:11], s60 offen offset:2048 lds\n"
    "v_mfma_f32_16x16x32_bf16 v[132:135], a[132:135], a[52:55], v[132:135]\n"
    "s_waitcnt vmcnt(13)\n"
    "ds_read_b64_tr_b16 a[152:153], v241 offset:26624\n"
    "v_mfma_f32_16x16x32_bf16 v[132:135], a[128:131], a[36:39], v[132:135]\n"
    "ds_read_b128 a[0:3], v245 offset:0\n"
    "v_mfma_f32_16x16x32_bf16 v[136:139], a[128:131], a[40:43], v[136:139]\n"
    "ds_read_b64_tr_b16 a[154:155], v241 offset:32768\n"
    "v_mfma_f32_16x16x32_bf16 v[136:139], a[132:135], a[56:59], v[136:139]\n"
    "ds_read_b128 a[16:19], v246 offset:0\n"
    "v_mfma_f32_16x16x32_bf16 v[140:143], a[132:135], a[60:63], v[140:143]\n"
    "ds_read_b64_tr_b16 a[156:157], v241 offset:38912\n"
    "v_mfma_f32_16x16x32_bf16 v[140:143], a[128:131], a[44:47], v[140:143]\n"
    "s_waitcnt lgkmcnt(11)\n"
    "ds_read_b128 a[4:7], v245 offset:2048\n"
    "v_mfma_f32_16x16x32_bf16 v[196:199], a[168:171], a[160:163], v[196:199]\n"
    "ds_read_b64_tr_b16 a[158:159], v241 offset:45056\n"
    "v_mfma_f32_16x16x32_bf16 v[196:199], a[168:171], a[164:167], v[196:199]\n"
    "s_waitcnt lgkmcnt(11)\n"
    "buffer_load_dwordx4 v250, s[8:11], s60 offen offset:3072 lds\n"
    "v_mfma_f32_16x16x32_bf16 v[156:159], a[136:139], a[44:47], v[156:159]\n"
    "s_add_u32 m0, s61, 24576\n"
    "ds_read_b128 a[20:23], v246 offset:2048\n"
    "v_mfma_f32_16x16x32_bf16 v[156:159], a[140:143], a[60:63], v[156:159]\n"
    "ds_read_b128 a[8:11], v245 offset:4096\n"
    "v_mfma_f32_16x16x32_bf16 v[152:155], a[140:143], a[56:59], v[152:155]\n"
    "ds_read_b128 a[24:27], v246 offset:4096\n"
    "v_mfma_f32_16x16x32_bf16 v[152:155], a[136:139], a[40:43], v[152:155]\n"
    "ds_read_b128 a[12:15], v245 offset:6144\n"
    "v_mfma_f32_16x16x32_bf16 v[148:151], a[136:139], a[36:39], v[148:151]\n"
    "ds_read_b128 a[28:31], v246 offset:6144\n"
    "v_mfma_f32_16x16x32_bf16 v[148:151], a[140:143], a[52:55], v[148:151]\n"
    "s_waitcnt vmcnt(8)\n"
    "v_mfma_f32_16x16x32_bf16 v[144:147], a[140:143], a[48:51], v[144:147]\n"
    "s_waitcnt lgkmcnt(5)\n"
    "v_mfma_f32_16x16x32_bf16 v[144:147], a[136:139], a[32:35], v[144:147]\n"
    "v_mfma_f32_16x16x32_bf16 v[160:163], a[144:147], a[32:35], v[160:163]\n"
    "s_barrier\n"
    "ds_read_b64_tr_b16 a[64:65], v238 offset:0\n"
    "v_mfma_f32_16x16x32_bf16 v[160:163], a[148:151], a[48:51], v[160:163]\n"
    "buffer_load_dwordx4 v242, s[12:15], s58 offen lds\n"
    "v_mfma_f32_16x16x32_bf16 v[164:167], a[148:151], a[52:55], v[164:167]\n"
    "s_add_u32 m0, s61, 25600\n"
    "ds_read_b64_tr_b16 a[66:67], v238 offset:6144\n"
    "v_mfma_f32_16x16x32_bf16 v[164:167], a[144:147], a[36:39], v[164:167]\n"
    "v_mfma_f32_16x16x32_bf16 v[168:171], a[144:147], a[40:43], v[168:171]\n"
    "ds_read_b64_tr_b16 a[68:69], v238 offset:12288\n"
    "v_mfma_f32_16x16x32_bf16 v[168:171], a[148:151], a[56:59], v[168:171]\n"
    "v_mfma_f32_16x16x32_bf16 v[172:175], a[148:151], a[60:63], v[172:175]\n"
    "ds_read_b64_tr_b16 a[70:71], v238 offset:18432\n"
    "v_mfma_f32_16x16x32_bf16 v[172:175], a[144:147], a[44:47], v[172:175]\n"
    "buffer_load_dwordx4 v243, s[12:15], s58 offen lds\n"
    "v_mfma_f32_16x16x32_bf16 v[188:191], a[152:155], a[44:47], v[188:191]\n"
    "s_add_u32 m0, s61, 26624\n"
    "ds_read_b64_tr_b16 a[72:73], v239 offset:0\n"
    "v_mfma_f32_16x16x32_bf16 v[188:191], a[156:159], a[60:63], v[188:191]\n"
    "v_mfma_f32_16x16x32_bf16 v[184:187], a[156:159], a[56:59], v[184:187]\n"
    "ds_read_b64_tr_b16 a[74:75], v239 offset:6144\n"
    "v_mfma_f32_16x16x32_bf16 v[184:187], a[152:155], a[40:43], v[184:187]\n"
    "v_mfma_f32_16x16x32_bf16 v[180:183], a[152:155], a[36:39], v[180:183]\n"
    "ds_read_b64_tr_b16 a[76:77], v239 offset:12288\n"
    "v_mfma_f32_16x16x32_bf16 v[180:183], a[156:159], a[52:55], v[180:183]\n"
    "buffer_load_dwordx4 v244, s[12:15], s58 offen lds\n"
    "v_mfma_f32_16x16x32_bf16 v[176:179], a[156:159], a[48:51], v[176:179]\n"
    "s_waitcnt lgkmcnt(3)\n"
    "ds_read_b64_tr_b16 a[78:79], v239 offset:18432\n"
    "v_mfma_f32_16x16x32_bf16 v[176:179], a[152:155], a[32:35], v[176:179]\n"
    "v_mfma_f32_16x16x32_bf16 v[0:3], a[64:67], a[0:3], v[0:3]\n"
    "s_add_u32 m0, s61, 36864\n"
    "ds_read_b64_tr_b16 a[80:81], v240 offset:0\n"
    "v_mfma_f32_16x16x32_bf16 v[0:3], a[68:71], a[16:19], v[0:3]\n"
    "s_add_u32 s58, s58, 294912\n"
    "ds_read_b64_tr_b16 a[82:83], v240 offset:6144\n"
    "v_mfma_f32_16x16x32_bf16 v[4:7], a[68:71], a[20:23], v[4:7]\n"
    "ds_read_b64_tr_b16 a[84:85], v240 offset:12288\n"
    "v_mfma_f32_16x16x32_bf16 v[4:7], a[64:67], a[4:7], v[4:7]\n"
    "buffer_load_dwordx4 v242, s[12:15], s58 offen lds\n"
    "v_mfma_f32_16x16x32_bf16 v[8:11], a[64:67], a[8:11], v[8:11]\n"
    "s_add_u32 m0, s61, 37888\n"
    "ds_read_b64_tr_b16 a[86:87], v240 offset:18432\n"
    "v_mfma_f32_16x16x32_bf16 v[8:11], a[68:71], a[24:27], v[8:11]\n"
    "ds_read_b128 a[160:163], v237 offset:0\n"
    "v_mfma_f32_16x16x32_bf16 v[12:15], a[68:71], a[28:31], v[12:15]\n"
    "ds_read_b64_tr_b16 a[88:89], v241 offset:0\n"
    "v_mfma_f32_16x16x32_bf16 v[12:15], a[64:67], a[12:15], v[12:15]\n"
    "s_waitcnt lgkmcnt(6)\n"
    "ds_read_b128 a[164:167], v237 offset:12288\n"
    "v_mfma_f32_16x16x32_bf16 v[28:31], a[72:75], a[12:15], v[28:31]\n"
    "s_add_u32 s56, s56, 1\n"
    "ds_read_b64_tr_b16 a[90:91], v241 offset:6144\n"
    "v_mfma_f32_16x16x32_bf16 v[28:31], a[76:79], a[28:31], v[28:31]\n"
    "s_cmp_eq_u32 s56, 9\n"
    "v_mfma_f32_16x16x32_bf16 v[24:27], a[76:79], a[24:27], v[24:27]\n"
    "s_cselect_b32 s56, 0, s56\n"
    "ds_read_b64_tr_b16 a[92:93], v241 offset:12288\n"
    "v_mfma_f32_16x16x32_bf16 v[24:27], a[72:75], a[8:11], v[24:27]\n"
    "buffer_load_dwordx4 v243, s[12:15], s58 offen lds\n"
    "v_mfma_f32_16x16x32_bf16 v[20:23], a[72:75], a[4:7], v[20:23]\n"
    "s_add_u32 m0, s61, 38912\n"
    "ds_read_b64_tr_b16 a[94:95], v241 offset:18432\n"
    "v_mfma_f32_16x16x32_bf16 v[20:23], a[76:79], a[20:23], v[20:23]\n"
    "s_cmp_ge_u32 s57, 7\n"
    "v_mfma_f32_16x16x32_bf16 v[16:19], a[76:79], a[16:19], v[16:19]\n"
    "s_cselect_b32 s56, 7000, s56\n"
    "ds_read_b64_tr_b16 a[96:97], v238 offset:1024\n"
    "v_mfma_f32_16x16x32_bf16 v[16:19], a[72:75], a[0:3], v[16:19]\n"
    "s_waitcnt lgkmcnt(7)\n"
    "v_mfma_f32_16x16x32_bf16 v[32:35], a[80:83], a[0:3], v[32:35]\n"
    "s_lshl_b32 s60, s56, 7\n"
    "ds_read_b64_tr_b16 a[98:99], v238 offset:7168\n"
    "v_mfma_f32_16x16x32_bf16 v[32:35], a[84:87], a[16:19], v[32:35]\n"
    "s_cmp_eq_u32 s57, 8\n"
    "buffer_load_dwordx4 v244, s[12:15], s58 offen lds\n"
    "v_mfma_f32_16x16x32_bf16 v[36:39], a[84:87], a[20:23], v[36:39]\n"
    "s_cselect_b32 s10, 0, s10\n"
    "ds_read_b64_tr_b16 a[100:101], v238 offset:13312\n"
    "v_mfma_f32_16x16x32_bf16 v[36:39], a[80:83], a[4:7], v[36:39]\n"
    "s_add_u32 m0, s65, 0\n"
    "v_mfma_f32_16x16x32_bf16 v[40:43], a[80:83], a[8:11], v[40:43]\n"
    "ds_read_b64_tr_b16 a[102:103], v238 offset:19456\n"
    "v_mfma_f32_16x16x32_bf16 v[40:43], a[84:87], a[24:27], v[40:43]\n"
    "v_mfma_f32_16x16x32_bf16 v[44:47], a[84:87], a[28:31], v[44:47]\n"
    "ds_read_b64_tr_b16 a[104:105], v239 offset:1024\n"
    "v_mfma_f32_16x16x32_bf16 v[44:47], a[80:83], a[12:15], v[44:47]\n"
    "s_waitcnt lgkmcnt(8)\n"
    "v_mfma_f32_16x16x32_bf16 v[192:195], a[168:171], a[160:163], v[192:195]\n"
    "ds_read_b64_tr_b16 a[106:107], v239 offset:7168\n"
    "v_mfma_f32_16x16x32_bf16 v[192:195], a[168:171], a[164:167], v[192:195]\n"
    "s_waitcnt lgkmcnt(6)\n"
    "buffer_load_dwordx4 v247, s[8:11], s60 offen offset:0 lds\n"
    "v_mfma_f32_16x16x32_bf16 v[60:63], a[88:91], a[12:15], v[60:63]\n"
    "s_mul_i32 s58, s56, 589824\n"
    "ds_read_b64_tr_b16 a[108:109], v239 offset:13312\n"
    "v_mfma_f32_16x16x32_bf16 v[60:63], a[92:95], a[28:31], v[60:63]\n"
    "s_add_u32 s57, s57, 1\n"
    "ds_read_b128 a[160:163], v237 offset:1024\n"
    "v_mfma_f32_16x16x32_bf16 v[56:59], a[92:95], a[24:27], v[56:59]\n"
    "ds_read_b64_tr_b16 a[110:111], v239 offset:19456\n"
    "v_mfma_f32_16x16x32_bf16 v[56:59], a[88:91], a[8:11], v[56:59]\n"
    "ds_read_b128 a[164:167], v237 offset:13312\n"
    "v_mfma_f32_16x16x32_bf16 v[52:55], a[88:91], a[4:7], v[52:55]\n"
    "ds_read_b64_tr_b16 a[112:113], v240 offset:1024\n"
    "v_mfma_f32_16x16x32_bf16 v[52:55], a[92:95], a[20:23], v[52:55]\n"
    "v_mfma_f32_16x16x32_bf16 v[48:51], a[92:95], a[16:19], v[48:51]\n"
    "ds_read_b64_tr_b16 a[114:115], v240 offset:7168\n"
    "v_mfma_f32_16x16x32_bf16 v[48:51], a[88:91], a[0:3], v[48:51]\n"
    "s_waitcnt lgkmcnt(8)\n"
    "buffer_load_dwordx4 v248, s[8:11], s60 offen offset:1024 lds\n"
    "v_mfma_f32_16x16x32_bf16 v[64:67], a[96:99], a[0:3], v[64:67]\n"
    "ds_read_b64_tr_b16 a[116:117], v240 offset:13312\n"
    "v_mfma_f32_16x16x32_bf16 v[64:67], a[100:103], a[16:19], v[64:67]\n"
    "v_mfma_f32_16x16x32_bf16 v[68:71], a[100:103], a[20:23], v[68:71]\n"
    "ds_read_b64_tr_b16 a[118:119], v240 offset:19456\n"
    "v_mfma_f32_16x16x32_bf16 v[68:71], a[96:99], a[4:7], v[68:71]\n"
    "v_mfma_f32_16x16x32_bf16 v[72:75], a[96:99], a[8:11], v[72:75]\n"
    "ds_read_b64_tr_b16 a[120:121], v241 offset:1024\n"
    "v_mfma_f32_16x16x32_bf16 v[72:75], a[100:103], a[24:27], v[72:75]\n"
    "v_mfma_f32_16x16x32_bf16 v[76:79], a[100:103], a[28:31], v[76:79]\n"
    "ds_read_b64_tr_b16 a[122:123], v241 offset:7168\n"
    "v_mfma_f32_16x16x32_bf16 v[76:79], a[96:99], a[12:15], v[76:79]\n"
    "s_waitcnt lgkmcnt(7)\n"
    "buffer_load_dwordx4 v249, s[8:11], s60 offen offset:2048 lds\n"
    "v_mfma_f32_16x16x32_bf16 v[92:95], a[104:107], a[12:15], v[92:95]\n"
    "ds_read_b64_tr_b16 a[124:125], v241 offset:13312\n"
    "v_mfma_f32_16x16x32_bf16 v[92:95], a[108:111], a[28:31], v[92:95]\n"
    "v_mfma_f32_16x16x32_bf16 v[88:91], a[108:111], a[24:27], v[88:91]\n"
    "ds_read_b64_tr_b16 a[126:127], v241 offset:19456\n"
    "v_mfma_f32_16x16x32_bf16 v[88:91], a[104:107], a[8:11], v[88:91]\n"
    "v_mfma_f32_16x16x32_bf16 v[84:87], a[104:107], a[4:7], v[84:87]\n"
    "ds_read_b64_tr_b16 a[128:129], v238 offset:2048\n"
    "v_mfma_f32_16x16x32_bf16 v[84:87], a[108:111], a[20:23], v[84:87]\n"
    "buffer_load_dwordx4 v250, s[8:11], s60 offen offset:3072 lds\n"
    "v_mfma_f32_16x16x32_bf16 v[80:83], a[108:111], a[16:19], v[80:83]\n"
    "s_add_u32 m0, s65, 4096\n"
    "ds_read_b64_tr_b16 a[130:131], v238 offset:8192\n"
    "v_mfma_f32_16x16x32_bf16 v[80:83], a[104:107], a[0:3], v[80:83]\n"
    "s_waitcnt lgkmcnt(10)\n"
    "v_mfma_f32_16x16x32_bf16 v[192:195], a[172:175], a[160:163], v[192:195]\n"
    "s_add_u32 s60, s60, 36864\n"
    "ds_read_b64_tr_b16 a[132:133], v238 offset:14336\n"
    "v_mfma_f32_16x16x32_bf16 v[192:195], a[172:175], a[164:167], v[192:195]\n"
    "s_waitcnt lgkmcnt(7)\n"
    "v_mfma_f32_16x16x32_bf16 v[96:99], a[112:115], a[0:3], v[96:99]\n"
    "ds_read_b64_tr_b16 a[134:135], v238 offset:20480\n"
    "v_mfma_f32_16x16x32_bf16 v[96:99], a[116:119], a[16:19], v[96:99]\n"
    "ds_read_b128 a[160:163], v237 offset:2048\n"
    "v_mfma_f32_16x16x32_bf16 v[100:103], a[116:119], a[20:23], v[100:103]\n"
    "ds_read_b64_tr_b16 a[136:137], v239 offset:2048\n"
    "v_mfma_f32_16x16x32_bf16 v[100:103], a[112:115], a[4:7], v[100:103]\n"
    "buffer_load_dwordx4 v247, s[8:11], s60 offen offset:0 lds\n"
    "v_mfma_f32_16x16x32_bf16 v[104:107], a[112:115], a[8:11], v[104:107]\n"
    "ds_read_b64_tr_b16 a[138:139], v239 offset:8192\n"
    "v_mfma_f32_16x16x32_bf16 v[104:107], a[116:119], a[24:27], v[104:107]\n"
    "ds_read_b128 a[164:167], v237 offset:14336\n"
    "v_mfma_f32_16x16x32_bf16 v[108:111], a[116:119], a[28:31], v[108:111]\n"
    "ds_read_b64_tr_b16 a[140:141], v239 offset:14336\n"
    "v_mfma_f32_16x16x32_bf16 v[108:111], a[112:115], a[12:15], v[108:111]\n"
    "s_waitcnt lgkmcnt(9)\n"
    "v_mfma_f32_16x16x32_bf16 v[124:127], a[120:123], a[12:15], v[124:127]\n"
    "ds_read_b64_tr_b16 a[142:143], v239 offset:20480\n"
    "v_mfma_f32_16x16x32_bf16 v[124:127], a[124:127], a[28:31], v[124:127]\n"
    "v_mfma_f32_16x16x32_bf16 v[120:123], a[124:127], a[24:27], v[120:123]\n"
    "ds_read_b64_tr_b16 a[144:145], v240 offset:2048\n"
    "v_mfma_f32_16x16x32_bf16 v[120:123], a[120:123], a[8:11], v[120:123]\n"
    "buffer_load_dwordx4 v248, s[8:11], s60 offen offset:1024 lds\n"
    "v_mfma_f32_16x16x32_bf16 v[116:119], a[120:123], a[4:7], v[116:119]\n"
    "ds_read_b64_tr_b16 a[146:147], v240 offset:8192\n"
    "v_mfma_f32_16x16x32_bf16 v[116:119], a[124:127], a[20:23], v[116:119]\n"
    "v_mfma_f32_16x16x32_bf16 v[112:115], a[124:127], a[16:19], v[112:115]\n"
    "ds_read_b64_tr_b16 a[148:149], v240 offset:14336\n"
    "v_mfma_f32_16x16x32_bf16 v[112:115], a[120:123], a[0:3], v[112:115]\n"
    "s_waitcnt lgkmcnt(9)\n"
    "v_mfma_f32_16x16x32_bf16 v[128:131], a[128:131], a[0:3], v[128:131]\n"
    "ds_read_b64_tr_b16 a[150:151], v240 offset:20480\n"
    "v_mfma_f32_16x16x32_bf16 v[128:131], a[132:135], a[16:19], v[128:131]\n"
    "buffer_load_dwordx4 v249, s[8:11], s60 offen offset:2048 lds\n"
    "v_mfma_f32_16x16x32_bf16 v[132:135], a[132:135], a[20:23], v[132:135]\n"
    "s_waitcnt vmcnt(13)\n"
    "ds_read_b64_tr_b16 a[152:153], v241 offset:2048\n"
    "v_mfma_f32_16x16x32_bf16 v[132:135], a[128:131], a[4:7], v[132:135]\n"
    "ds_read_b128 a[32:35], v245 offset:8192\n"
    "v_mfma_f32_16x16x32_bf16 v[136:139], a[128:131], a[8:11], v[136:139]\n"
    "ds_read_b64_tr_b16 a[154:155], v241 offset:8192\n"
    "v_mfma_f32_16x16x32_bf16 v[136:139], a[132:135], a[24:27], v[136:139]\n"
    "ds_read_b128 a[48:51], v246 offset:8192\n"
    "v_mfma_f32_16x16x32_bf16 v[140:143], a[132:135], a[28:31], v[140:143]\n"
    "ds_read_b64_tr_b16 a[156:157], v241 offset:14336\n"
    "v_mfma_f32_16x16x32_bf16 v[140:143], a[128:131], a[12:15], v[140:143]\n"
    "s_waitcnt lgkmcnt(11)\n"
    "ds_read_b128 a[36:39], v245 offset:10240\n"
    "v_mfma_f32_16x16x32_bf16 v[196:199], a[168:171], a[160:163], v[196:199]\n"
    "ds_read_b64_tr_b16 a[158:159], v241 offset:20480\n"
    "v_mfma_f32_16x16x32_bf16 v[196:199], a[168:171], a[164:167], v[196:199]\n"
    "s_waitcnt lgkmcnt(11)\n"
    "buffer_load_dwordx4 v250, s[8:11], s60 offen offset:3072 lds\n"
    "v_mfma_f32_16x16x32_bf16 v[156:159], a[136:139], a[12:15], v[156:159]\n"
    "s_add_u32 m0, s61, 0\n"
    "ds_read_b128 a[52:55], v246 offset:10240\n"
    "v_mfma_f32_16x16x32_bf16 v[156:159], a[140:143], a[28:31], v[156:159]\n"
    "ds_read_b128 a[40:43], v245 offset:12288\n"
    "v_mfma_f32_16x16x32_bf16 v[152:155], a[140:143], a[24:27], v[152:155]\n"
    "ds_read_b128 a[56:59], v246 offset:12288\n"
    "v_mfma_f32_16x16x32_bf16 v[152:155], a[136:139], a[8:11], v[152:155]\n"
    "ds_read_b128 a[44:47], v245 offset:14336\n"
    "v_mfma_f32_16x16x32_bf16 v[148:151], a[136:139], a[4:7], v[148:151]\n"
    "ds_read_b128 a[60:63], v246 offset:14336\n"
    "v_mfma_f32_16x16x32_bf16 v[148:151], a[140:143], a[20:23], v[148:151]\n"
    "s_waitcnt vmcnt(8)\n"
    "v_mfma_f32_16x16x32_bf16 v[144:147], a[140:143], a[16:19], v[144:147]\n"
    "s_waitcnt lgkmcnt(5)\n"
    "v_mfma_f32_16x16x32_bf16 v[144:147], a[136:139], a[0:3], v[144:147]\n"
    "v_mfma_f32_16x16x32_bf16 v[160:163], a[144:147], a[0:3], v[160:163]\n"
    "s_barrier\n"
    "ds_read_b64_tr_b16 a[64:65], v238 offset:24576\n"
    "v_mfma_f32_16x16x32_bf16 v[160:163], a[148:151], a[16:19], v[160:163]\n"
    "buffer_load_dwordx4 v242, s[12:15], s58 offen lds\n"
    "v_mfma_f32_16x16x32_bf16 v[164:167], a[148:151], a[20:23], v[164:167]\n"
    "s_add_u32 m0, s61, 1024\n"
    "ds_read_b64_tr_b16 a[66:67], v238 offset:30720\n"
    "v_mfma_f32_16x16x32_bf16 v[164:167], a[144:147], a[4:7], v[164:167]\n"
    "v_mfma_f32_16x16x32_bf16 v[168:171], a[144:147], a[8:11], v[168:171]\n"
    "ds_read_b64_tr_b16 a[68:69], v238 offset:36864\n"
    "v_mfma_f32_16x16x32_bf16 v[168:171], a[148:151], a[24:27], v[168:171]\n"
    "v_mfma_f32_16x16x32_bf16 v[172:175], a[148:151], a[28:31], v[172:175]\n"
    "ds_read_b64_tr_b16 a[70:71], v238 offset:43008\n"
    "v_mfma_f32_16x16x32_bf16 v[172:175], a[144:147], a[12:15], v[172:175]\n"
    "buffer_load_dwordx4 v243, s[12:15], s58 offen lds\n"
    "v_mfma_f32_16x16x32_bf16 v[188:191], a[152:155], a[12:15], v[188:191]\n"
    "s_add_u32 m0, s61, 2048\n"
    "ds_read_b64_tr_b16 a[72:73], v239 offset:24576\n"
    "v_mfma_f32_16x16x32_bf16 v[188:191], a[156:159], a[28:31], v[188:191]\n"
    "v_mfma_f32_16x16x32_bf16 v[184:187], a[156:159], a[24:27], v[184:187]\n"
    "ds_read_b64_tr_b16 a[74:75], v239 offset:30720\n"
    "v_mfma_f32_16x16x32_bf16 v[184:187], a[152:155], a[8:11], v[184:187]\n"
    "v_mfma_f32_16x16x32_bf16 v[180:183], a[152:155], a[4:7], v[180:183]\n"
    "ds_read_b64_tr_b16 a[76:77], v239 offset:36864\n"
    "v_mfma_f32_16x16x32_bf16 v[180:183], a[156:159], a[20:23], v[180:183]\n"
    "s_cmp_lt_u32 s57, 9\n"
    "buffer_load_dwordx4 v244, s[12:15], s58 offen lds\n"
    "v_mfma_f32_16x16x32_bf16 v[176:179], a[156:159], a[16:19], v[176:179]\n"
    "s_waitcnt lgkmcnt(3)\n"
    "ds_read_b64_tr_b16 a[78:79], v239 offset:43008\n"
    "v_mfma_f32_16x16x32_bf16 v[176:179], a[152:155], a[0:3], v[176:179]\n"
    "s_cbranch_scc1 LP1A_%=\n"
    "buffer_load_dwordx4 v[204:207], v200, s[20:23], s37 offen sc1\n"
    "s_mov_b32 s8, 0xbdd2d3e8\n"
    "s_mov_b32 s9, 0xc0135761\n"
    "v_add_f32_dpp v192, v192, v192 row_ror:8 row_mask:0xf bank_mask:0xf\n"
    "v_add_f32_dpp v193, v193, v193 row_ror:8 row_mask:0xf bank_mask:0xf\n"
    "v_add_f32_dpp v194, v194, v194 row_ror:8 row_mask:0xf bank_mask:0xf\n"
    "v_add_f32_dpp v195, v195, v195 row_ror:8 row_mask:0xf bank_mask:0xf\n"
    "v_add_f32_dpp v196, v196, v196 row_ror:8 row_mask:0xf bank_mask:0xf\n"
    "v_add_f32_dpp v197, v197, v197 row_ror:8 row_mask:0xf bank_mask:0xf\n"
    "v_add_f32_dpp v198, v198, v198 row_ror:8 row_mask:0xf bank_mask:0xf\n"
    "v_add_f32_dpp v199, v199, v199 row_ror:8 row_mask:0xf bank_mask:0xf\n"
    "ds_write_b128 v232, v[192:195]\n"
    "ds_write_b128 v232, v[196:199] offset:512\n"
    "v_lshlrev_b32 v236, 16, v234\n"
    "v_and_b32 v237, 0xffff0000, v234\n"
    "v_lshlrev_b32 v238, 16, v235\n"
    "v_and_b32 v239, 0xffff0000, v235\n"
    "s_mov_b32 exec_hi, 0xffff\n"
    "ds_write_b128 v229, v[236:239] offset:34560\n"
    "s_mov_b32 exec_hi, -1\n"
    "s_waitcnt vmcnt(1)\n"
    "s_waitcnt lgkmcnt(0)\n"
    "s_barrier\n"
    "ds_read_b128 v[208:211], v228 offset:8192\n"
    "ds_read_b128 v[212:215], v228 offset:9216\n"
    "ds_read_b128 v[216:219], v228 offset:10240\n"
    "ds_read_b128 v[220:223], v228 offset:11264\n"
    "s_add_u32 m0, s78, 9216\n"
    "s_add_u32 s56, s68, 73728\n"
    "buffer_load_dwordx4 v251, s[24:27], s56 offen offset:0 lds\n"
    "buffer_load_dwordx4 v252, s[24:27], s56 offen offset:1024 lds\n"
    "s_add_u32 m0, s79, 9216\n"
    "s_nop 0\n"
    "buffer_load_dwordx4 v253, s[24:27], s56 offen offset:0 lds\n"
    "v_mov_b32 v250, s66\n"
    "v_lshlrev_b32 v249, 2, v255\n"
    "s_waitcnt lgkmcnt(2)\n"
    "v_pk_add_f32 v[236:237], v[208:209], v[212:213]\n"
    "v_pk_add_f32 v[238:239], v[210:211], v[214:215]\n"
    "s_waitcnt lgkmcnt(0)\n"
    "v_pk_add_f32 v[240:241], v[216:217], v[220:221]\n"
    "v_pk_add_f32 v[242:243], v[218:219], v[222:223]\n"
    "v_pk_add_f32 v[236:237], v[236:237], v[240:241]\n"
    "v_pk_add_f32 v[238:239], v[238:239], v[242:243]\n"
    "s_mov_b32 exec_hi, 0xffff\n"
    "ds_write_b128 v229, v[236:239] offset:33792\n"
    "s_mov_b32 exec_hi, -1\n"
    "s_waitcnt vmcnt(3)\n"
    "v_cmp_ne_u32 vcc, s66, v207\n"
    "s_nop 1\n"
    "s_cbranch_vccnz RT1_%=\n"
    "RB1_%=:\n"
    "v_mul_f32 v206, 0x3fb8aa3b, v206\n"
    "v_exp_f32 v207, -v206\n"
    "v_exp_f32 v206, v206\n"
    "v_add_f32 v207, 1.0, v207\n"
    "v_add_f32 v206, 1.0, v206\n"
    "v_rcp_f32 v207, v207\n"
    "v_min_f32 v206, 0x7f000000, v206\n"
    "ds_write_b128 v229, v[204:207] offset:0\n"
    "ds_read_b128 v[208:211], v231 offset:0\n"
    "ds_read_b128 v[212:215], v231 offset:256\n"
    "ds_read_b128 v[216:219], v231 offset:512\n"
    "ds_read_b128 v[220:223], v231 offset:768\n"
    "ds_read_b128 v[192:195], v230 offset:640\n"
    "ds_read_b128 v[196:199], v230 offset:1408\n"
    "ds_read_b128 v[200:203], v230 offset:704\n"
    "ds_read_b128 v[204:207], v230 offset:1472\n"
    "s_waitcnt lgkmcnt(2)\n"
    "v_pk_fma_f32 v[236:237], v[192:193], v[208:209], v[196:197] op_sel:[0,1,0] op_sel_hi:[1,1,1]\n"
    "v_pk_fma_f32 v[238:239], v[194:195], v[208:209], v[198:199] op_sel:[0,1,0] op_sel_hi:[1,1,1]\n"
    "s_add_u32 s81, s74, 4096\n"
    "s_waitcnt lgkmcnt(0)\n"
    "v_pk_fma_f32 v[240:241], v[200:201], v[208:209], v[204:205] op_sel:[0,1,0] op_sel_hi:[1,1,1]\n"
    "v_pk_fma_f32 v[242:243], v[202:203], v[208:209], v[206:207] op_sel:[0,1,0] op_sel_hi:[1,1,1]\n"
    "v_pk_fma_f32 v[160:161], v[160:161], v[208:209], v[236:237] op_sel_hi:[1,0,1]\n"
    "v_pk_fma_f32 v[162:163], v[162:163], v[208:209], v[238:239] op_sel_hi:[1,0,1]\n"
    "v_pk_fma_f32 v[176:177], v[176:177], v[208:209], v[240:241] op_sel_hi:[1,0,1]\n"
    "v_pk_fma_f32 v[178:179], v[178:179], v[208:209], v[242:243] op_sel_hi:[1,0,1]\n"
    "v_pk_mul_f32 v[236:237], v[160:161], v[160:161]\n"
    "v_pk_mul_f32 v[238:239], v[162:163], v[162:163]\n"
    "v_pk_mul_f32 v[240:241], v[176:177], v[176:177]\n"
    "v_pk_mul_f32 v[242:243], v[178:179], v[178:179]\n"
    "v_pk_fma_f32 v[236:237], v[236:237], s[8:9], s[8:9] op_sel:[0,0,1] op_sel_hi:[1,0,1]\n"
    "v_pk_fma_f32 v[238:239], v[238:239], s[8:9], s[8:9] op_sel:[0,0,1] op_sel_hi:[1,0,1]\n"
    "v_pk_fma_f32 v[240:241], v[240:241], s[8:9], s[8:9] op_sel:[0,0,1] op_sel_hi:[1,0,1]\n"
    "v_pk_fma_f32 v[242:243], v[242:243], s[8:9], s[8:9] op_sel:[0,0,1] op_sel_hi:[1,0,1]\n"
    "s_add_u32 m0, s78, 18432\n"
    "s_add_u32 s56, s56, 36864\n"
    "buffer_load_dwordx4 v251, s[24:27], s56 offen offset:0 lds\n"
    "v_pk_mul_f32 v[236:237], v[160:161], v[236:237]\n"
    "v_pk_mul_f32 v[238:239], v[162:163], v[238:239]\n"
    "v_pk_mul_f32 v[240:241], v[176:177], v[240:241]\n"
    "v_pk_mul_f32 v[242:243], v[178:179], v[242:243]\n"
    "v_exp_f32 v236, v236\n"
    "v_exp_f32 v238, v238\n"
    "v_exp_f32 v240, v240\n"
    "v_exp_f32 v242, v242\n"
    "v_exp_f32 v237, v237\n"
    "v_exp_f32 v239, v239\n"
    "v_exp_f32 v241, v241\n"
    "v_exp_f32 v243, v243\n"
    "v_pk_fma_f32 v[236:237], v[236:237], v[210:211], v[210:211] op_sel_hi:[1,0,0]\n"
    "v_pk_fma_f32 v[238:239], v[238:239], v[210:211], v[210:211] op_sel_hi:[1,0,0]\n"
    "buffer_load_dwordx4 v252, s[24:27], s56 offen offset:1024 lds\n"
    "v_pk_fma_f32 v[240:241], v[240:241], v[210:211], v[210:211] op_sel_hi:[1,0,0]\n"
    "v_pk_fma_f32 v[242:243], v[242:243], v[210:211], v[210:211] op_sel_hi:[1,0,0]\n"
    "v_rcp_f32 v236, v236\n"
    "v_rcp_f32 v238, v238\n"
    "v_rcp_f32 v240, v240\n"
    "v_rcp_f32 v242, v242\n"
    "v_rcp_f32 v237, v237\n"
    "v_rcp_f32 v239, v239\n"
    "v_rcp_f32 v241, v241\n"
    "v_rcp_f32 v243, v243\n"
    "v_pk_mul_f32 v[160:161], v[160:161], v[236:237]\n"
    "v_pk_mul_f32 v[162:163], v[162:163], v[238:239]\n"
    "v_pk_mul_f32 v[176:177], v[176:177], v[240:241]\n"
    "v_pk_mul_f32 v[178:179], v[178:179], v[242:243]\n"
    "v_cvt_pk_bf16_f32 v160, v160, v161\n"
    "v_cvt_pk_bf16_f32 v161, v162, v163\n"
    "v_cvt_pk_bf16_f32 v162, v176, v177\n"
    "v_cvt_pk_bf16_f32 v163, v178, v179\n"
    "buffer_store_dwordx4 v[160:163], v254, s[20:23], s81 offen offset:0 sc1\n"
    "v_pk_fma_f32 v[236:237], v[192:193], v[212:213], v[196:197] op_sel:[0,1,0] op_sel_hi:[1,1,1]\n"
    "v_pk_fma_f32 v[238:239], v[194:195], v[212:213], v[198:199] op_sel:[0,1,0] op_sel_hi:[1,1,1]\n"
    "s_add_u32 m0, s78, 27648\n"
    "s_add_u32 s56, s68, 0\n"
    "buffer_load_dwordx4 v251, s[24:27], s56 offen offset:0 lds\n"
    "v_pk_fma_f32 v[240:241], v[200:201], v[212:213], v[204:205] op_sel:[0,1,0] op_sel_hi:[1,1,1]\n"
    "v_pk_fma_f32 v[242:243], v[202:203], v[212:213], v[206:207] op_sel:[0,1,0] op_sel_hi:[1,1,1]\n"
    "v_pk_fma_f32 v[164:165], v[164:165], v[212:213], v[236:237] op_sel_hi:[1,0,1]\n"
    "v_pk_fma_f32 v[166:167], v[166:167], v[212:213], v[238:239] op_sel_hi:[1,0,1]\n"
    "v_pk_fma_f32 v[180:181], v[180:181], v[212:213], v[240:241] op_sel_hi:[1,0,1]\n"
    "v_pk_fma_f32 v[182:183], v[182:183], v[212:213], v[242:243] op_sel_hi:[1,0,1]\n"
    "v_pk_mul_f32 v[236:237], v[164:165], v[164:165]\n"
    "v_pk_mul_f32 v[238:239], v[166:167], v[166:167]\n"
    "v_pk_mul_f32 v[240:241], v[180:181], v[180:181]\n"
    "v_pk_mul_f32 v[242:243], v[182:183], v[182:183]\n"
    "v_pk_fma_f32 v[236:237], v[236:237], s[8:9], s[8:9] op_sel:[0,0,1] op_sel_hi:[1,0,1]\n"
    "v_pk_fma_f32 v[238:239], v[238:239], s[8:9], s[8:9] op_sel:[0,0,1] op_sel_hi:[1,0,1]\n"
    "v_pk_fma_f32 v[240:241], v[240:241], s[8:9], s[8:9] op_sel:[0,0,1] op_sel_hi:[1,0,1]\n"
    "v_pk_fma_f32 v[242:243], v[242:243], s[8:9], s[8:9] op_sel:[0,0,1] op_sel_hi:[1,0,1]\n"
    "v_pk_mul_f32 v[236:237], v[164:165], v[236:237]\n"
    "v_pk_mul_f32 v[238:239], v[166:167], v[238:239]\n"
    "v_pk_mul_f32 v[240:241], v[180:181], v[240:241]\n"
    "v_pk_mul_f32 v[242:243], v[182:183], v[242:243]\n"
    "v_exp_f32 v236, v236\n"
    "v_exp_f32 v238, v238\n"
    "v_exp_f32 v240, v240\n"
    "buffer_load_dwordx4 v252, s[24:27], s56 offen offset:1024 lds\n"
    "v_exp_f32 v242, v242\n"
    "v_exp_f32 v237, v237\n"
    "v_exp_f32 v239, v239\n"
    "v_exp_f32 v241, v241\n"
    "v_exp_f32 v243, v243\n"
    "v_pk_fma_f32 v[236:237], v[236:237], v[214:215], v[214:215] op_sel_hi:[1,0,0]\n"
    "v_pk_fma_f32 v[238:239], v[238:239], v[214:215], v[214:215] op_sel_hi:[1,0,0]\n"
    "v_pk_fma_f32 v[240:241], v[240:241], v[214:215], v[214:215] op_sel_hi:[1,0,0]\n"
    "v_pk_fma_f32 v[242:243], v[242:243], v[214:215], v[214:215] op_sel_hi:[1,0,0]\n"
    "v_rcp_f32 v236, v236\n"
    "v_rcp_f32 v238, v238\n"
    "v_rcp_f32 v240, v240\n"
    "v_rcp_f32 v242, v242\n"
    "v_rcp_f32 v237, v237\n"
    "v_rcp_f32 v239, v239\n"
    "v_rcp_f32 v241, v241\n"
    "v_rcp_f32 v243, v243\n"
    "v_pk_mul_f32 v[164:165], v[164:165], v[236:237]\n"
    "v_pk_mul_f32 v[166:167], v[166:167], v[238:239]\n"
    "v_pk_mul_f32 v[180:181], v[180:181], v[240:241]\n"
    "v_pk_mul_f32 v[182:183], v[182:183], v[242:243]\n"
    "v_cvt_pk_bf16_f32 v164, v164, v165\n"
    "v_cvt_pk_bf16_f32 v165, v166, v167\n"
    "v_cvt_pk_bf16_f32 v166, v180, v181\n"
    "v_cvt_pk_bf16_f32 v167, v182, v183\n"
    "buffer_store_dwordx4 v[164:167], v254, s[20:23], s81 offen offset:1024 sc1\n"
    "v_pk_fma_f32 v[236:237], v[192:193], v[216:217], v[196:197] op_sel:[0,1,0] op_sel_hi:[1,1,1]\n"
    "v_pk_fma_f32 v[238:239], v[194:195], v[216:217], v[198:199] op_sel:[0,1,0] op_sel_hi:[1,1,1]\n"
    "s_add_u32 m0, s79, 27648\n"
    "s_nop 0\n"
    "buffer_load_dwordx4 v253, s[24:27], s56 offen offset:0 lds\n"
    "v_pk_fma_f32 v[240:241], v[200:201], v[216:217], v[204:205] op_sel:[0,1,0] op_sel_hi:[1,1,1]\n"
    "v_pk_fma_f32 v[242:243], v[202:203], v[216:217], v[206:207] op_sel:[0,1,0] op_sel_hi:[1,1,1]\n"
    "v_pk_fma_f32 v[168:169], v[168:169], v[216:217], v[236:237] op_sel_hi:[1,0,1]\n"
    "v_pk_fma_f32 v[170:171], v[170:171], v[216:217], v[238:239] op_sel_hi:[1,0,1]\n"
    "v_pk_fma_f32 v[184:185], v[184:185], v[216:217], v[240:241] op_sel_hi:[1,0,1]\n"
    "v_pk_fma_f32 v[186:187], v[186:187], v[216:217], v[242:243] op_sel_hi:[1,0,1]\n"
    "v_pk_mul_f32 v[236:237], v[168:169], v[168:169]\n"
    "v_pk_mul_f32 v[238:239], v[170:171], v[170:171]\n"
    "v_pk_mul_f32 v[240:241], v[184:185], v[184:185]\n"
    "v_pk_mul_f32 v[242:243], v[186:187], v[186:187]\n"
    "v_pk_fma_f32 v[236:237], v[236:237], s[8:9], s[8:9] op_sel:[0,0,1] op_sel_hi:[1,0,1]\n"
    "v_pk_fma_f32 v[238:239], v[238:239], s[8:9], s[8:9] op_sel:[0,0,1] op_sel_hi:[1,0,1]\n"
    "v_pk_fma_f32 v[240:241], v[240:241], s[8:9], s[8:9] op_sel:[0,0,1] op_sel_hi:[1,0,1]\n"
    "v_pk_fma_f32 v[242:243], v[242:243], s[8:9], s[8:9] op_sel:[0,0,1] op_sel_hi:[1,0,1]\n"
    "v_pk_mul_f32 v[236:237], v[168:169], v[236:237]\n"
    "v_pk_mul_f32 v[238:239], v[170:171], v[238:239]\n"
    "v_pk_mul_f32 v[240:241], v[184:185], v[240:241]\n"
    "v_pk_mul_f32 v[242:243], v[186:187], v[242:243]\n"
    "v_exp_f32 v236, v236\n"
    "v_exp_f32 v238, v238\n"
    "v_exp_f32 v240, v240\n"
    "s_add_u32 m0, s78, 36864\n"
    "s_add_u32 s56, s56, 36864\n"
    "buffer_load_dwordx4 v251, s[24:27], s56 offen offset:0 lds\n"
    "v_exp_f32 v242, v242\n"
    "v_exp_f32 v237, v237\n"
    "v_exp_f32 v239, v239\n"
    "v_exp_f32 v241, v241\n"
    "v_exp_f32 v243, v243\n"
    "v_pk_fma_f32 v[236:237], v[236:237], v[218:219], v[218:219] op_sel_hi:[1,0,0]\n"
    "v_pk_fma_f32 v[238:239], v[238:239], v[218:219], v[218:219] op_sel_hi:[1,0,0]\n"
    "v_pk_fma_f32 v[240:241], v[240:241], v[218:219], v[218:219] op_sel_hi:[1,0,0]\n"
    "v_pk_fma_f32 v[242:243], v[242:243], v[218:219], v[218:219] op_sel_hi:[1,0,0]\n"
    "v_rcp_f32 v236, v236\n"
    "v_rcp_f32 v238, v238\n"
    "v_rcp_f32 v240, v240\n"
    "v_rcp_f32 v242, v242\n"
    "v_rcp_f32 v237, v237\n"
    "v_rcp_f32 v239, v239\n"
    "v_rcp_f32 v241, v241\n"
    "v_rcp_f32 v243, v243\n"
    "v_pk_mul_f32 v[168:169], v[168:169], v[236:237]\n"
    "v_pk_mul_f32 v[170:171], v[170:171], v[238:239]\n"
    "v_pk_mul_f32 v[184:185], v[184:185], v[240:241]\n"
    "v_pk_mul_f32 v[186:187], v[186:187], v[242:243]\n"
    "v_cvt_pk_bf16_f32 v168, v168, v169\n"
    "v_cvt_pk_bf16_f32 v169, v170, v171\n"
    "v_cvt_pk_bf16_f32 v170, v184, v185\n"
    "v_cvt_pk_bf16_f32 v171, v186, v187\n"
    "buffer_store_dwordx4 v[168:171], v254, s[20:23], s81 offen offset:2048 sc1\n"
    "v_pk_fma_f32 v[236:237], v[192:193], v[220:221], v[196:197] op_sel:[0,1,0] op_sel_hi:[1,1,1]\n"
    "v_pk_fma_f32 v[238:239], v[194:195], v[220:221], v[198:199] op_sel:[0,1,0] op_sel_hi:[1,1,1]\n"
    "ds_read_b128 v[176:179], v230 offset:512\n"
    "ds_read_b128 v[180:183], v230 offset:1280\n"
    "ds_read_b128 v[184:187], v230 offset:576\n"
    "ds_read_b128 v[160:163], v230 offset:1344\n"
    "v_pk_fma_f32 v[240:241], v[200:201], v[220:221], v[204:205] op_sel:[0,1,0] op_sel_hi:[1,1,1]\n"
    "v_pk_fma_f32 v[242:243], v[202:203], v[220:221], v[206:207] op_sel:[0,1,0] op_sel_hi:[1,1,1]\n"
    "v_pk_fma_f32 v[172:173], v[172:173], v[220:221], v[236:237] op_sel_hi:[1,0,1]\n"
    "v_pk_fma_f32 v[174:175], v[174:175], v[220:221], v[238:239] op_sel_hi:[1,0,1]\n"
    "v_pk_fma_f32 v[188:189], v[188:189], v[220:221], v[240:241] op_sel_hi:[1,0,1]\n"
    "v_pk_fma_f32 v[190:191], v[190:191], v[220:221], v[242:243] op_sel_hi:[1,0,1]\n"
    "v_pk_mul_f32 v[236:237], v[172:173], v[172:173]\n"
    "v_pk_mul_f32 v[238:239], v[174:175], v[174:175]\n"
    "v_pk_mul_f32 v[240:241], v[188:189], v[188:189]\n"
    "v_pk_mul_f32 v[242:243], v[190:191], v[190:191]\n"
    "v_pk_fma_f32 v[236:237], v[236:237], s[8:9], s[8:9] op_sel:[0,0,1] op_sel_hi:[1,0,1]\n"
    "v_pk_fma_f32 v[238:239], v[238:239], s[8:9], s[8:9] op_sel:[0,0,1] op_sel_hi:[1,0,1]\n"
    "v_pk_fma_f32 v[240:241], v[240:241], s[8:9], s[8:9] op_sel:[0,0,1] op_sel_hi:[1,0,1]\n"
    "v_pk_fma_f32 v[242:243], v[242:243], s[8:9], s[8:9] op_sel:[0,0,1] op_sel_hi:[1,0,1]\n"
    "buffer_load_dwordx4 v252, s[24:27], s56 offen offset:1024 lds\n"
    "v_pk_mul_f32 v[236:237], v[172:173], v[236:237]\n"
    "v_pk_mul_f32 v[238:239], v[174:175], v[238:239]\n"
    "v_pk_mul_f32 v[240:241], v[188:189], v[240:241]\n"
    "v_pk_mul_f32 v[242:243], v[190:191], v[242:243]\n"
    "v_exp_f32 v236, v236\n"
    "v_exp_f32 v238, v238\n"
    "v_exp_f32 v240, v240\n"
    "v_exp_f32 v242, v242\n"
    "v_exp_f32 v237, v237\n"
    "v_exp_f32 v239, v239\n"
    "v_exp_f32 v241, v241\n"
    "v_exp_f32 v243, v243\n"
    "v_pk_fma_f32 v[236:237], v[236:237], v[222:223], v[222:223] op_sel_hi:[1,0,0]\n"
    "v_pk_fma_f32 v[238:239], v[238:239], v[222:223], v[222:223] op_sel_hi:[1,0,0]\n"
    "s_add_u32 m0, s78, 46080\n"
    "s_add_u32 s56, s68, 147456\n"
    "buffer_load_dwordx4 v251, s[24:27], s56 offen offset:0 lds\n"
    "v_pk_fma_f32 v[240:241], v[240:241], v[222:223], v[222:223] op_sel_hi:[1,0,0]\n"
    "v_pk_fma_f32 v[242:243], v[242:243], v[222:223], v[222:223] op_sel_hi:[1,0,0]\n"
    "v_rcp_f32 v236, v236\n"
    "v_rcp_f32 v238, v238\n"
    "v_rcp_f32 v240, v240\n"
    "v_rcp_f32 v242, v242\n"
    "v_rcp_f32 v237, v237\n"
    "v_rcp_f32 v239, v239\n"
    "v_rcp_f32 v241, v241\n"
    "v_rcp_f32 v243, v243\n"
    "v_pk_mul_f32 v[172:173], v[172:173], v[236:237]\n"
    "v_pk_mul_f32 v[174:175], v[174:175], v[238:239]\n"
    "v_pk_mul_f32 v[188:189], v[188:189], v[240:241]\n"
    "v_pk_mul_f32 v[190:191], v[190:191], v[242:243]\n"
    "v_cvt_pk_bf16_f32 v172, v172, v173\n"
    "v_cvt_pk_bf16_f32 v173, v174, v175\n"
    "v_cvt_pk_bf16_f32 v174, v188, v189\n"
    "v_cvt_pk_bf16_f32 v175, v190, v191\n"
    "buffer_store_dwordx4 v[172:175], v254, s[20:23], s81 offen offset:3072 sc1\n"
    "s_waitcnt lgkmcnt(2)\n"
    "v_pk_fma_f32 v[236:237], v[176:177], v[208:209], v[180:181] op_sel:[0,1,0] op_sel_hi:[1,1,1]\n"
    "v_pk_fma_f32 v[238:239], v[178:179], v[208:209], v[182:183] op_sel:[0,1,0] op_sel_hi:[1,1,1]\n"
    "s_add_u32 s81, s74, 0\n"
    "s_waitcnt lgkmcnt(0)\n"
    "v_pk_fma_f32 v[240:241], v[184:185], v[208:209], v[160:161] op_sel:[0,1,0] op_sel_hi:[1,1,1]\n"
    "v_pk_fma_f32 v[242:243], v[186:187], v[208:209], v[162:163] op_sel:[0,1,0] op_sel_hi:[1,1,1]\n"
    "v_pk_fma_f32 v[128:129], v[128:129], v[208:209], v[236:237] op_sel_hi:[1,0,1]\n"
    "v_pk_fma_f32 v[130:131], v[130:131], v[208:209], v[238:239] op_sel_hi:[1,0,1]\n"
    "v_pk_fma_f32 v[144:145], v[144:145], v[208:209], v[240:241] op_sel_hi:[1,0,1]\n"
    "v_pk_fma_f32 v[146:147], v[146:147], v[208:209], v[242:243] op_sel_hi:[1,0,1]\n"
    "v_pk_mul_f32 v[236:237], v[128:129], v[128:129]\n"
    "v_pk_mul_f32 v[238:239], v[130:131], v[130:131]\n"
    "v_pk_mul_f32 v[240:241], v[144:145], v[144:145]\n"
    "v_pk_mul_f32 v[242:243], v[146:147], v[146:147]\n"
    "s_barrier\n"
    "v_pk_fma_f32 v[236:237], v[236:237], s[8:9], s[8:9] op_sel:[0,0,1] op_sel_hi:[1,0,1]\n"
    "v_pk_fma_f32 v[238:239], v[238:239], s[8:9], s[8:9] op_sel:[0,0,1] op_sel_hi:[1,0,1]\n"
    "v_pk_fma_f32 v[240:241], v[240:241], s[8:9], s[8:9] op_sel:[0,0,1] op_sel_hi:[1,0,1]\n"
    "v_pk_fma_f32 v[242:243], v[242:243], s[8:9], s[8:9] op_sel:[0,0,1] op_sel_hi:[1,0,1]\n"
    "v_pk_mul_f32 v[236:237], v[128:129], v[236:237]\n"
    "v_pk_mul_f32 v[238:239], v[130:131], v[238:239]\n"
    "v_pk_mul_f32 v[240:241], v[144:145], v[240:241]\n"
    "v_pk_mul_f32 v[242:243], v[146:147], v[242:243]\n"
    "v_exp_f32 v236, v236\n"
    "v_exp_f32 v238, v238\n"
    "v_exp_f32 v240, v240\n"
    "buffer_load_dwordx4 v252, s[24:27], s56 offen offset:1024 lds\n"
    "v_exp_f32 v242, v242\n"
    "v_exp_f32 v237, v237\n"
    "v_exp_f32 v239, v239\n"
    "v_exp_f32 v241, v241\n"
    "v_exp_f32 v243, v243\n"
    "v_pk_fma_f32 v[236:237], v[236:237], v[210:211], v[210:211] op_sel_hi:[1,0,0]\n"
    "v_pk_fma_f32 v[238:239], v[238:239], v[210:211], v[210:211] op_sel_hi:[1,0,0]\n"
    "v_pk_fma_f32 v[240:241], v[240:241], v[210:211], v[210:211] op_sel_hi:[1,0,0]\n"
    "v_pk_fma_f32 v[242:243], v[242:243], v[210:211], v[210:211] op_sel_hi:[1,0,0]\n"
    "v_rcp_f32 v236, v236\n"
    "s_add_u32 m0, s79, 46080\n"
    "s_nop 0\n"
    "buffer_load_dwordx4 v253, s[24:27], s56 offen offset:0 lds\n"
    "v_rcp_f32 v238, v238\n"
    "v_rcp_f32 v240, v240\n"
    "v_rcp_f32 v242, v242\n"
    "v_rcp_f32 v237, v237\n"
    "v_rcp_f32 v239, v239\n"
    "v_rcp_f32 v241, v241\n"
    "v_rcp_f32 v243, v243\n"
    "v_pk_mul_f32 v[128:129], v[128:129], v[236:237]\n"
    "v_pk_mul_f32 v[130:131], v[130:131], v[238:239]\n"
    "v_pk_mul_f32 v[144:145], v[144:145], v[240:241]\n"
    "v_pk_mul_f32 v[146:147], v[146:147], v[242:243]\n"
    "v_cvt_pk_bf16_f32 v128, v128, v129\n"
    "v_cvt_pk_bf16_f32 v129, v130, v131\n"
    "v_cvt_pk_bf16_f32 v130, v144, v145\n"
    "v_cvt_pk_bf16_f32 v131, v146, v147\n"
    "buffer_store_dwordx4 v[128:131], v254, s[20:23], s81 offen offset:0 sc1\n"
    "v_pk_fma_f32 v[236:237], v[176:177], v[212:213], v[180:181] op_sel:[0,1,0] op_sel_hi:[1,1,1]\n"
    "v_pk_fma_f32 v[238:239], v[178:179], v[212:213], v[182:183] op_sel:[0,1,0] op_sel_hi:[1,1,1]\n"
    "s_add_u32 m0, s78, 55296\n"
    "s_add_u32 s56, s56, 36864\n"
    "buffer_load_dwordx4 v251, s[24:27], s56 offen offset:0 lds\n"
    "v_pk_fma_f32 v[240:241], v[184:185], v[212:213], v[160:161] op_sel:[0,1,0] op_sel_hi:[1,1,1]\n"
    "v_pk_fma_f32 v[242:243], v[186:187], v[212:213], v[162:163] op_sel:[0,1,0] op_sel_hi:[1,1,1]\n"
    "v_pk_fma_f32 v[132:133], v[132:133], v[212:213], v[236:237] op_sel_hi:[1,0,1]\n"
    "v_pk_fma_f32 v[134:135], v[134:135], v[212:213], v[238:239] op_sel_hi:[1,0,1]\n"
    "v_pk_fma_f32 v[148:149], v[148:149], v[212:213], v[240:241] op_sel_hi:[1,0,1]\n"
    "v_pk_fma_f32 v[150:151], v[150:151], v[212:213], v[242:243] op_sel_hi:[1,0,1]\n"
    "v_pk_mul_f32 v[236:237], v[132:133], v[132:133]\n"
    "v_pk_mul_f32 v[238:239], v[134:135], v[134:135]\n"
    "v_pk_mul_f32 v[240:241], v[148:149], v[148:149]\n"
    "v_pk_mul_f32 v[242:243], v[150:151], v[150:151]\n"
    "v_pk_fma_f32 v[236:237], v[236:237], s[8:9], s[8:9] op_sel:[0,0,1] op_sel_hi:[1,0,1]\n"
    "v_pk_fma_f32 v[238:239], v[238:239], s[8:9], s[8:9] op_sel:[0,0,1] op_sel_hi:[1,0,1]\n"
    "v_pk_fma_f32 v[240:241], v[240:241], s[8:9], s[8:9] op_sel:[0,0,1] op_sel_hi:[1,0,1]\n"
    "v_pk_fma_f32 v[242:243], v[242:243], s[8:9], s[8:9] op_sel:[0,0,1] op_sel_hi:[1,0,1]\n"
    "v_pk_mul_f32 v[236:237], v[132:133], v[236:237]\n"
    "v_pk_mul_f32 v[238:239], v[134:135], v[238:239]\n"
    "v_pk_mul_f32 v[240:241], v[148:149], v[240:241]\n"
    "v_pk_mul_f32 v[242:243], v[150:151], v[242:243]\n"
    "v_exp_f32 v236, v236\n"
    "v_exp_f32 v238, v238\n"
    "v_exp_f32 v240, v240\n"
    "buffer_load_dwordx4 v252, s[24:27], s56 offen offset:1024 lds\n"
    "v_exp_f32 v242, v242\n"
    "v_exp_f32 v237, v237\n"
    "v_exp_f32 v239, v239\n"
    "v_exp_f32 v241, v241\n"
    "v_exp_f32 v243, v243\n"
    "v_pk_fma_f32 v[236:237], v[236:237], v[214:215], v[214:215] op_sel_hi:[1,0,0]\n"
    "v_pk_fma_f32 v[238:239], v[238:239], v[214:215], v[214:215] op_sel_hi:[1,0,0]\n"
    "v_pk_fma_f32 v[240:241], v[240:241], v[214:215], v[214:215] op_sel_hi:[1,0,0]\n"
    "v_pk_fma_f32 v[242:243], v[242:243], v[214:215], v[214:215] op_sel_hi:[1,0,0]\n"
    "v_rcp_f32 v236, v236\n"
    "v_rcp_f32 v238, v238\n"
    "v_rcp_f32 v240, v240\n"
    "v_rcp_f32 v242, v242\n"
    "v_rcp_f32 v237, v237\n"
    "v_rcp_f32 v239, v239\n"
    "v_rcp_f32 v241, v241\n"
    "v_rcp_f32 v243, v243\n"
    "v_pk_mul_f32 v[132:133], v[132:133], v[236:237]\n"
    "v_pk_mul_f32 v[134:135], v[134:135], v[238:239]\n"
    "v_pk_mul_f32 v[148:149], v[148:149], v[240:241]\n"
    "v_pk_mul_f32 v[150:151], v[150:151], v[242:243]\n"
    "v_cvt_pk_bf16_f32 v132, v132, v133\n"
    "v_cvt_pk_bf16_f32 v133, v134, v135\n"
    "v_cvt_pk_bf16_f32 v134, v148, v149\n"
    "v_cvt_pk_bf16_f32 v135, v150, v151\n"
    "buffer_store_dwordx4 v[132:135], v254, s[20:23], s81 offen offset:1024 sc1\n"
    "v_pk_fma_f32 v[236:237], v[176:177], v[216:217], v[180:181] op_sel:[0,1,0] op_sel_hi:[1,1,1]\n"
    "v_pk_fma_f32 v[238:239], v[178:179], v[216:217], v[182:183] op_sel:[0,1,0] op_sel_hi:[1,1,1]\n"
    "s_add_u32 m0, s78, 64512\n"
    "s_add_u32 s56, s69, 147456\n"
    "buffer_load_dwordx4 v251, s[24:27], s56 offen offset:0 lds\n"
    "v_pk_fma_f32 v[240:241], v[184:185], v[216:217], v[160:161] op_sel:[0,1,0] op_sel_hi:[1,1,1]\n"
    "v_pk_fma_f32 v[242:243], v[186:187], v[216:217], v[162:163] op_sel:[0,1,0] op_sel_hi:[1,1,1]\n"
    "v_pk_fma_f32 v[136:137], v[136:137], v[216:217], v[236:237] op_sel_hi:[1,0,1]\n"
    "v_pk_fma_f32 v[138:139], v[138:139], v[216:217], v[238:239] op_sel_hi:[1,0,1]\n"
    "v_pk_fma_f32 v[152:153], v[152:153], v[216:217], v[240:241] op_sel_hi:[1,0,1]\n"
    "v_pk_fma_f32 v[154:155], v[154:155], v[216:217], v[242:243] op_sel_hi:[1,0,1]\n"
    "v_pk_mul_f32 v[236:237], v[136:137], v[136:137]\n"
    "v_pk_mul_f32 v[238:239], v[138:139], v[138:139]\n"
    "v_pk_mul_f32 v[240:241], v[152:153], v[152:153]\n"
    "v_pk_mul_f32 v[242:243], v[154:155], v[154:155]\n"
    "v_pk_fma_f32 v[236:237], v[236:237], s[8:9], s[8:9] op_sel:[0,0,1] op_sel_hi:[1,0,1]\n"
    "v_pk_fma_f32 v[238:239], v[238:239], s[8:9], s[8:9] op_sel:[0,0,1] op_sel_hi:[1,0,1]\n"
    "v_pk_fma_f32 v[240:241], v[240:241], s[8:9], s[8:9] op_sel:[0,0,1] op_sel_hi:[1,0,1]\n"
    "v_pk_fma_f32 v[242:243], v[242:243], s[8:9], s[8:9] op_sel:[0,0,1] op_sel_hi:[1,0,1]\n"
    "v_pk_mul_f32 v[236:237], v[136:137], v[236:237]\n"
    "v_pk_mul_f32 v[238:239], v[138:139], v[238:239]\n"
    "v_pk_mul_f32 v[240:241], v[152:153], v[240:241]\n"
    "v_pk_mul_f32 v[242:243], v[154:155], v[242:243]\n"
    "v_exp_f32 v236, v236\n"
    "v_exp_f32 v238, v238\n"
    "v_exp_f32 v240, v240\n"
    "buffer_load_dwordx4 v252, s[24:27], s56 offen offset:1024 lds\n"
    "v_exp_f32 v242, v242\n"
    "v_exp_f32 v237, v237\n"
    "v_exp_f32 v239, v239\n"
    "v_exp_f32 v241, v241\n"
    "v_exp_f32 v243, v243\n"
    "v_pk_fma_f32 v[236:237], v[236:237], v[218:219], v[218:219] op_sel_hi:[1,0,0]\n"
    "v_pk_fma_f32 v[238:239], v[238:239], v[218:219], v[218:219] op_sel_hi:[1,0,0]\n"
    "v_pk_fma_f32 v[240:241], v[240:241], v[218:219], v[218:219] op_sel_hi:[1,0,0]\n"
    "v_pk_fma_f32 v[242:243], v[242:243], v[218:219], v[218:219] op_sel_hi:[1,0,0]\n"
    "v_rcp_f32 v236, v236\n"
    "v_rcp_f32 v238, v238\n"
    "v_rcp_f32 v240, v240\n"
    "v_rcp_f32 v242, v242\n"
    "v_rcp_f32 v237, v237\n"
    "v_rcp_f32 v239, v239\n"
    "v_rcp_f32 v241, v241\n"
    "v_rcp_f32 v243, v243\n"
    "v_pk_mul_f32 v[136:137], v[136:137], v[236:237]\n"
    "v_pk_mul_f32 v[138:139], v[138:139], v[238:239]\n"
    "v_pk_mul_f32 v[152:153], v[152:153], v[240:241]\n"
    "v_pk_mul_f32 v[154:155], v[154:155], v[242:243]\n"
    "v_cvt_pk_bf16_f32 v136, v136, v137\n"
    "v_cvt_pk_bf16_f32 v137, v138, v139\n"
    "v_cvt_pk_bf16_f32 v138, v152, v153\n"
    "v_cvt_pk_bf16_f32 v139, v154, v155\n"
    "buffer_store_dwordx4 v[136:139], v254, s[20:23], s81 offen offset:2048 sc1\n"
    "v_pk_fma_f32 v[236:237], v[176:177], v[220:221], v[180:181] op_sel:[0,1,0] op_sel_hi:[1,1,1]\n"
    "v_pk_fma_f32 v[238:239], v[178:179], v[220:221], v[182:183] op_sel:[0,1,0] op_sel_hi:[1,1,1]\n"
    "ds_read_b128 v[144:147], v230 offset:384\n"
    "ds_read_b128 v[148:151], v230 offset:1152\n"
    "ds_read_b128 v[152:155], v230 offset:448\n"
    "ds_read_b128 v[128:131], v230 offset:1216\n"
    "v_pk_fma_f32 v[240:241], v[184:185], v[220:221], v[160:161] op_sel:[0,1,0] op_sel_hi:[1,1,1]\n"
    "v_pk_fma_f32 v[242:243], v[186:187], v[220:221], v[162:163] op_sel:[0,1,0] op_sel_hi:[1,1,1]\n"
    "v_pk_fma_f32 v[140:141], v[140:141], v[220:221], v[236:237] op_sel_hi:[1,0,1]\n"
    "v_pk_fma_f32 v[142:143], v[142:143], v[220:221], v[238:239] op_sel_hi:[1,0,1]\n"
    "v_pk_fma_f32 v[156:157], v[156:157], v[220:221], v[240:241] op_sel_hi:[1,0,1]\n"
    "v_pk_fma_f32 v[158:159], v[158:159], v[220:221], v[242:243] op_sel_hi:[1,0,1]\n"
    "v_pk_mul_f32 v[236:237], v[140:141], v[140:141]\n"
    "v_pk_mul_f32 v[238:239], v[142:143], v[142:143]\n"
    "v_pk_mul_f32 v[240:241], v[156:157], v[156:157]\n"
    "v_pk_mul_f32 v[242:243], v[158:159], v[158:159]\n"
    "v_pk_fma_f32 v[236:237], v[236:237], s[8:9], s[8:9] op_sel:[0,0,1] op_sel_hi:[1,0,1]\n"
    "v_pk_fma_f32 v[238:239], v[238:239], s[8:9], s[8:9] op_sel:[0,0,1] op_sel_hi:[1,0,1]\n"
    "v_pk_fma_f32 v[240:241], v[240:241], s[8:9], s[8:9] op_sel:[0,0,1] op_sel_hi:[1,0,1]\n"
    "v_pk_fma_f32 v[242:243], v[242:243], s[8:9], s[8:9] op_sel:[0,0,1] op_sel_hi:[1,0,1]\n"
    "s_add_u32 m0, s79, 64512\n"
    "s_nop 0\n"
    "buffer_load_dwordx4 v253, s[24:27], s56 offen offset:0 lds\n"
    "v_pk_mul_f32 v[236:237], v[140:141], v[236:237]\n"
    "v_pk_mul_f32 v[238:239], v[142:143], v[238:239]\n"
    "v_pk_mul_f32 v[240:241], v[156:157], v[240:241]\n"
    "v_pk_mul_f32 v[242:243], v[158:159], v[242:243]\n"
    "v_exp_f32 v236, v236\n"
    "v_exp_f32 v238, v238\n"
    "v_exp_f32 v240, v240\n"
    "v_exp_f32 v242, v242\n"
    "v_exp_f32 v237, v237\n"
    "v_exp_f32 v239, v239\n"
    "v_exp_f32 v241, v241\n"
    "v_exp_f32 v243, v243\n"
    "v_pk_fma_f32 v[236:237], v[236:237], v[222:223], v[222:223] op_sel_hi:[1,0,0]\n"
    "v_pk_fma_f32 v[238:239], v[238:239], v[222:223], v[222:223] op_sel_hi:[1,0,0]\n"
    "s_add_u32 m0, s78, 73728\n"
    "s_add_u32 s56, s56, 36864\n"
    "buffer_load_dwordx4 v251, s[24:27], s56 offen offset:0 lds\n"
    "v_pk_fma_f32 v[240:241], v[240:241], v[222:223], v[222:223] op_sel_hi:[1,0,0]\n"
    "v_pk_fma_f32 v[242:243], v[242:243], v[222:223], v[222:223] op_sel_hi:[1,0,0]\n"
    "v_rcp_f32 v236, v236\n"
    "v_rcp_f32 v238, v238\n"
    "v_rcp_f32 v240, v240\n"
    "v_rcp_f32 v242, v242\n"
    "v_rcp_f32 v237, v237\n"
    "v_rcp_f32 v239, v239\n"
    "v_rcp_f32 v241, v241\n"
    "v_rcp_f32 v243, v243\n"
    "v_pk_mul_f32 v[140:141], v[140:141], v[236:237]\n"
    "v_pk_mul_f32 v[142:143], v[142:143], v[238:239]\n"
    "v_pk_mul_f32 v[156:157], v[156:157], v[240:241]\n"
    "v_pk_mul_f32 v[158:159], v[158:159], v[242:243]\n"
    "v_cvt_pk_bf16_f32 v140, v140, v141\n"
    "v_cvt_pk_bf16_f32 v141, v142, v143\n"
    "v_cvt_pk_bf16_f32 v142, v156, v157\n"
    "v_cvt_pk_bf16_f32 v143, v158, v159\n"
    "buffer_store_dwordx4 v[140:143], v254, s[20:23], s81 offen offset:3072 sc1\n"
    "s_waitcnt lgkmcnt(2)\n"
    "v_pk_fma_f32 v[236:237], v[144:145], v[208:209], v[148:149] op_sel:[0,1,0] op_sel_hi:[1,1,1]\n"
    "v_pk_fma_f32 v[238:239], v[146:147], v[208:209], v[150:151] op_sel:[0,1,0] op_sel_hi:[1,1,1]\n"
    "s_add_u32 s81, s74, 12288\n"
    "s_waitcnt lgkmcnt(0)\n"
    "v_pk_fma_f32 v[240:241], v[152:153], v[208:209], v[128:129] op_sel:[0,1,0] op_sel_hi:[1,1,1]\n"
    "v_pk_fma_f32 v[242:243], v[154:155], v[208:209], v[130:131] op_sel:[0,1,0] op_sel_hi:[1,1,1]\n"
    "v_pk_fma_f32 v[96:97], v[96:97], v[208:209], v[236:237] op_sel_hi:[1,0,1]\n"
    "v_pk_fma_f32 v[98:99], v[98:99], v[208:209], v[238:239] op_sel_hi:[1,0,1]\n"
    "v_pk_fma_f32 v[112:113], v[112:113], v[208:209], v[240:241] op_sel_hi:[1,0,1]\n"
    "v_pk_fma_f32 v[114:115], v[114:115], v[208:209], v[242:243] op_sel_hi:[1,0,1]\n"
    "v_pk_mul_f32 v[236:237], v[96:97], v[96:97]\n"
    "v_pk_mul_f32 v[238:239], v[98:99], v[98:99]\n"
    "v_pk_mul_f32 v[240:241], v[112:113], v[112:113]\n"
    "v_pk_mul_f32 v[242:243], v[114:115], v[114:115]\n"
    "v_pk_fma_f32 v[236:237], v[236:237], s[8:9], s[8:9] op_sel:[0,0,1] op_sel_hi:[1,0,1]\n"
    "v_pk_fma_f32 v[238:239], v[238:239], s[8:9], s[8:9] op_sel:[0,0,1] op_sel_hi:[1,0,1]\n"
    "v_pk_fma_f32 v[240:241], v[240:241], s[8:9], s[8:9] op_sel:[0,0,1] op_sel_hi:[1,0,1]\n"
    "v_pk_fma_f32 v[242:243], v[242:243], s[8:9], s[8:9] op_sel:[0,0,1] op_sel_hi:[1,0,1]\n"
    "buffer_load_dwordx4 v252, s[24:27], s56 offen offset:1024 lds\n"
    "v_pk_mul_f32 v[236:237], v[96:97], v[236:237]\n"
    "v_pk_mul_f32 v[238:239], v[98:99], v[238:239]\n"
    "v_pk_mul_f32 v[240:241], v[112:113], v[240:241]\n"
    "v_pk_mul_f32 v[242:243], v[114:115], v[242:243]\n"
    "v_exp_f32 v236, v236\n"
    "v_exp_f32 v238, v238\n"
    "v_exp_f32 v240, v240\n"
    "v_exp_f32 v242, v242\n"
    "v_exp_f32 v237, v237\n"
    "v_exp_f32 v239, v239\n"
    "v_exp_f32 v241, v241\n"
    "v_exp_f32 v243, v243\n"
    "v_pk_fma_f32 v[236:237], v[236:237], v[210:211], v[210:211] op_sel_hi:[1,0,0]\n"
    "v_pk_fma_f32 v[238:239], v[238:239], v[210:211], v[210:211] op_sel_hi:[1,0,0]\n"
    "s_add_u32 m0, s78, 82944\n"
    "s_add_u32 s56, s70, 147456\n"
    "buffer_load_dwordx4 v251, s[24:27], s56 offen offset:0 lds\n"
    "v_pk_fma_f32 v[240:241], v[240:241], v[210:211], v[210:211] op_sel_hi:[1,0,0]\n"
    "v_pk_fma_f32 v[242:243], v[242:243], v[210:211], v[210:211] op_sel_hi:[1,0,0]\n"
    "v_rcp_f32 v236, v236\n"
    "v_rcp_f32 v238, v238\n"
    "v_rcp_f32 v240, v240\n"
    "v_rcp_f32 v242, v242\n"
    "v_rcp_f32 v237, v237\n"
    "v_rcp_f32 v239, v239\n"
    "v_rcp_f32 v241, v241\n"
    "v_rcp_f32 v243, v243\n"
    "v_pk_mul_f32 v[96:97], v[96:97], v[236:237]\n"
    "v_pk_mul_f32 v[98:99], v[98:99], v[238:239]\n"
    "v_pk_mul_f32 v[112:113], v[112:113], v[240:241]\n"
    "v_pk_mul_f32 v[114:115], v[114:115], v[242:243]\n"
    "v_cvt_pk_bf16_f32 v160, v96, v97\n"
    "v_cvt_pk_bf16_f32 v161, v98, v99\n"
    "v_cvt_pk_bf16_f32 v162, v112, v113\n"
    "v_cvt_pk_bf16_f32 v163, v114, v115\n"
    "buffer_store_dwordx4 v[160:163], v254, s[20:23], s81 offen offset:0 sc1\n"
    "v_pk_fma_f32 v[236:237], v[144:145], v[212:213], v[148:149] op_sel:[0,1,0] op_sel_hi:[1,1,1]\n"
    "v_pk_fma_f32 v[238:239], v[146:147], v[212:213], v[150:151] op_sel:[0,1,0] op_sel_hi:[1,1,1]\n"
    "buffer_load_dwordx4 v252, s[24:27], s56 offen offset:1024 lds\n"
    "v_pk_fma_f32 v[240:241], v[152:153], v[212:213], v[128:129] op_sel:[0,1,0] op_sel_hi:[1,1,1]\n"
    "v_pk_fma_f32 v[242:243], v[154:155], v[212:213], v[130:131] op_sel:[0,1,0] op_sel_hi:[1,1,1]\n"
    "v_pk_fma_f32 v[100:101], v[100:101], v[212:213], v[236:237] op_sel_hi:[1,0,1]\n"
    "v_pk_fma_f32 v[102:103], v[102:103], v[212:213], v[238:239] op_sel_hi:[1,0,1]\n"
    "v_pk_fma_f32 v[116:117], v[116:117], v[212:213], v[240:241] op_sel_hi:[1,0,1]\n"
    "v_pk_fma_f32 v[118:119], v[118:119], v[212:213], v[242:243] op_sel_hi:[1,0,1]\n"
    "v_pk_mul_f32 v[236:237], v[100:101], v[100:101]\n"
    "v_pk_mul_f32 v[238:239], v[102:103], v[102:103]\n"
    "v_pk_mul_f32 v[240:241], v[116:117], v[116:117]\n"
    "v_pk_mul_f32 v[242:243], v[118:119], v[118:119]\n"
    "v_pk_fma_f32 v[236:237], v[236:237], s[8:9], s[8:9] op_sel:[0,0,1] op_sel_hi:[1,0,1]\n"
    "v_pk_fma_f32 v[238:239], v[238:239], s[8:9], s[8:9] op_sel:[0,0,1] op_sel_hi:[1,0,1]\n"
    "v_pk_fma_f32 v[240:241], v[240:241], s[8:9], s[8:9] op_sel:[0,0,1] op_sel_hi:[1,0,1]\n"
    "v_pk_fma_f32 v[242:243], v[242:243], s[8:9], s[8:9] op_sel:[0,0,1] op_sel_hi:[1,0,1]\n"
    "v_pk_mul_f32 v[236:237], v[100:101], v[236:237]\n"
    "v_pk_mul_f32 v[238:239], v[102:103], v[238:239]\n"
    "v_pk_mul_f32 v[240:241], v[116:117], v[240:241]\n"
    "v_pk_mul_f32 v[242:243], v[118:119], v[242:243]\n"
    "v_exp_f32 v236, v236\n"
    "v_exp_f32 v238, v238\n"
    "v_exp_f32 v240, v240\n"
    "s_add_u32 m0, s79, 82944\n"
    "s_nop 0\n"
    "buffer_load_dwordx4 v253, s[24:27], s56 offen offset:0 lds\n"
    "v_exp_f32 v242, v242\n"
    "v_exp_f32 v237, v237\n"
    "v_exp_f32 v239, v239\n"
    "v_exp_f32 v241, v241\n"
    "v_exp_f32 v243, v243\n"
    "v_pk_fma_f32 v[236:237], v[236:237], v[214:215], v[214:215] op_sel_hi:[1,0,0]\n"
    "v_pk_fma_f32 v[238:239], v[238:239], v[214:215], v[214:215] op_sel_hi:[1,0,0]\n"
    "v_pk_fma_f32 v[240:241], v[240:241], v[214:215], v[214:215] op_sel_hi:[1,0,0]\n"
    "v_pk_fma_f32 v[242:243], v[242:243], v[214:215], v[214:215] op_sel_hi:[1,0,0]\n"
    "v_rcp_f32 v236, v236\n"
    "v_rcp_f32 v238, v238\n"
    "v_rcp_f32 v240, v240\n"
    "v_rcp_f32 v242, v242\n"
    "v_rcp_f32 v237, v237\n"
    "v_rcp_f32 v239, v239\n"
    "v_rcp_f32 v241, v241\n"
    "v_rcp_f32 v243, v243\n"
    "v_pk_mul_f32 v[100:101], v[100:101], v[236:237]\n"
    "v_pk_mul_f32 v[102:103], v[102:103], v[238:239]\n"
    "v_pk_mul_f32 v[116:117], v[116:117], v[240:241]\n"
    "v_pk_mul_f32 v[118:119], v[118:119], v[242:243]\n"
    "v_cvt_pk_bf16_f32 v164, v100, v101\n"
    "v_cvt_pk_bf16_f32 v165, v102, v103\n"
    "v_cvt_pk_bf16_f32 v166, v116, v117\n"
    "v_cvt_pk_bf16_f32 v167, v118, v119\n"
    "buffer_store_dwordx4 v[164:167], v254, s[20:23], s81 offen offset:1024 sc1\n"
    "v_pk_fma_f32 v[236:237], v[144:145], v[216:217], v[148:149] op_sel:[0,1,0] op_sel_hi:[1,1,1]\n"
    "v_pk_fma_f32 v[238:239], v[146:147], v[216:217], v[150:151] op_sel:[0,1,0] op_sel_hi:[1,1,1]\n"
    "s_add_u32 m0, s78, 92160\n"
    "s_add_u32 s56, s56, 36864\n"
    "buffer_load_dwordx4 v251, s[24:27], s56 offen offset:0 lds\n"
    "v_pk_fma_f32 v[240:241], v[152:153], v[216:217], v[128:129] op_sel:[0,1,0] op_sel_hi:[1,1,1]\n"
    "v_pk_fma_f32 v[242:243], v[154:155], v[216:217], v[130:131] op_sel:[0,1,0] op_sel_hi:[1,1,1]\n"
    "v_pk_fma_f32 v[104:105], v[104:105], v[216:217], v[236:237] op_sel_hi:[1,0,1]\n"
    "v_pk_fma_f32 v[106:107], v[106:107], v[216:217], v[238:239] op_sel_hi:[1,0,1]\n"
    "v_pk_fma_f32 v[120:121], v[120:121], v[216:217], v[240:241] op_sel_hi:[1,0,1]\n"
    "v_pk_fma_f32 v[122:123], v[122:123], v[216:217], v[242:243] op_sel_hi:[1,0,1]\n"
    "v_pk_mul_f32 v[236:237], v[104:105], v[104:105]\n"
    "v_pk_mul_f32 v[238:239], v[106:107], v[106:107]\n"
    "v_pk_mul_f32 v[240:241], v[120:121], v[120:121]\n"
    "v_pk_mul_f32 v[242:243], v[122:123], v[122:123]\n"
    "v_pk_fma_f32 v[236:237], v[236:237], s[8:9], s[8:9] op_sel:[0,0,1] op_sel_hi:[1,0,1]\n"
    "v_pk_fma_f32 v[238:239], v[238:239], s[8:9], s[8:9] op_sel:[0,0,1] op_sel_hi:[1,0,1]\n"
    "v_pk_fma_f32 v[240:241], v[240:241], s[8:9], s[8:9] op_sel:[0,0,1] op_sel_hi:[1,0,1]\n"
    "v_pk_fma_f32 v[242:243], v[242:243], s[8:9], s[8:9] op_sel:[0,0,1] op_sel_hi:[1,0,1]\n"
    "v_pk_mul_f32 v[236:237], v[104:105], v[236:237]\n"
    "v_pk_mul_f32 v[238:239], v[106:107], v[238:239]\n"
    "v_pk_mul_f32 v[240:241], v[120:121], v[240:241]\n"
    "v_pk_mul_f32 v[242:243], v[122:123], v[242:243]\n"
    "v_exp_f32 v236, v236\n"
    "v_exp_f32 v238, v238\n"
    "v_exp_f32 v240, v240\n"
    "buffer_load_dwordx4 v252, s[24:27], s56 offen offset:1024 lds\n"
    "v_exp_f32 v242, v242\n"
    "v_exp_f32 v237, v237\n"
    "v_exp_f32 v239, v239\n"
    "v_exp_f32 v241, v241\n"
    "v_exp_f32 v243, v243\n"
    "v_pk_fma_f32 v[236:237], v[236:237], v[218:219], v[218:219] op_sel_hi:[1,0,0]\n"
    "v_pk_fma_f32 v[238:239], v[238:239], v[218:219], v[218:219] op_sel_hi:[1,0,0]\n"
    "v_pk_fma_f32 v[240:241], v[240:241], v[218:219], v[218:219] op_sel_hi:[1,0,0]\n"
    "v_pk_fma_f32 v[242:243], v[242:243], v[218:219], v[218:219] op_sel_hi:[1,0,0]\n"
    "v_rcp_f32 v236, v236\n"
    "v_rcp_f32 v238, v238\n"
    "v_rcp_f32 v240, v240\n"
    "v_rcp_f32 v242, v242\n"
    "v_rcp_f32 v237, v237\n"
    "v_rcp_f32 v239, v239\n"
    "v_rcp_f32 v241, v241\n"
    "v_rcp_f32 v243, v243\n"
    "v_pk_mul_f32 v[104:105], v[104:105], v[236:237]\n"
    "v_pk_mul_f32 v[106:107], v[106:107], v[238:239]\n"
    "v_pk_mul_f32 v[120:121], v[120:121], v[240:241]\n"
    "v_pk_mul_f32 v[122:123], v[122:123], v[242:243]\n"
    "v_cvt_pk_bf16_f32 v168, v104, v105\n"
    "v_cvt_pk_bf16_f32 v169, v106, v107\n"
    "v_cvt_pk_bf16_f32 v170, v120, v121\n"
    "v_cvt_pk_bf16_f32 v171, v122, v123\n"
    "buffer_store_dwordx4 v[168:171], v254, s[20:23], s81 offen offset:2048 sc1\n"
    "v_pk_fma_f32 v[236:237], v[144:145], v[220:221], v[148:149] op_sel:[0,1,0] op_sel_hi:[1,1,1]\n"
    "v_pk_fma_f32 v[238:239], v[146:147], v[220:221], v[150:151] op_sel:[0,1,0] op_sel_hi:[1,1,1]\n"
    "ds_read_b128 v[112:115], v230 offset:256\n"
    "ds_read_b128 v[116:119], v230 offset:1024\n"
    "ds_read_b128 v[120:123], v230 offset:320\n"
    "ds_read_b128 v[96:99], v230 offset:1088\n"
    "v_pk_fma_f32 v[240:241], v[152:153], v[220:221], v[128:129] op_sel:[0,1,0] op_sel_hi:[1,1,1]\n"
    "v_pk_fma_f32 v[242:243], v[154:155], v[220:221], v[130:131] op_sel:[0,1,0] op_sel_hi:[1,1,1]\n"
    "v_pk_fma_f32 v[108:109], v[108:109], v[220:221], v[236:237] op_sel_hi:[1,0,1]\n"
    "v_pk_fma_f32 v[110:111], v[110:111], v[220:221], v[238:239] op_sel_hi:[1,0,1]\n"
    "v_pk_fma_f32 v[124:125], v[124:125], v[220:221], v[240:241] op_sel_hi:[1,0,1]\n"
    "v_pk_fma_f32 v[126:127], v[126:127], v[220:221], v[242:243] op_sel_hi:[1,0,1]\n"
    "v_pk_mul_f32 v[236:237], v[108:109], v[108:109]\n"
    "v_pk_mul_f32 v[238:239], v[110:111], v[110:111]\n"
    "v_pk_mul_f32 v[240:241], v[124:125], v[124:125]\n"
    "v_pk_mul_f32 v[242:243], v[126:127], v[126:127]\n"
    "s_add_u32 m0, s78, 101376\n"
    "s_add_u32 s56, s69, 73728\n"
    "buffer_load_dwordx4 v251, s[24:27], s56 offen offset:0 lds\n"
    "v_pk_fma_f32 v[236:237], v[236:237], s[8:9], s[8:9] op_sel:[0,0,1] op_sel_hi:[1,0,1]\n"
    "v_pk_fma_f32 v[238:239], v[238:239], s[8:9], s[8:9] op_sel:[0,0,1] op_sel_hi:[1,0,1]\n"
    "v_pk_fma_f32 v[240:241], v[240:241], s[8:9], s[8:9] op_sel:[0,0,1] op_sel_hi:[1,0,1]\n"
    "v_pk_fma_f32 v[242:243], v[242:243], s[8:9], s[8:9] op_sel:[0,0,1] op_sel_hi:[1,0,1]\n"
    "v_pk_mul_f32 v[236:237], v[108:109], v[236:237]\n"
    "v_pk_mul_f32 v[238:239], v[110:111], v[238:239]\n"
    "v_pk_mul_f32 v[240:241], v[124:125], v[240:241]\n"
    "v_pk_mul_f32 v[242:243], v[126:127], v[242:243]\n"
    "v_exp_f32 v236, v236\n"
    "v_exp_f32 v238, v238\n"
    "v_exp_f32 v240, v240\n"
    "buffer_load_dwordx4 v252, s[24:27], s56 offen offset:1024 lds\n"
    "v_exp_f32 v242, v242\n"
    "v_exp_f32 v237, v237\n"
    "v_exp_f32 v239, v239\n"
    "v_exp_f32 v241, v241\n"
    "v_exp_f32 v243, v243\n"
    "v_pk_fma_f32 v[236:237], v[236:237], v[222:223], v[222:223] op_sel_hi:[1,0,0]\n"
    "v_pk_fma_f32 v[238:239], v[238:239], v[222:223], v[222:223] op_sel_hi:[1,0,0]\n"
    "v_pk_fma_f32 v[240:241], v[240:241], v[222:223], v[222:223] op_sel_hi:[1,0,0]\n"
    "v_pk_fma_f32 v[242:243], v[242:243], v[222:223], v[222:223] op_sel_hi:[1,0,0]\n"
    "v_rcp_f32 v236, v236\n"
    "s_waitcnt vmcnt(11)\n"
    "buffer_store_dword v250, v249, s[20:23], s76 offen offset:0 sc1\n"
    "v_rcp_f32 v238, v238\n"
    "v_rcp_f32 v240, v240\n"
    "v_rcp_f32 v242, v242\n"
    "v_rcp_f32 v237, v237\n"
    "v_rcp_f32 v239, v239\n"
    "v_rcp_f32 v241, v241\n"
    "v_rcp_f32 v243, v243\n"
    "v_pk_mul_f32 v[108:109], v[108:109], v[236:237]\n"
    "v_pk_mul_f32 v[110:111], v[110:111], v[238:239]\n"
    "v_pk_mul_f32 v[124:125], v[124:125], v[240:241]\n"
    "v_pk_mul_f32 v[126:127], v[126:127], v[242:243]\n"
    "v_cvt_pk_bf16_f32 v172, v108, v109\n"
    "v_cvt_pk_bf16_f32 v173, v110, v111\n"
    "v_cvt_pk_bf16_f32 v174, v124, v125\n"
    "v_cvt_pk_bf16_f32 v175, v126, v127\n"
    "buffer_store_dwordx4 v[172:175], v254, s[20:23], s81 offen offset:3072 sc1\n"
    "s_waitcnt lgkmcnt(2)\n"
    "v_pk_fma_f32 v[236:237], v[112:113], v[208:209], v[116:117] op_sel:[0,1,0] op_sel_hi:[1,1,1]\n"
    "v_pk_fma_f32 v[238:239], v[114:115], v[208:209], v[118:119] op_sel:[0,1,0] op_sel_hi:[1,1,1]\n"
    "s_add_u32 s81, s74, 8192\n"
    "s_waitcnt lgkmcnt(0)\n"
    "v_pk_fma_f32 v[240:241], v[120:121], v[208:209], v[96:97] op_sel:[0,1,0] op_sel_hi:[1,1,1]\n"
    "v_pk_fma_f32 v[242:243], v[122:123], v[208:209], v[98:99] op_sel:[0,1,0] op_sel_hi:[1,1,1]\n"
    "v_pk_fma_f32 v[64:65], v[64:65], v[208:209], v[236:237] op_sel_hi:[1,0,1]\n"
    "v_pk_fma_f32 v[66:67], v[66:67], v[208:209], v[238:239] op_sel_hi:[1,0,1]\n"
    "v_pk_fma_f32 v[80:81], v[80:81], v[208:209], v[240:241] op_sel_hi:[1,0,1]\n"
    "v_pk_fma_f32 v[82:83], v[82:83], v[208:209], v[242:243] op_sel_hi:[1,0,1]\n"
    "v_pk_mul_f32 v[236:237], v[64:65], v[64:65]\n"
    "v_pk_mul_f32 v[238:239], v[66:67], v[66:67]\n"
    "v_pk_mul_f32 v[240:241], v[80:81], v[80:81]\n"
    "v_pk_mul_f32 v[242:243], v[82:83], v[82:83]\n"
    "v_pk_fma_f32 v[236:237], v[236:237], s[8:9], s[8:9] op_sel:[0,0,1] op_sel_hi:[1,0,1]\n"
    "v_pk_fma_f32 v[238:239], v[238:239], s[8:9], s[8:9] op_sel:[0,0,1] op_sel_hi:[1,0,1]\n"
    "v_pk_fma_f32 v[240:241], v[240:241], s[8:9], s[8:9] op_sel:[0,0,1] op_sel_hi:[1,0,1]\n"
    "v_pk_fma_f32 v[242:243], v[242:243], s[8:9], s[8:9] op_sel:[0,0,1] op_sel_hi:[1,0,1]\n"
    "s_add_u32 m0, s79, 101376\n"
    "s_nop 0\n"
    "buffer_load_dwordx4 v253, s[24:27], s56 offen offset:0 lds\n"
    "v_pk_mul_f32 v[236:237], v[64:65], v[236:237]\n"
    "v_pk_mul_f32 v[238:239], v[66:67], v[238:239]\n"
    "v_pk_mul_f32 v[240:241], v[80:81], v[240:241]\n"
    "v_pk_mul_f32 v[242:243], v[82:83], v[242:243]\n"
    "v_exp_f32 v236, v236\n"
    "v_exp_f32 v238, v238\n"
    "v_exp_f32 v240, v240\n"
    "v_exp_f32 v242, v242\n"
    "v_exp_f32 v237, v237\n"
    "v_exp_f32 v239, v239\n"
    "v_exp_f32 v241, v241\n"
    "v_exp_f32 v243, v243\n"
    "v_pk_fma_f32 v[236:237], v[236:237], v[210:211], v[210:211] op_sel_hi:[1,0,0]\n"
    "v_pk_fma_f32 v[238:239], v[238:239], v[210:211], v[210:211] op_sel_hi:[1,0,0]\n"
    "s_add_u32 m0, s78, 110592\n"
    "s_add_u32 s56, s56, 36864\n"
    "buffer_load_dwordx4 v251, s[24:27], s56 offen offset:0 lds\n"
    "v_pk_fma_f32 v[240:241], v[240:241], v[210:211], v[210:211] op_sel_hi:[1,0,0]\n"
    "v_pk_fma_f32 v[242:243], v[242:243], v[210:211], v[210:211] op_sel_hi:[1,0,0]\n"
    "v_rcp_f32 v236, v236\n"
    "v_rcp_f32 v238, v238\n"
    "v_rcp_f32 v240, v240\n"
    "v_rcp_f32 v242, v242\n"
    "v_rcp_f32 v237, v237\n"
    "v_rcp_f32 v239, v239\n"
    "v_rcp_f32 v241, v241\n"
    "v_rcp_f32 v243, v243\n"
    "v_pk_mul_f32 v[64:65], v[64:65], v[236:237]\n"
    "v_pk_mul_f32 v[66:67], v[66:67], v[238:239]\n"
    "v_pk_mul_f32 v[80:81], v[80:81], v[240:241]\n"
    "v_pk_mul_f32 v[82:83], v[82:83], v[242:243]\n"
    "v_cvt_pk_bf16_f32 v144, v64, v65\n"
    "v_cvt_pk_bf16_f32 v145, v66, v67\n"
    "v_cvt_pk_bf16_f32 v146, v80, v81\n"
    "v_cvt_pk_bf16_f32 v147, v82, v83\n"
    "buffer_store_dwordx4 v[144:147], v254, s[20:23], s81 offen offset:0 sc1\n"
    "v_pk_fma_f32 v[236:237], v[112:113], v[212:213], v[116:117] op_sel:[0,1,0] op_sel_hi:[1,1,1]\n"
    "v_pk_fma_f32 v[238:239], v[114:115], v[212:213], v[118:119] op_sel:[0,1,0] op_sel_hi:[1,1,1]\n"
    "buffer_load_dwordx4 v252, s[24:27], s56 offen offset:1024 lds\n"
    "v_pk_fma_f32 v[240:241], v[120:121], v[212:213], v[96:97] op_sel:[0,1,0] op_sel_hi:[1,1,1]\n"
    "v_pk_fma_f32 v[242:243], v[122:123], v[212:213], v[98:99] op_sel:[0,1,0] op_sel_hi:[1,1,1]\n"
    "v_pk_fma_f32 v[68:69], v[68:69], v[212:213], v[236:237] op_sel_hi:[1,0,1]\n"
    "v_pk_fma_f32 v[70:71], v[70:71], v[212:213], v[238:239] op_sel_hi:[1,0,1]\n"
    "v_pk_fma_f32 v[84:85], v[84:85], v[212:213], v[240:241] op_sel_hi:[1,0,1]\n"
    "v_pk_fma_f32 v[86:87], v[86:87], v[212:213], v[242:243] op_sel_hi:[1,0,1]\n"
    "v_pk_mul_f32 v[236:237], v[68:69], v[68:69]\n"
    "v_pk_mul_f32 v[238:239], v[70:71], v[70:71]\n"
    "v_pk_mul_f32 v[240:241], v[84:85], v[84:85]\n"
    "v_pk_mul_f32 v[242:243], v[86:87], v[86:87]\n"
    "v_pk_fma_f32 v[236:237], v[236:237], s[8:9], s[8:9] op_sel:[0,0,1] op_sel_hi:[1,0,1]\n"
    "v_pk_fma_f32 v[238:239], v[238:239], s[8:9], s[8:9] op_sel:[0,0,1] op_sel_hi:[1,0,1]\n"
    "v_pk_fma_f32 v[240:241], v[240:241], s[8:9], s[8:9] op_sel:[0,0,1] op_sel_hi:[1,0,1]\n"
    "v_pk_fma_f32 v[242:243], v[242:243], s[8:9], s[8:9] op_sel:[0,0,1] op_sel_hi:[1,0,1]\n"
    "v_pk_mul_f32 v[236:237], v[68:69], v[236:237]\n"
    "v_pk_mul_f32 v[238:239], v[70:71], v[238:239]\n"
    "v_pk_mul_f32 v[240:241], v[84:85], v[240:241]\n"
    "v_pk_mul_f32 v[242:243], v[86:87], v[242:243]\n"
    "v_exp_f32 v236, v236\n"
    "v_exp_f32 v238, v238\n"
    "v_exp_f32 v240, v240\n"
    "s_add_u32 m0, s78, 119808\n"
    "s_add_u32 s56, s70, 73728\n"
    "buffer_load_dwordx4 v251, s[24:27], s56 offen offset:0 lds\n"
    "v_exp_f32 v242, v242\n"
    "v_exp_f32 v237, v237\n"
    "v_exp_f32 v239, v239\n"
    "v_exp_f32 v241, v241\n"
    "v_exp_f32 v243, v243\n"
    "v_pk_fma_f32 v[236:237], v[236:237], v[214:215], v[214:215] op_sel_hi:[1,0,0]\n"
    "v_pk_fma_f32 v[238:239], v[238:239], v[214:215], v[214:215] op_sel_hi:[1,0,0]\n"
    "v_pk_fma_f32 v[240:241], v[240:241], v[214:215], v[214:215] op_sel_hi:[1,0,0]\n"
    "v_pk_fma_f32 v[242:243], v[242:243], v[214:215], v[214:215] op_sel_hi:[1,0,0]\n"
    "v_rcp_f32 v236, v236\n"
    "v_rcp_f32 v238, v238\n"
    "v_rcp_f32 v240, v240\n"
    "v_rcp_f32 v242, v242\n"
    "v_rcp_f32 v237, v237\n"
    "v_rcp_f32 v239, v239\n"
    "v_rcp_f32 v241, v241\n"
    "v_rcp_f32 v243, v243\n"
    "v_pk_mul_f32 v[68:69], v[68:69], v[236:237]\n"
    "v_pk_mul_f32 v[70:71], v[70:71], v[238:239]\n"
    "v_pk_mul_f32 v[84:85], v[84:85], v[240:241]\n"
    "v_pk_mul_f32 v[86:87], v[86:87], v[242:243]\n"
    "v_cvt_pk_bf16_f32 v148, v68, v69\n"
    "v_cvt_pk_bf16_f32 v149, v70, v71\n"
    "v_cvt_pk_bf16_f32 v150, v84, v85\n"
    "v_cvt_pk_bf16_f32 v151, v86, v87\n"
    "buffer_store_dwordx4 v[148:151], v254, s[20:23], s81 offen offset:1024 sc1\n"
    "v_pk_fma_f32 v[236:237], v[112:113], v[216:217], v[116:117] op_sel:[0,1,0] op_sel_hi:[1,1,1]\n"
    "v_pk_fma_f32 v[238:239], v[114:115], v[216:217], v[118:119] op_sel:[0,1,0] op_sel_hi:[1,1,1]\n"
    "buffer_load_dwordx4 v252, s[24:27], s56 offen offset:1024 lds\n"
    "v_pk_fma_f32 v[240:241], v[120:121], v[216:217], v[96:97] op_sel:[0,1,0] op_sel_hi:[1,1,1]\n"
    "v_pk_fma_f32 v[242:243], v[122:123], v[216:217], v[98:99] op_sel:[0,1,0] op_sel_hi:[1,1,1]\n"
    "v_pk_fma_f32 v[72:73], v[72:73], v[216:217], v[236:237] op_sel_hi:[1,0,1]\n"
    "v_pk_fma_f32 v[74:75], v[74:75], v[216:217], v[238:239] op_sel_hi:[1,0,1]\n"
    "v_pk_fma_f32 v[88:89], v[88:89], v[216:217], v[240:241] op_sel_hi:[1,0,1]\n"
    "v_pk_fma_f32 v[90:91], v[90:91], v[216:217], v[242:243] op_sel_hi:[1,0,1]\n"
    "v_pk_mul_f32 v[236:237], v[72:73], v[72:73]\n"
    "v_pk_mul_f32 v[238:239], v[74:75], v[74:75]\n"
    "v_pk_mul_f32 v[240:241], v[88:89], v[88:89]\n"
    "v_pk_mul_f32 v[242:243], v[90:91], v[90:91]\n"
    "v_pk_fma_f32 v[236:237], v[236:237], s[8:9], s[8:9] op_sel:[0,0,1] op_sel_hi:[1,0,1]\n"
    "v_pk_fma_f32 v[238:239], v[238:239], s[8:9], s[8:9] op_sel:[0,0,1] op_sel_hi:[1,0,1]\n"
    "v_pk_fma_f32 v[240:241], v[240:241], s[8:9], s[8:9] op_sel:[0,0,1] op_sel_hi:[1,0,1]\n"
    "v_pk_fma_f32 v[242:243], v[242:243], s[8:9], s[8:9] op_sel:[0,0,1] op_sel_hi:[1,0,1]\n"
    "s_add_u32 m0, s79, 119808\n"
    "s_nop 0\n"
    "buffer_load_dwordx4 v253, s[24:27], s56 offen offset:0 lds\n"
    "v_pk_mul_f32 v[236:237], v[72:73], v[236:237]\n"
    "v_pk_mul_f32 v[238:239], v[74:75], v[238:239]\n"
    "v_pk_mul_f32 v[240:241], v[88:89], v[240:241]\n"
    "v_pk_mul_f32 v[242:243], v[90:91], v[242:243]\n"
    "v_exp_f32 v236, v236\n"
    "v_exp_f32 v238, v238\n"
    "v_exp_f32 v240, v240\n"
    "v_exp_f32 v242, v242\n"
    "v_exp_f32 v237, v237\n"
    "v_exp_f32 v239, v239\n"
    "v_exp_f32 v241, v241\n"
    "v_exp_f32 v243, v243\n"
    "v_pk_fma_f32 v[236:237], v[236:237], v[218:219], v[218:219] op_sel_hi:[1,0,0]\n"
    "v_pk_fma_f32 v[238:239], v[238:239], v[218:219], v[218:219] op_sel_hi:[1,0,0]\n"
    "v_lshrrev_b32 v224, 2, v255\n"
    "v_and_b32 v225, 3, v255\n"
    "v_mul_u32_u24 v226, 288, v224\n"
    "v_lshl_add_u32 v244, v225, 3, v226\n"
    "v_add_u32 v245, 55296, v244\n"
    "v_add_u32 v246, 110592, v244\n"
    "v_min_u32 v247, 2, v225\n"
    "v_lshlrev_b32 v247, 15, v247\n"
    "buffer_load_dword v248, v247, s[20:23], s77 offen offset:0 sc1\n"
    "v_pk_fma_f32 v[240:241], v[240:241], v[218:219], v[218:219] op_sel_hi:[1,0,0]\n"
    "v_pk_fma_f32 v[242:243], v[242:243], v[218:219], v[218:219] op_sel_hi:[1,0,0]\n"
    "v_rcp_f32 v236, v236\n"
    "v_rcp_f32 v238, v238\n"
    "v_rcp_f32 v240, v240\n"
    "v_rcp_f32 v242, v242\n"
    "v_rcp_f32 v237, v237\n"
    "v_rcp_f32 v239, v239\n"
    "v_rcp_f32 v241, v241\n"
    "v_rcp_f32 v243, v243\n"
    "v_pk_mul_f32 v[72:73], v[72:73], v[236:237]\n"
    "v_pk_mul_f32 v[74:75], v[74:75], v[238:239]\n"
    "v_pk_mul_f32 v[88:89], v[88:89], v[240:241]\n"
    "v_pk_mul_f32 v[90:91], v[90:91], v[242:243]\n"
    "v_cvt_pk_bf16_f32 v152, v72, v73\n"
    "v_cvt_pk_bf16_f32 v153, v74, v75\n"
    "v_cvt_pk_bf16_f32 v154, v88, v89\n"
    "v_cvt_pk_bf16_f32 v155, v90, v91\n"
    "buffer_store_dwordx4 v[152:155], v254, s[20:23], s81 offen offset:2048 sc1\n"
    "v_pk_fma_f32 v[236:237], v[112:113], v[220:221], v[116:117] op_sel:[0,1,0] op_sel_hi:[1,1,1]\n"
    "v_pk_fma_f32 v[238:239], v[114:115], v[220:221], v[118:119] op_sel:[0,1,0] op_sel_hi:[1,1,1]\n"
    "ds_read_b128 v[80:83], v230 offset:128\n"
    "ds_read_b128 v[84:87], v230 offset:896\n"
    "ds_read_b128 v[88:91], v230 offset:192\n"
    "ds_read_b128 v[64:67], v230 offset:960\n"
    "v_pk_fma_f32 v[240:241], v[120:121], v[220:221], v[96:97] op_sel:[0,1,0] op_sel_hi:[1,1,1]\n"
    "v_pk_fma_f32 v[242:243], v[122:123], v[220:221], v[98:99] op_sel:[0,1,0] op_sel_hi:[1,1,1]\n"
    "v_pk_fma_f32 v[76:77], v[76:77], v[220:221], v[236:237] op_sel_hi:[1,0,1]\n"
    "v_pk_fma_f32 v[78:79], v[78:79], v[220:221], v[238:239] op_sel_hi:[1,0,1]\n"
    "v_pk_fma_f32 v[92:93], v[92:93], v[220:221], v[240:241] op_sel_hi:[1,0,1]\n"
    "v_pk_fma_f32 v[94:95], v[94:95], v[220:221], v[242:243] op_sel_hi:[1,0,1]\n"
    "v_pk_mul_f32 v[236:237], v[76:77], v[76:77]\n"
    "v_pk_mul_f32 v[238:239], v[78:79], v[78:79]\n"
    "v_pk_mul_f32 v[240:241], v[92:93], v[92:93]\n"
    "v_pk_mul_f32 v[242:243], v[94:95], v[94:95]\n"
    "v_pk_fma_f32 v[236:237], v[236:237], s[8:9], s[8:9] op_sel:[0,0,1] op_sel_hi:[1,0,1]\n"
    "v_pk_fma_f32 v[238:239], v[238:239], s[8:9], s[8:9] op_sel:[0,0,1] op_sel_hi:[1,0,1]\n"
    "v_pk_fma_f32 v[240:241], v[240:241], s[8:9], s[8:9] op_sel:[0,0,1] op_sel_hi:[1,0,1]\n"
    "v_pk_fma_f32 v[242:243], v[242:243], s[8:9], s[8:9] op_sel:[0,0,1] op_sel_hi:[1,0,1]\n"
    "s_add_u32 m0, s78, 129024\n"
    "s_add_u32 s56, s56, 36864\n"
    "buffer_load_dwordx4 v251, s[24:27], s56 offen offset:0 lds\n"
    "v_pk_mul_f32 v[236:237], v[76:77], v[236:237]\n"
    "v_pk_mul_f32 v[238:239], v[78:79], v[238:239]\n"
    "v_pk_mul_f32 v[240:241], v[92:93], v[240:241]\n"
    "v_pk_mul_f32 v[242:243], v[94:95], v[242:243]\n"
    "v_exp_f32 v236, v236\n"
    "v_exp_f32 v238, v238\n"
    "v_exp_f32 v240, v240\n"
    "v_exp_f32 v242, v242\n"
    "v_exp_f32 v237, v237\n"
    "v_exp_f32 v239, v239\n"
    "v_exp_f32 v241, v241\n"
    "v_exp_f32 v243, v243\n"
    "v_pk_fma_f32 v[236:237], v[236:237], v[222:223], v[222:223] op_sel_hi:[1,0,0]\n"
    "v_pk_fma_f32 v[238:239], v[238:239], v[222:223], v[222:223] op_sel_hi:[1,0,0]\n"
    "buffer_load_dwordx4 v252, s[24:27], s56 offen offset:1024 lds\n"
    "v_pk_fma_f32 v[240:241], v[240:241], v[222:223], v[222:223] op_sel_hi:[1,0,0]\n"
    "v_pk_fma_f32 v[242:243], v[242:243], v[222:223], v[222:223] op_sel_hi:[1,0,0]\n"
    "v_rcp_f32 v236, v236\n"
    "v_rcp_f32 v238, v238\n"
    "v_rcp_f32 v240, v240\n"
    "v_rcp_f32 v242, v242\n"
    "v_rcp_f32 v237, v237\n"
    "v_rcp_f32 v239, v239\n"
    "v_rcp_f32 v241, v241\n"
    "v_rcp_f32 v243, v243\n"
    "v_pk_mul_f32 v[76:77], v[76:77], v[236:237]\n"
    "v_pk_mul_f32 v[78:79], v[78:79], v[238:239]\n"
    "v_pk_mul_f32 v[92:93], v[92:93], v[240:241]\n"
    "v_pk_mul_f32 v[94:95], v[94:95], v[242:243]\n"
    "v_cvt_pk_bf16_f32 v156, v76, v77\n"
    "v_cvt_pk_bf16_f32 v157, v78, v79\n"
    "v_cvt_pk_bf16_f32 v158, v92, v93\n"
    "v_cvt_pk_bf16_f32 v159, v94, v95\n"
    "buffer_store_dwordx4 v[156:159], v254, s[20:23], s81 offen offset:3072 sc1\n"
    "s_waitcnt lgkmcnt(2)\n"
    "v_pk_fma_f32 v[236:237], v[80:81], v[208:209], v[84:85] op_sel:[0,1,0] op_sel_hi:[1,1,1]\n"
    "v_pk_fma_f32 v[238:239], v[82:83], v[208:209], v[86:87] op_sel:[0,1,0] op_sel_hi:[1,1,1]\n"
    "s_add_u32 s81, s74, 20480\n"
    "s_waitcnt lgkmcnt(0)\n"
    "v_pk_fma_f32 v[240:241], v[88:89], v[208:209], v[64:65] op_sel:[0,1,0] op_sel_hi:[1,1,1]\n"
    "v_pk_fma_f32 v[242:243], v[90:91], v[208:209], v[66:67] op_sel:[0,1,0] op_sel_hi:[1,1,1]\n"
    "v_pk_fma_f32 v[32:33], v[32:33], v[208:209], v[236:237] op_sel_hi:[1,0,1]\n"
    "v_pk_fma_f32 v[34:35], v[34:35], v[208:209], v[238:239] op_sel_hi:[1,0,1]\n"
    "v_pk_fma_f32 v[48:49], v[48:49], v[208:209], v[240:241] op_sel_hi:[1,0,1]\n"
    "v_pk_fma_f32 v[50:51], v[50:51], v[208:209], v[242:243] op_sel_hi:[1,0,1]\n"
    "v_pk_mul_f32 v[236:237], v[32:33], v[32:33]\n"
    "v_pk_mul_f32 v[238:239], v[34:35], v[34:35]\n"
    "v_pk_mul_f32 v[240:241], v[48:49], v[48:49]\n"
    "v_pk_mul_f32 v[242:243], v[50:51], v[50:51]\n"
    "v_pk_fma_f32 v[236:237], v[236:237], s[8:9], s[8:9] op_sel:[0,0,1] op_sel_hi:[1,0,1]\n"
    "v_pk_fma_f32 v[238:239], v[238:239], s[8:9], s[8:9] op_sel:[0,0,1] op_sel_hi:[1,0,1]\n"
    "v_pk_fma_f32 v[240:241], v[240:241], s[8:9], s[8:9] op_sel:[0,0,1] op_sel_hi:[1,0,1]\n"
    "v_pk_fma_f32 v[242:243], v[242:243], s[8:9], s[8:9] op_sel:[0,0,1] op_sel_hi:[1,0,1]\n"
    "s_add_u32 m0, s78, 138240\n"
    "s_add_u32 s56, s69, 0\n"
    "buffer_load_dwordx4 v251, s[24:27], s56 offen offset:0 lds\n"
    "v_pk_mul_f32 v[236:237], v[32:33], v[236:237]\n"
    "v_pk_mul_f32 v[238:239], v[34:35], v[238:239]\n"
    "v_pk_mul_f32 v[240:241], v[48:49], v[240:241]\n"
    "v_pk_mul_f32 v[242:243], v[50:51], v[242:243]\n"
    "v_exp_f32 v236, v236\n"
    "v_exp_f32 v238, v238\n"
    "v_exp_f32 v240, v240\n"
    "v_exp_f32 v242, v242\n"
    "v_exp_f32 v237, v237\n"
    "v_exp_f32 v239, v239\n"
    "v_exp_f32 v241, v241\n"
    "v_exp_f32 v243, v243\n"
    "v_pk_fma_f32 v[236:237], v[236:237], v[210:211], v[210:211] op_sel_hi:[1,0,0]\n"
    "v_pk_fma_f32 v[238:239], v[238:239], v[210:211], v[210:211] op_sel_hi:[1,0,0]\n"
    "buffer_load_dwordx4 v252, s[24:27], s56 offen offset:1024 lds\n"
    "v_pk_fma_f32 v[240:241], v[240:241], v[210:211], v[210:211] op_sel_hi:[1,0,0]\n"
    "v_pk_fma_f32 v[242:243], v[242:243], v[210:211], v[210:211] op_sel_hi:[1,0,0]\n"
    "v_rcp_f32 v236, v236\n"
    "v_rcp_f32 v238, v238\n"
    "v_rcp_f32 v240, v240\n"
    "v_rcp_f32 v242, v242\n"
    "v_rcp_f32 v237, v237\n"
    "v_rcp_f32 v239, v239\n"
    "v_rcp_f32 v241, v241\n"
    "v_rcp_f32 v243, v243\n"
    "v_pk_mul_f32 v[32:33], v[32:33], v[236:237]\n"
    "v_pk_mul_f32 v[34:35], v[34:35], v[238:239]\n"
    "v_pk_mul_f32 v[48:49], v[48:49], v[240:241]\n"
    "v_pk_mul_f32 v[50:51], v[50:51], v[242:243]\n"
    "v_cvt_pk_bf16_f32 v192, v32, v33\n"
    "v_cvt_pk_bf16_f32 v193, v34, v35\n"
    "v_cvt_pk_bf16_f32 v194, v48, v49\n"
    "v_cvt_pk_bf16_f32 v195, v50, v51\n"
    "buffer_store_dwordx4 v[192:195], v254, s[20:23], s81 offen offset:0 sc1\n"
    "v_pk_fma_f32 v[236:237], v[80:81], v[212:213], v[84:85] op_sel:[0,1,0] op_sel_hi:[1,1,1]\n"
    "v_pk_fma_f32 v[238:239], v[82:83], v[212:213], v[86:87] op_sel:[0,1,0] op_sel_hi:[1,1,1]\n"
    "s_waitcnt vmcnt(7)\n"
    "v_cmp_ne_u32 vcc, s66, v248\n"
    "s_nop 1\n"
    "s_cbranch_vccnz RT2_%=\n"
    "RB2_%=:\n"
    "v_pk_fma_f32 v[240:241], v[88:89], v[212:213], v[64:65] op_sel:[0,1,0] op_sel_hi:[1,1,1]\n"
    "v_pk_fma_f32 v[242:243], v[90:91], v[212:213], v[66:67] op_sel:[0,1,0] op_sel_hi:[1,1,1]\n"
    "v_pk_fma_f32 v[36:37], v[36:37], v[212:213], v[236:237] op_sel_hi:[1,0,1]\n"
    "v_pk_fma_f32 v[38:39], v[38:39], v[212:213], v[238:239] op_sel_hi:[1,0,1]\n"
    "v_pk_fma_f32 v[52:53], v[52:53], v[212:213], v[240:241] op_sel_hi:[1,0,1]\n"
    "v_pk_fma_f32 v[54:55], v[54:55], v[212:213], v[242:243] op_sel_hi:[1,0,1]\n"
    "v_pk_mul_f32 v[236:237], v[36:37], v[36:37]\n"
    "s_add_u32 m0, s79, 138240\n"
    "s_nop 0\n"
    "buffer_load_dwordx4 v253, s[24:27], s56 offen offset:0 lds\n"
    "v_pk_mul_f32 v[238:239], v[38:39], v[38:39]\n"
    "v_pk_mul_f32 v[240:241], v[52:53], v[52:53]\n"
    "v_pk_mul_f32 v[242:243], v[54:55], v[54:55]\n"
    "v_pk_fma_f32 v[236:237], v[236:237], s[8:9], s[8:9] op_sel:[0,0,1] op_sel_hi:[1,0,1]\n"
    "v_pk_fma_f32 v[238:239], v[238:239], s[8:9], s[8:9] op_sel:[0,0,1] op_sel_hi:[1,0,1]\n"
    "v_pk_fma_f32 v[240:241], v[240:241], s[8:9], s[8:9] op_sel:[0,0,1] op_sel_hi:[1,0,1]\n"
    "v_pk_fma_f32 v[242:243], v[242:243], s[8:9], s[8:9] op_sel:[0,0,1] op_sel_hi:[1,0,1]\n"
    "s_add_u32 m0, s78, 147456\n"
    "s_add_u32 s56, s56, 36864\n"
    "buffer_load_dwordx4 v251, s[24:27], s56 offen offset:0 lds\n"
    "v_pk_mul_f32 v[236:237], v[36:37], v[236:237]\n"
    "v_pk_mul_f32 v[238:239], v[38:39], v[238:239]\n"
    "v_pk_mul_f32 v[240:241], v[52:53], v[240:241]\n"
    "v_pk_mul_f32 v[242:243], v[54:55], v[242:243]\n"
    "v_exp_f32 v236, v236\n"
    "v_exp_f32 v238, v238\n"
    "v_exp_f32 v240, v240\n"
    "s_add_u32 s57, s71, 0\n"
    "buffer_load_dwordx4 a[0:3], v254, s[20:23], s57 offen offset:0 sc1\n"
    "v_exp_f32 v242, v242\n"
    "v_exp_f32 v237, v237\n"
    "v_exp_f32 v239, v239\n"
    "v_exp_f32 v241, v241\n"
    "v_exp_f32 v243, v243\n"
    "v_pk_fma_f32 v[236:237], v[236:237], v[214:215], v[214:215] op_sel_hi:[1,0,0]\n"
    "v_pk_fma_f32 v[238:239], v[238:239], v[214:215], v[214:215] op_sel_hi:[1,0,0]\n"
    "buffer_load_dwordx4 a[4:7], v254, s[20:23], s57 offen offset:1024 sc1\n"
    "v_pk_fma_f32 v[240:241], v[240:241], v[214:215], v[214:215] op_sel_hi:[1,0,0]\n"
    "v_pk_fma_f32 v[242:243], v[242:243], v[214:215], v[214:215] op_sel_hi:[1,0,0]\n"
    "v_rcp_f32 v236, v236\n"
    "v_rcp_f32 v238, v238\n"
    "v_rcp_f32 v240, v240\n"
    "v_rcp_f32 v242, v242\n"
    "v_rcp_f32 v237, v237\n"
    "buffer_load_dwordx4 a[8:11], v254, s[20:23], s57 offen offset:2048 sc1\n"
    "v_rcp_f32 v239, v239\n"
    "v_rcp_f32 v241, v241\n"
    "v_rcp_f32 v243, v243\n"
    "v_pk_mul_f32 v[36:37], v[36:37], v[236:237]\n"
    "v_pk_mul_f32 v[38:39], v[38:39], v[238:239]\n"
    "v_pk_mul_f32 v[52:53], v[52:53], v[240:241]\n"
    "v_pk_mul_f32 v[54:55], v[54:55], v[242:243]\n"
    "v_cvt_pk_bf16_f32 v196, v36, v37\n"
    "v_cvt_pk_bf16_f32 v197, v38, v39\n"
    "v_cvt_pk_bf16_f32 v198, v52, v53\n"
    "v_cvt_pk_bf16_f32 v199, v54, v55\n"
    "buffer_store_dwordx4 v[196:199], v254, s[20:23], s81 offen offset:1024 sc1\n"
    "v_pk_fma_f32 v[236:237], v[80:81], v[216:217], v[84:85] op_sel:[0,1,0] op_sel_hi:[1,1,1]\n"
    "v_pk_fma_f32 v[238:239], v[82:83], v[216:217], v[86:87] op_sel:[0,1,0] op_sel_hi:[1,1,1]\n"
    "buffer_load_dwordx4 v252, s[24:27], s56 offen offset:1024 lds\n"
    "v_pk_fma_f32 v[240:241], v[88:89], v[216:217], v[64:65] op_sel:[0,1,0] op_sel_hi:[1,1,1]\n"
    "v_pk_fma_f32 v[242:243], v[90:91], v[216:217], v[66:67] op_sel:[0,1,0] op_sel_hi:[1,1,1]\n"
    "v_pk_fma_f32 v[40:41], v[40:41], v[216:217], v[236:237] op_sel_hi:[1,0,1]\n"
    "v_pk_fma_f32 v[42:43], v[42:43], v[216:217], v[238:239] op_sel_hi:[1,0,1]\n"
    "v_pk_fma_f32 v[56:57], v[56:57], v[216:217], v[240:241] op_sel_hi:[1,0,1]\n"
    "v_pk_fma_f32 v[58:59], v[58:59], v[216:217], v[242:243] op_sel_hi:[1,0,1]\n"
    "v_pk_mul_f32 v[236:237], v[40:41], v[40:41]\n"
    "v_pk_mul_f32 v[238:239], v[42:43], v[42:43]\n"
    "v_pk_mul_f32 v[240:241], v[56:57], v[56:57]\n"
    "v_pk_mul_f32 v[242:243], v[58:59], v[58:59]\n"
    "buffer_load_dwordx4 a[12:15], v254, s[20:23], s57 offen offset:3072 sc1\n"
    "v_pk_fma_f32 v[236:237], v[236:237], s[8:9], s[8:9] op_sel:[0,0,1] op_sel_hi:[1,0,1]\n"
    "v_pk_fma_f32 v[238:239], v[238:239], s[8:9], s[8:9] op_sel:[0,0,1] op_sel_hi:[1,0,1]\n"
    "v_pk_fma_f32 v[240:241], v[240:241], s[8:9], s[8:9] op_sel:[0,0,1] op_sel_hi:[1,0,1]\n"
    "v_pk_fma_f32 v[242:243], v[242:243], s[8:9], s[8:9] op_sel:[0,0,1] op_sel_hi:[1,0,1]\n"
    "v_pk_mul_f32 v[236:237], v[40:41], v[236:237]\n"
    "v_pk_mul_f32 v[238:239], v[42:43], v[238:239]\n"
    "v_pk_mul_f32 v[240:241], v[56:57], v[240:241]\n"
    "v_pk_mul_f32 v[242:243], v[58:59], v[242:243]\n"
    "v_exp_f32 v236, v236\n"
    "v_exp_f32 v238, v238\n"
    "v_exp_f32 v240, v240\n"
    "s_add_u32 s57, s71, 4096\n"
    "buffer_load_dwordx4 a[16:19], v254, s[20:23], s57 offen offset:0 sc1\n"
    "v_exp_f32 v242, v242\n"
    "v_exp_f32 v237, v237\n"
    "v_exp_f32 v239, v239\n"
    "v_exp_f32 v241, v241\n"
    "v_exp_f32 v243, v243\n"
    "v_pk_fma_f32 v[236:237], v[236:237], v[218:219], v[218:219] op_sel_hi:[1,0,0]\n"
    "v_pk_fma_f32 v[238:239], v[238:239], v[218:219], v[218:219] op_sel_hi:[1,0,0]\n"
    "v_pk_fma_f32 v[240:241], v[240:241], v[218:219], v[218:219] op_sel_hi:[1,0,0]\n"
    "v_pk_fma_f32 v[242:243], v[242:243], v[218:219], v[218:219] op_sel_hi:[1,0,0]\n"
    "v_rcp_f32 v236, v236\n"
    "buffer_load_dwordx4 a[20:23], v254, s[20:23], s57 offen offset:1024 sc1\n"
    "v_rcp_f32 v238, v238\n"
    "v_rcp_f32 v240, v240\n"
    "v_rcp_f32 v242, v242\n"
    "v_rcp_f32 v237, v237\n"
    "v_rcp_f32 v239, v239\n"
    "v_rcp_f32 v241, v241\n"
    "v_rcp_f32 v243, v243\n"
    "v_pk_mul_f32 v[40:41], v[40:41], v[236:237]\n"
    "v_pk_mul_f32 v[42:43], v[42:43], v[238:239]\n"
    "v_pk_mul_f32 v[56:57], v[56:57], v[240:241]\n"
    "v_pk_mul_f32 v[58:59], v[58:59], v[242:243]\n"
    "v_cvt_pk_bf16_f32 v200, v40, v41\n"
    "v_cvt_pk_bf16_f32 v201, v42, v43\n"
    "v_cvt_pk_bf16_f32 v202, v56, v57\n"
    "v_cvt_pk_bf16_f32 v203, v58, v59\n"
    "buffer_store_dwordx4 v[200:203], v254, s[20:23], s81 offen offset:2048 sc1\n"
    "v_pk_fma_f32 v[236:237], v[80:81], v[220:221], v[84:85] op_sel:[0,1,0] op_sel_hi:[1,1,1]\n"
    "v_pk_fma_f32 v[238:239], v[82:83], v[220:221], v[86:87] op_sel:[0,1,0] op_sel_hi:[1,1,1]\n"
    "ds_read_b128 v[48:51], v230 offset:0\n"
    "ds_read_b128 v[52:55], v230 offset:768\n"
    "ds_read_b128 v[56:59], v230 offset:64\n"
    "ds_read_b128 v[32:35], v230 offset:832\n"
    "v_pk_fma_f32 v[240:241], v[88:89], v[220:221], v[64:65] op_sel:[0,1,0] op_sel_hi:[1,1,1]\n"
    "v_pk_fma_f32 v[242:243], v[90:91], v[220:221], v[66:67] op_sel:[0,1,0] op_sel_hi:[1,1,1]\n"
    "v_pk_fma_f32 v[44:45], v[44:45], v[220:221], v[236:237] op_sel_hi:[1,0,1]\n"
    "v_pk_fma_f32 v[46:47], v[46:47], v[220:221], v[238:239] op_sel_hi:[1,0,1]\n"
    "v_pk_fma_f32 v[60:61], v[60:61], v[220:221], v[240:241] op_sel_hi:[1,0,1]\n"
    "v_pk_fma_f32 v[62:63], v[62:63], v[220:221], v[242:243] op_sel_hi:[1,0,1]\n"
    "v_pk_mul_f32 v[236:237], v[44:45], v[44:45]\n"
    "v_pk_mul_f32 v[238:239], v[46:47], v[46:47]\n"
    "s_waitcnt vmcnt(14)\n"
    "buffer_store_dword v250, v249, s[20:23], s76 offen offset:256 sc1\n"
    "v_pk_mul_f32 v[240:241], v[60:61], v[60:61]\n"
    "v_pk_mul_f32 v[242:243], v[62:63], v[62:63]\n"
    "v_pk_fma_f32 v[236:237], v[236:237], s[8:9], s[8:9] op_sel:[0,0,1] op_sel_hi:[1,0,1]\n"
    "v_pk_fma_f32 v[238:239], v[238:239], s[8:9], s[8:9] op_sel:[0,0,1] op_sel_hi:[1,0,1]\n"
    "v_pk_fma_f32 v[240:241], v[240:241], s[8:9], s[8:9] op_sel:[0,0,1] op_sel_hi:[1,0,1]\n"
    "v_pk_fma_f32 v[242:243], v[242:243], s[8:9], s[8:9] op_sel:[0,0,1] op_sel_hi:[1,0,1]\n"
    "v_pk_mul_f32 v[236:237], v[44:45], v[236:237]\n"
    "v_pk_mul_f32 v[238:239], v[46:47], v[238:239]\n"
    "buffer_load_dwordx4 a[24:27], v254, s[20:23], s57 offen offset:2048 sc1\n"
    "v_pk_mul_f32 v[240:241], v[60:61], v[240:241]\n"
    "v_pk_mul_f32 v[242:243], v[62:63], v[242:243]\n"
    "v_exp_f32 v236, v236\n"
    "v_exp_f32 v238, v238\n"
    "v_exp_f32 v240, v240\n"
    "v_exp_f32 v242, v242\n"
    "v_exp_f32 v237, v237\n"
    "v_exp_f32 v239, v239\n"
    "v_exp_f32 v241, v241\n"
    "buffer_load_dwordx4 a[28:31], v254, s[20:23], s57 offen offset:3072 sc1\n"
    "v_exp_f32 v243, v243\n"
    "v_pk_fma_f32 v[236:237], v[236:237], v[222:223], v[222:223] op_sel_hi:[1,0,0]\n"
    "v_pk_fma_f32 v[238:239], v[238:239], v[222:223], v[222:223] op_sel_hi:[1,0,0]\n"
    "v_pk_fma_f32 v[240:241], v[240:241], v[222:223], v[222:223] op_sel_hi:[1,0,0]\n"
    "v_pk_fma_f32 v[242:243], v[242:243], v[222:223], v[222:223] op_sel_hi:[1,0,0]\n"
    "v_rcp_f32 v236, v236\n"
    "v_rcp_f32 v238, v238\n"
    "v_rcp_f32 v240, v240\n"
    "s_add_u32 s57, s72, 0\n"
    "buffer_load_dwordx4 a[32:35], v254, s[20:23], s57 offen offset:0 sc1\n"
    "v_rcp_f32 v242, v242\n"
    "v_rcp_f32 v237, v237\n"
    "v_rcp_f32 v239, v239\n"
    "v_rcp_f32 v241, v241\n"
    "v_rcp_f32 v243, v243\n"
    "v_pk_mul_f32 v[44:45], v[44:45], v[236:237]\n"
    "v_pk_mul_f32 v[46:47], v[46:47], v[238:239]\n"
    "v_pk_mul_f32 v[60:61], v[60:61], v[240:241]\n"
    "v_pk_mul_f32 v[62:63], v[62:63], v[242:243]\n"
    "v_cvt_pk_bf16_f32 v204, v44, v45\n"
    "v_cvt_pk_bf16_f32 v205, v46, v47\n"
    "v_cvt_pk_bf16_f32 v206, v60, v61\n"
    "v_cvt_pk_bf16_f32 v207, v62, v63\n"
    "buffer_store_dwordx4 v[204:207], v254, s[20:23], s81 offen offset:3072 sc1\n"
    "s_waitcnt lgkmcnt(2)\n"
    "v_pk_fma_f32 v[236:237], v[48:49], v[208:209], v[52:53] op_sel:[0,1,0] op_sel_hi:[1,1,1]\n"
    "v_pk_fma_f32 v[238:239], v[50:51], v[208:209], v[54:55] op_sel:[0,1,0] op_sel_hi:[1,1,1]\n"
    "s_add_u32 s81, s74, 16384\n"
    "s_waitcnt lgkmcnt(0)\n"
    "v_pk_fma_f32 v[240:241], v[56:57], v[208:209], v[32:33] op_sel:[0,1,0] op_sel_hi:[1,1,1]\n"
    "v_pk_fma_f32 v[242:243], v[58:59], v[208:209], v[34:35] op_sel:[0,1,0] op_sel_hi:[1,1,1]\n"
    "v_pk_fma_f32 v[0:1], v[0:1], v[208:209], v[236:237] op_sel_hi:[1,0,1]\n"
    "v_pk_fma_f32 v[2:3], v[2:3], v[208:209], v[238:239] op_sel_hi:[1,0,1]\n"
    "v_pk_fma_f32 v[16:17], v[16:17], v[208:209], v[240:241] op_sel_hi:[1,0,1]\n"
    "v_pk_fma_f32 v[18:19], v[18:19], v[208:209], v[242:243] op_sel_hi:[1,0,1]\n"
    "v_pk_mul_f32 v[236:237], v[0:1], v[0:1]\n"
    "v_pk_mul_f32 v[238:239], v[2:3], v[2:3]\n"
    "v_pk_mul_f32 v[240:241], v[16:17], v[16:17]\n"
    "v_pk_mul_f32 v[242:243], v[18:19], v[18:19]\n"
    "buffer_load_dwordx4 a[36:39], v254, s[20:23], s57 offen offset:1024 sc1\n"
    "v_pk_fma_f32 v[236:237], v[236:237], s[8:9], s[8:9] op_sel:[0,0,1] op_sel_hi:[1,0,1]\n"
    "v_pk_fma_f32 v[238:239], v[238:239], s[8:9], s[8:9] op_sel:[0,0,1] op_sel_hi:[1,0,1]\n"
    "v_pk_fma_f32 v[240:241], v[240:241], s[8:9], s[8:9] op_sel:[0,0,1] op_sel_hi:[1,0,1]\n"
    "v_pk_fma_f32 v[242:243], v[242:243], s[8:9], s[8:9] op_sel:[0,0,1] op_sel_hi:[1,0,1]\n"
    "v_pk_mul_f32 v[236:237], v[0:1], v[236:237]\n"
    "v_pk_mul_f32 v[238:239], v[2:3], v[238:239]\n"
    "v_pk_mul_f32 v[240:241], v[16:17], v[240:241]\n"
    "v_pk_mul_f32 v[242:243], v[18:19], v[242:243]\n"
    "v_exp_f32 v236, v236\n"
    "v_exp_f32 v238, v238\n"
    "v_exp_f32 v240, v240\n"
    "buffer_load_dwordx4 a[40:43], v254, s[20:23], s57 offen offset:2048 sc1\n"
    "v_exp_f32 v242, v242\n"
    "v_exp_f32 v237, v237\n"
    "v_exp_f32 v239, v239\n"
    "v_exp_f32 v241, v241\n"
    "v_exp_f32 v243, v243\n"
    "v_pk_fma_f32 v[236:237], v[236:237], v[210:211], v[210:211] op_sel_hi:[1,0,0]\n"
    "v_pk_fma_f32 v[238:239], v[238:239], v[210:211], v[210:211] op_sel_hi:[1,0,0]\n"
    "v_pk_fma_f32 v[240:241], v[240:241], v[210:211], v[210:211] op_sel_hi:[1,0,0]\n"
    "v_pk_fma_f32 v[242:243], v[242:243], v[210:211], v[210:211] op_sel_hi:[1,0,0]\n"
    "v_rcp_f32 v236, v236\n"
    "buffer_load_dwordx4 a[44:47], v254, s[20:23], s57 offen offset:3072 sc1\n"
    "v_rcp_f32 v238, v238\n"
    "v_rcp_f32 v240, v240\n"
    "v_rcp_f32 v242, v242\n"
    "v_rcp_f32 v237, v237\n"
    "v_rcp_f32 v239, v239\n"
    "v_rcp_f32 v241, v241\n"
    "v_rcp_f32 v243, v243\n"
    "v_pk_mul_f32 v[0:1], v[0:1], v[236:237]\n"
    "v_pk_mul_f32 v[2:3], v[2:3], v[238:239]\n"
    "v_pk_mul_f32 v[16:17], v[16:17], v[240:241]\n"
    "v_pk_mul_f32 v[18:19], v[18:19], v[242:243]\n"
    "v_cvt_pk_bf16_f32 v176, v0, v1\n"
    "v_cvt_pk_bf16_f32 v177, v2, v3\n"
    "v_cvt_pk_bf16_f32 v178, v16, v17\n"
    "v_cvt_pk_bf16_f32 v179, v18, v19\n"
    "buffer_store_dwordx4 v[176:179], v254, s[20:23], s81 offen offset:0 sc1\n"
    "v_pk_fma_f32 v[236:237], v[48:49], v[212:213], v[52:53] op_sel:[0,1,0] op_sel_hi:[1,1,1]\n"
    "v_pk_fma_f32 v[238:239], v[50:51], v[212:213], v[54:55] op_sel:[0,1,0] op_sel_hi:[1,1,1]\n"
    "s_add_u32 s57, s72, 4096\n"
    "buffer_load_dwordx4 a[48:51], v254, s[20:23], s57 offen offset:0 sc1\n"
    "v_pk_fma_f32 v[240:241], v[56:57], v[212:213], v[32:33] op_sel:[0,1,0] op_sel_hi:[1,1,1]\n"
    "v_pk_fma_f32 v[242:243], v[58:59], v[212:213], v[34:35] op_sel:[0,1,0] op_sel_hi:[1,1,1]\n"
    "v_pk_fma_f32 v[4:5], v[4:5], v[212:213], v[236:237] op_sel_hi:[1,0,1]\n"
    "v_pk_fma_f32 v[6:7], v[6:7], v[212:213], v[238:239] op_sel_hi:[1,0,1]\n"
    "v_pk_fma_f32 v[20:21], v[20:21], v[212:213], v[240:241] op_sel_hi:[1,0,1]\n"
    "v_pk_fma_f32 v[22:23], v[22:23], v[212:213], v[242:243] op_sel_hi:[1,0,1]\n"
    "v_pk_mul_f32 v[236:237], v[4:5], v[4:5]\n"
    "v_pk_mul_f32 v[238:239], v[6:7], v[6:7]\n"
    "v_pk_mul_f32 v[240:241], v[20:21], v[20:21]\n"
    "v_pk_mul_f32 v[242:243], v[22:23], v[22:23]\n"
    "v_pk_fma_f32 v[236:237], v[236:237], s[8:9], s[8:9] op_sel:[0,0,1] op_sel_hi:[1,0,1]\n"
    "v_pk_fma_f32 v[238:239], v[238:239], s[8:9], s[8:9] op_sel:[0,0,1] op_sel_hi:[1,0,1]\n"
    "v_pk_fma_f32 v[240:241], v[240:241], s[8:9], s[8:9] op_sel:[0,0,1] op_sel_hi:[1,0,1]\n"
    "v_pk_fma_f32 v[242:243], v[242:243], s[8:9], s[8:9] op_sel:[0,0,1] op_sel_hi:[1,0,1]\n"
    "buffer_load_dwordx4 a[52:55], v254, s[20:23], s57 offen offset:1024 sc1\n"
    "v_pk_mul_f32 v[236:237], v[4:5], v[236:237]\n"
    "v_pk_mul_f32 v[238:239], v[6:7], v[238:239]\n"
    "v_pk_mul_f32 v[240:241], v[20:21], v[240:241]\n"
    "v_pk_mul_f32 v[242:243], v[22:23], v[242:243]\n"
    "v_exp_f32 v236, v236\n"
    "v_exp_f32 v238, v238\n"
    "v_exp_f32 v240, v240\n"
    "v_exp_f32 v242, v242\n"
    "v_exp_f32 v237, v237\n"
    "v_exp_f32 v239, v239\n"
    "v_exp_f32 v241, v241\n"
    "v_exp_f32 v243, v243\n"
    "v_pk_fma_f32 v[236:237], v[236:237], v[214:215], v[214:215] op_sel_hi:[1,0,0]\n"
    "v_pk_fma_f32 v[238:239], v[238:239], v[214:215], v[214:215] op_sel_hi:[1,0,0]\n"
    "buffer_load_dwordx4 a[56:59], v254, s[20:23], s57 offen offset:2048 sc1\n"
    "v_pk_fma_f32 v[240:241], v[240:241], v[214:215], v[214:215] op_sel_hi:[1,0,0]\n"
    "v_pk_fma_f32 v[242:243], v[242:243], v[214:215], v[214:215] op_sel_hi:[1,0,0]\n"
    "v_rcp_f32 v236, v236\n"
    "v_rcp_f32 v238, v238\n"
    "v_rcp_f32 v240, v240\n"
    "v_rcp_f32 v242, v242\n"
    "v_rcp_f32 v237, v237\n"
    "v_rcp_f32 v239, v239\n"
    "v_rcp_f32 v241, v241\n"
    "v_rcp_f32 v243, v243\n"
    "v_pk_mul_f32 v[4:5], v[4:5], v[236:237]\n"
    "v_pk_mul_f32 v[6:7], v[6:7], v[238:239]\n"
    "v_pk_mul_f32 v[20:21], v[20:21], v[240:241]\n"
    "v_pk_mul_f32 v[22:23], v[22:23], v[242:243]\n"
    "v_cvt_pk_bf16_f32 v180, v4, v5\n"
    "v_cvt_pk_bf16_f32 v181, v6, v7\n"
    "v_cvt_pk_bf16_f32 v182, v20, v21\n"
    "v_cvt_pk_bf16_f32 v183, v22, v23\n"
    "buffer_store_dwordx4 v[180:183], v254, s[20:23], s81 offen offset:1024 sc1\n"
    "v_pk_fma_f32 v[236:237], v[48:49], v[216:217], v[52:53] op_sel:[0,1,0] op_sel_hi:[1,1,1]\n"
    "v_pk_fma_f32 v[238:239], v[50:51], v[216:217], v[54:55] op_sel:[0,1,0] op_sel_hi:[1,1,1]\n"
    "buffer_load_dwordx4 a[60:63], v254, s[20:23], s57 offen offset:3072 sc1\n"
    "v_pk_fma_f32 v[240:241], v[56:57], v[216:217], v[32:33] op_sel:[0,1,0] op_sel_hi:[1,1,1]\n"
    "v_pk_fma_f32 v[242:243], v[58:59], v[216:217], v[34:35] op_sel:[0,1,0] op_sel_hi:[1,1,1]\n"
    "v_pk_fma_f32 v[8:9], v[8:9], v[216:217], v[236:237] op_sel_hi:[1,0,1]\n"
    "v_pk_fma_f32 v[10:11], v[10:11], v[216:217], v[238:239] op_sel_hi:[1,0,1]\n"
    "v_pk_fma_f32 v[24:25], v[24:25], v[216:217], v[240:241] op_sel_hi:[1,0,1]\n"
    "v_pk_fma_f32 v[26:27], v[26:27], v[216:217], v[242:243] op_sel_hi:[1,0,1]\n"
    "v_pk_mul_f32 v[236:237], v[8:9], v[8:9]\n"
    "v_pk_mul_f32 v[238:239], v[10:11], v[10:11]\n"
    "v_pk_mul_f32 v[240:241], v[24:25], v[24:25]\n"
    "v_pk_mul_f32 v[242:243], v[26:27], v[26:27]\n"
    "v_pk_fma_f32 v[236:237], v[236:237], s[8:9], s[8:9] op_sel:[0,0,1] op_sel_hi:[1,0,1]\n"
    "v_pk_fma_f32 v[238:239], v[238:239], s[8:9], s[8:9] op_sel:[0,0,1] op_sel_hi:[1,0,1]\n"
    "v_pk_fma_f32 v[240:241], v[240:241], s[8:9], s[8:9] op_sel:[0,0,1] op_sel_hi:[1,0,1]\n"
    "v_pk_fma_f32 v[242:243], v[242:243], s[8:9], s[8:9] op_sel:[0,0,1] op_sel_hi:[1,0,1]\n"
    "s_add_u32 s57, s73, 0\n"
    "buffer_load_dwordx4 a[64:67], v254, s[20:23], s57 offen offset:0 sc1\n"
    "v_pk_mul_f32 v[236:237], v[8:9], v[236:237]\n"
    "v_pk_mul_f32 v[238:239], v[10:11], v[238:239]\n"
    "v_pk_mul_f32 v[240:241], v[24:25], v[240:241]\n"
    "v_pk_mul_f32 v[242:243], v[26:27], v[242:243]\n"
    "v_exp_f32 v236, v236\n"
    "v_exp_f32 v238, v238\n"
    "v_exp_f32 v240, v240\n"
    "v_exp_f32 v242, v242\n"
    "v_exp_f32 v237, v237\n"
    "v_exp_f32 v239, v239\n"
    "v_exp_f32 v241, v241\n"
    "v_exp_f32 v243, v243\n"
    "v_pk_fma_f32 v[236:237], v[236:237], v[218:219], v[218:219] op_sel_hi:[1,0,0]\n"
    "v_pk_fma_f32 v[238:239], v[238:239], v[218:219], v[218:219] op_sel_hi:[1,0,0]\n"
    "buffer_load_dwordx4 a[68:71], v254, s[20:23], s57 offen offset:1024 sc1\n"
    "v_pk_fma_f32 v[240:241], v[240:241], v[218:219], v[218:219] op_sel_hi:[1,0,0]\n"
    "v_pk_fma_f32 v[242:243], v[242:243], v[218:219], v[218:219] op_sel_hi:[1,0,0]\n"
    "v_rcp_f32 v236, v236\n"
    "v_rcp_f32 v238, v238\n"
    "v_rcp_f32 v240, v240\n"
    "v_rcp_f32 v242, v242\n"
    "v_rcp_f32 v237, v237\n"
    "v_rcp_f32 v239, v239\n"
    "v_rcp_f32 v241, v241\n"
    "v_rcp_f32 v243, v243\n"
    "v_pk_mul_f32 v[8:9], v[8:9], v[236:237]\n"
    "v_pk_mul_f32 v[10:11], v[10:11], v[238:239]\n"
    "v_pk_mul_f32 v[24:25], v[24:25], v[240:241]\n"
    "v_pk_mul_f32 v[26:27], v[26:27], v[242:243]\n"
    "v_cvt_pk_bf16_f32 v184, v8, v9\n"
    "v_cvt_pk_bf16_f32 v185, v10, v11\n"
    "v_cvt_pk_bf16_f32 v186, v24, v25\n"
    "v_cvt_pk_bf16_f32 v187, v26, v27\n"
    "buffer_store_dwordx4 v[184:187], v254, s[20:23], s81 offen offset:2048 sc1\n"
    "v_pk_fma_f32 v[236:237], v[48:49], v[220:221], v[52:53] op_sel:[0,1,0] op_sel_hi:[1,1,1]\n"
    "v_pk_fma_f32 v[238:239], v[50:51], v[220:221], v[54:55] op_sel:[0,1,0] op_sel_hi:[1,1,1]\n"
    "buffer_load_dwordx4 a[72:75], v254, s[20:23], s57 offen offset:2048 sc1\n"
    "v_pk_fma_f32 v[240:241], v[56:57], v[220:221], v[32:33] op_sel:[0,1,0] op_sel_hi:[1,1,1]\n"
    "v_pk_fma_f32 v[242:243], v[58:59], v[220:221], v[34:35] op_sel:[0,1,0] op_sel_hi:[1,1,1]\n"
    "v_pk_fma_f32 v[12:13], v[12:13], v[220:221], v[236:237] op_sel_hi:[1,0,1]\n"
    "v_pk_fma_f32 v[14:15], v[14:15], v[220:221], v[238:239] op_sel_hi:[1,0,1]\n"
    "v_pk_fma_f32 v[28:29], v[28:29], v[220:221], v[240:241] op_sel_hi:[1,0,1]\n"
    "v_pk_fma_f32 v[30:31], v[30:31], v[220:221], v[242:243] op_sel_hi:[1,0,1]\n"
    "v_pk_mul_f32 v[236:237], v[12:13], v[12:13]\n"
    "v_pk_mul_f32 v[238:239], v[14:15], v[14:15]\n"
    "v_pk_mul_f32 v[240:241], v[28:29], v[28:29]\n"
    "v_pk_mul_f32 v[242:243], v[30:31], v[30:31]\n"
    "v_pk_fma_f32 v[236:237], v[236:237], s[8:9], s[8:9] op_sel:[0,0,1] op_sel_hi:[1,0,1]\n"
    "v_pk_fma_f32 v[238:239], v[238:239], s[8:9], s[8:9] op_sel:[0,0,1] op_sel_hi:[1,0,1]\n"
    "v_pk_fma_f32 v[240:241], v[240:241], s[8:9], s[8:9] op_sel:[0,0,1] op_sel_hi:[1,0,1]\n"
    "v_pk_fma_f32 v[242:243], v[242:243], s[8:9], s[8:9] op_sel:[0,0,1] op_sel_hi:[1,0,1]\n"
    "buffer_load_dwordx4 a[76:79], v254, s[20:23], s57 offen offset:3072 sc1\n"
    "v_pk_mul_f32 v[236:237], v[12:13], v[236:237]\n"
    "v_pk_mul_f32 v[238:239], v[14:15], v[238:239]\n"
    "v_pk_mul_f32 v[240:241], v[28:29], v[240:241]\n"
    "v_pk_mul_f32 v[242:243], v[30:31], v[242:243]\n"
    "v_exp_f32 v236, v236\n"
    "v_exp_f32 v238, v238\n"
    "v_exp_f32 v240, v240\n"
    "v_exp_f32 v242, v242\n"
    "v_exp_f32 v237, v237\n"
    "v_exp_f32 v239, v239\n"
    "v_exp_f32 v241, v241\n"
    "v_exp_f32 v243, v243\n"
    "v_pk_fma_f32 v[236:237], v[236:237], v[222:223], v[222:223] op_sel_hi:[1,0,0]\n"
    "v_pk_fma_f32 v[238:239], v[238:239], v[222:223], v[222:223] op_sel_hi:[1,0,0]\n"
    "s_add_u32 s57, s73, 4096\n"
    "buffer_load_dwordx4 a[80:83], v254, s[20:23], s57 offen offset:0 sc1\n"
    "v_pk_fma_f32 v[240:241], v[240:241], v[222:223], v[222:223] op_sel_hi:[1,0,0]\n"
    "v_pk_fma_f32 v[242:243], v[242:243], v[222:223], v[222:223] op_sel_hi:[1,0,0]\n"
    "v_rcp_f32 v236, v236\n"
    "v_rcp_f32 v238, v238\n"
    "v_rcp_f32 v240, v240\n"
    "v_rcp_f32 v242, v242\n"
    "v_rcp_f32 v237, v237\n"
    "v_rcp_f32 v239, v239\n"
    "v_rcp_f32 v241, v241\n"
    "v_rcp_f32 v243, v243\n"
    "v_pk_mul_f32 v[12:13], v[12:13], v[236:237]\n"
    "v_pk_mul_f32 v[14:15], v[14:15], v[238:239]\n"
    "v_pk_mul_f32 v[28:29], v[28:29], v[240:241]\n"
    "v_pk_mul_f32 v[30:31], v[30:31], v[242:243]\n"
    "v_cvt_pk_bf16_f32 v188, v12, v13\n"
    "v_cvt_pk_bf16_f32 v189, v14, v15\n"
    "v_cvt_pk_bf16_f32 v190, v28, v29\n"
    "v_cvt_pk_bf16_f32 v191, v30, v31\n"
    "buffer_store_dwordx4 v[188:191], v254, s[20:23], s81 offen offset:3072 sc1\n"
    "s_barrier\n"
    "ds_read_b64_tr_b16 a[224:225], v244 offset:9216\n"
    "ds_read_b64_tr_b16 a[226:227], v244 offset:13824\n"
    "ds_read_b64_tr_b16 a[228:229], v244 offset:18432\n"
    "ds_read_b64_tr_b16 a[230:231], v244 offset:23040\n"
    "ds_read_b64_tr_b16 a[232:233], v244 offset:9248\n"
    "ds_read_b64_tr_b16 a[234:235], v244 offset:13856\n"
    "ds_read_b64_tr_b16 a[236:237], v244 offset:18464\n"
    "ds_read_b64_tr_b16 a[238:239], v244 offset:23072\n"
    "v_lshrrev_b32 v236, 4, v255\n"
    "v_lshlrev_b32 v236, 3, v236\n"
    "s_waitcnt lgkmcnt(6)\n"
    "v_mfma_f32_16x16x32_bf16 v[0:3], a[224:227], v[144:147], 0\n"
    "ds_read_b64_tr_b16 a[240:241], v244 offset:9280\n"
    "s_waitcnt lgkmcnt(5)\n"
    "v_mfma_f32_16x16x32_bf16 v[0:3], a[228:231], v[160:163], v[0:3]\n"
    "buffer_load_dwordx4 a[84:87], v254, s[20:23], s57 offen offset:1024 sc1\n"
    "v_mfma_f32_16x16x32_bf16 v[4:7], a[228:231], v[164:167], 0\n"
    "s_and_b32 s84, %2, 0xf8\n"
    "ds_read_b64_tr_b16 a[242:243], v244 offset:13888\n"
    "v_mfma_f32_16x16x32_bf16 v[4:7], a[224:227], v[148:151], v[4:7]\n"
    "s_mul_i32 s84, s84, 81920\n"
    "v_mfma_f32_16x16x32_bf16 v[8:11], a[224:227], v[152:155], 0\n"
    "s_add_u32 s84, s84, 29360128\n"
    "ds_read_b64_tr_b16 a[244:245], v244 offset:18496\n"
    "v_mfma_f32_16x16x32_bf16 v[8:11], a[228:231], v[168:171], v[8:11]\n"
    "s_mul_i32 s85, s7, 17920\n"
    "buffer_load_dwordx4 a[88:91], v254, s[20:23], s57 offen offset:2048 sc1\n"
    "v_mfma_f32_16x16x32_bf16 v[12:15], a[228:231], v[172:175], 0\n"
    "s_add_u32 s58, s84, s85\n"
    "ds_read_b64_tr_b16 a[246:247], v244 offset:23104\n"
    "v_mfma_f32_16x16x32_bf16 v[12:15], a[224:227], v[156:159], v[12:15]\n"
    "s_waitcnt lgkmcnt(4)\n"
    "v_mfma_f32_16x16x32_bf16 v[28:31], a[232:235], v[156:159], 0\n"
    "s_mul_i32 s85, s4, 81920\n"
    "ds_read_b64_tr_b16 a[248:249], v244 offset:9312\n"
    "v_mfma_f32_16x16x32_bf16 v[28:31], a[236:239], v[172:175], v[28:31]\n"
    "s_add_u32 s59, s58, s85\n"
    "buffer_load_dwordx4 a[92:95], v254, s[20:23], s57 offen offset:3072 sc1\n"
    "v_mfma_f32_16x16x32_bf16 v[24:27], a[236:239], v[168:171], 0\n"
    "s_lshl_b32 s82, s5, 8\n"
    "ds_read_b64_tr_b16 a[250:251], v244 offset:13920\n"
    "v_mfma_f32_16x16x32_bf16 v[24:27], a[232:235], v[152:155], v[24:27]\n"
    "s_lshl_b32 s83, s4, 5\n"
    "v_mfma_f32_16x16x32_bf16 v[20:23], a[232:235], v[148:151], 0\n"
    "s_add_u32 s82, s82, s83\n"
    "ds_read_b64_tr_b16 a[252:253], v244 offset:18528\n"
    "v_mfma_f32_16x16x32_bf16 v[20:23], a[236:239], v[164:167], v[20:23]\n"
    "s_add_u32 s84, s84, 71680\n"
    "buffer_load_dwordx2 v[224:225], v236, s[44:47], s82 offen\n"
    "v_mfma_f32_16x16x32_bf16 v[16:19], a[236:239], v[160:163], 0\n"
    "s_lshl_b32 s82, s5, 5\n"
    "ds_read_b64_tr_b16 a[254:255], v244 offset:23136\n"
    "v_mfma_f32_16x16x32_bf16 v[16:19], a[232:235], v[144:147], v[16:19]\n"
    "s_waitcnt lgkmcnt(4)\n"
    "v_mfma_f32_16x16x32_bf16 v[32:35], a[240:243], v[144:147], 0\n"
    "s_lshl_b32 s83, s4, 12\n"
    "ds_read_b64_tr_b16 a[224:225], v244 offset:9344\n"
    "v_mfma_f32_16x16x32_bf16 v[32:35], a[244:247], v[160:163], v[32:35]\n"
    "s_add_u32 s82, s82, s83\n"
    "buffer_load_dwordx2 v[226:227], v236, s[44:47], s82 offen offset:1024\n"
    "v_mfma_f32_16x16x32_bf16 v[36:39], a[244:247], v[164:167], 0\n"
    "s_add_u32 s61, s84, s85\n"
    "ds_read_b64_tr_b16 a[226:227], v244 offset:13952\n"
    "v_mfma_f32_16x16x32_bf16 v[36:39], a[240:243], v[148:151], v[36:39]\n"
    "s_mul_i32 s85, s7, 163840\n"
    "v_mfma_f32_16x16x32_bf16 v[40:43], a[240:243], v[152:155], 0\n"
    "s_add_u32 s84, s84, s85\n"
    "ds_read_b64_tr_b16 a[228:229], v244 offset:18560\n"
    "v_mfma_f32_16x16x32_bf16 v[40:43], a[244:247], v[168:171], v[40:43]\n"
    "s_mul_i32 s85, s4, 1280\n"
    "buffer_load_dword v248, v247, s[20:23], s77 offen offset:256 sc1\n"
    "v_mfma_f32_16x16x32_bf16 v[44:47], a[244:247], v[172:175], 0\n"
    "s_add_u32 s60, s84, s85\n"
    "ds_read_b64_tr_b16 a[230:231], v244 offset:23168\n"
    "v_mfma_f32_16x16x32_bf16 v[44:47], a[240:243], v[156:159], v[44:47]\n"
    "s_waitcnt lgkmcnt(4)\n"
    "v_mfma_f32_16x16x32_bf16 v[60:63], a[248:251], v[156:159], 0\n"
    "s_mul_i32 s84, s87, 73728\n"
    "ds_read_b64_tr_b16 a[232:233], v244 offset:9376\n"
    "v_mfma_f32_16x16x32_bf16 v[60:63], a[252:255], v[172:175], v[60:63]\n"
    "s_lshl_b32 s85, s5, 3\n"
    "v_mfma_f32_16x16x32_bf16 v[56:59], a[252:255], v[168:171], 0\n"
    "s_add_u32 s85, s85, s4\n"
    "ds_read_b64_tr_b16 a[234:235], v244 offset:13984\n"
    "v_mfma_f32_16x16x32_bf16 v[56:59], a[248:251], v[152:155], v[56:59]\n"
    "s_lshl_b32 s85, s85, 5\n"
    "v_mfma_f32_16x16x32_bf16 v[52:55], a[248:251], v[148:151], 0\n"
    "s_add_u32 s62, s84, s85\n"
    "ds_read_b64_tr_b16 a[236:237], v244 offset:18592\n"
    "v_mfma_f32_16x16x32_bf16 v[52:55], a[252:255], v[164:167], v[52:55]\n"
    "s_and_b32 s85, s4, 1\n"
    "v_mfma_f32_16x16x32_bf16 v[48:51], a[252:255], v[160:163], 0\n"
    "s_mul_i32 s85, s85, 36864\n"
    "ds_read_b64_tr_b16 a[238:239], v244 offset:23200\n"
    "v_mfma_f32_16x16x32_bf16 v[48:51], a[248:251], v[144:147], v[48:51]\n"
    "s_waitcnt lgkmcnt(4)\n"
    "v_mfma_f32_16x16x32_bf16 v[64:67], a[224:227], v[144:147], 0\n"
    "s_add_u32 s84, s84, s85\n"
    "ds_read_b64_tr_b16 a[240:241], v244 offset:9408\n"
    "v_mfma_f32_16x16x32_bf16 v[64:67], a[228:231], v[160:163], v[64:67]\n"
    "s_lshl_b32 s85, s5, 5\n"
    "v_mfma_f32_16x16x32_bf16 v[68:71], a[228:231], v[164:167], 0\n"
    "s_add_u32 s63, s84, s85\n"
    "ds_read_b64_tr_b16 a[242:243], v244 offset:14016\n"
    "v_mfma_f32_16x16x32_bf16 v[68:71], a[224:227], v[148:151], v[68:71]\n"
    "s_mov_b64 s[48:49], s[20:21]\n"
    "v_mfma_f32_16x16x32_bf16 v[72:75], a[224:227], v[152:155], 0\n"
    "s_mov_b32 s51, s23\n"
    "ds_read_b64_tr_b16 a[244:245], v244 offset:18624\n"
    "v_mfma_f32_16x16x32_bf16 v[72:75], a[228:231], v[168:171], v[72:75]\n"
    "s_waitcnt vmcnt(6)\n"
    "v_mfma_f32_16x16x32_bf16 v[76:79], a[228:231], v[172:175], 0\n"
    "s_mov_b64 s[52:53], s[32:33]\n"
    "ds_read_b64_tr_b16 a[246:247], v244 offset:23232\n"
    "v_mfma_f32_16x16x32_bf16 v[76:79], a[224:227], v[156:159], v[76:79]\n"
    "s_waitcnt lgkmcnt(4)\n"
    "v_mfma_f32_16x16x32_bf16 v[92:95], a[232:235], v[156:159], 0\n"
    "s_mov_b32 s55, s35\n"
    "ds_read_b64_tr_b16 a[248:249], v244 offset:9440\n"
    "v_mfma_f32_16x16x32_bf16 v[92:95], a[236:239], v[172:175], v[92:95]\n"
    "s_lshr_b32 s84, s4, 1\n"
    "buffer_store_dword v250, v249, s[20:23], s76 offen offset:512 sc1\n"
    "v_mfma_f32_16x16x32_bf16 v[88:91], a[236:239], v[168:171], 0\n"
    "s_cmp_eq_u32 s84, s7\n"
    "ds_read_b64_tr_b16 a[250:251], v244 offset:14048\n"
    "v_mfma_f32_16x16x32_bf16 v[88:91], a[232:235], v[152:155], v[88:91]\n"
    "s_cselect_b32 s50, s22, 0\n"
    "v_mfma_f32_16x16x32_bf16 v[84:87], a[232:235], v[148:151], 0\n"
    "s_cselect_b32 s54, s34, 0\n"
    "ds_read_b64_tr_b16 a[252:253], v244 offset:18656\n"
    "v_mfma_f32_16x16x32_bf16 v[84:87], a[236:239], v[164:167], v[84:87]\n"
    "s_cselect_b64 s[64:65], -1, 0\n"
    "v_mfma_f32_16x16x32_bf16 v[80:83], a[236:239], v[160:163], 0\n"
    "ds_read_b64_tr_b16 a[254:255], v244 offset:23264\n"
    "v_mfma_f32_16x16x32_bf16 v[80:83], a[232:235], v[144:147], v[80:83]\n"
    "s_waitcnt lgkmcnt(4)\n"
    "v_mfma_f32_16x16x32_bf16 v[96:99], a[240:243], v[144:147], 0\n"
    "ds_read_b64_tr_b16 a[224:225], v244 offset:9472\n"
    "v_mfma_f32_16x16x32_bf16 v[96:99], a[244:247], v[160:163], v[96:99]\n"
    "v_mfma_f32_16x16x32_bf16 v[100:103], a[244:247], v[164:167], 0\n"
    "ds_read_b64_tr_b16 a[226:227], v244 offset:14080\n"
    "v_mfma_f32_16x16x32_bf16 v[100:103], a[240:243], v[148:151], v[100:103]\n"
    "v_mfma_f32_16x16x32_bf16 v[104:107], a[240:243], v[152:155], 0\n"
    "ds_read_b64_tr_b16 a[228:229], v244 offset:18688\n"
    "v_mfma_f32_16x16x32_bf16 v[104:107], a[244:247], v[168:171], v[104:107]\n"
    "v_mfma_f32_16x16x32_bf16 v[108:111], a[244:247], v[172:175], 0\n"
    "ds_read_b64_tr_b16 a[230:231], v244 offset:23296\n"
    "v_mfma_f32_16x16x32_bf16 v[108:111], a[240:243], v[156:159], v[108:111]\n"
    "s_waitcnt lgkmcnt(4)\n"
    "v_mfma_f32_16x16x32_bf16 v[124:127], a[248:251], v[156:159], 0\n"
    "ds_read_b64_tr_b16 a[232:233], v244 offset:27648\n"
    "v_mfma_f32_16x16x32_bf16 v[124:127], a[252:255], v[172:175], v[124:127]\n"
    "v_mfma_f32_16x16x32_bf16 v[120:123], a[252:255], v[168:171], 0\n"
    "ds_read_b64_tr_b16 a[234:235], v244 offset:32256\n"
    "v_mfma_f32_16x16x32_bf16 v[120:123], a[248:251], v[152:155], v[120:123]\n"
    "v_mfma_f32_16x16x32_bf16 v[116:119], a[248:251], v[148:151], 0\n"
    "s_waitcnt vmcnt(1)\n"
    "ds_read_b64_tr_b16 a[236:237], v244 offset:36864\n"
    "v_cmp_ne_u32 vcc, s66, v248\n"
    "v_mfma_f32_16x16x32_bf16 v[116:119], a[252:255], v[164:167], v[116:119]\n"
    "s_nop 0\n"
    "s_cbranch_vccnz RT3_%=\n"
    "RB3_%=:\n"
    "v_mfma_f32_16x16x32_bf16 v[112:115], a[252:255], v[160:163], 0\n"
    "s_add_u32 s57, s72, 8192\n"
    "ds_read_b64_tr_b16 a[238:239], v244 offset:41472\n"
    "v_mfma_f32_16x16x32_bf16 v[112:115], a[248:251], v[144:147], v[112:115]\n"
    "s_waitcnt lgkmcnt(4)\n"
    "buffer_load_dwordx4 a[96:99], v254, s[20:23], s57 offen offset:0 sc1\n"
    "v_mfma_f32_16x16x32_bf16 v[128:131], a[224:227], v[144:147], 0\n"
    "ds_read_b64_tr_b16 a[240:241], v244 offset:27680\n"
    "v_mfma_f32_16x16x32_bf16 v[128:131], a[228:231], v[160:163], v[128:131]\n"
    "v_mfma_f32_16x16x32_bf16 v[132:135], a[228:231], v[164:167], 0\n"
    "ds_read_b64_tr_b16 a[242:243], v244 offset:32288\n"
    "v_mfma_f32_16x16x32_bf16 v[132:135], a[224:227], v[148:151], v[132:135]\n"
    "buffer_load_dwordx4 a[100:103], v254, s[20:23], s57 offen offset:1024 sc1\n"
    "v_mfma_f32_16x16x32_bf16 v[136:139], a[224:227], v[152:155], 0\n"
    "ds_read_b64_tr_b16 a[244:245], v244 offset:36896\n"
    "v_mfma_f32_16x16x32_bf16 v[136:139], a[228:231], v[168:171], v[136:139]\n"
    "v_mfma_f32_16x16x32_bf16 v[140:143], a[228:231], v[172:175], 0\n"
    "ds_read_b64_tr_b16 a[246:247], v244 offset:41504\n"
    "v_mfma_f32_16x16x32_bf16 v[140:143], a[224:227], v[156:159], v[140:143]\n"
    "s_waitcnt lgkmcnt(4)\n"
    "buffer_load_dwordx4 a[104:107], v254, s[20:23], s57 offen offset:2048 sc1\n"
    "v_mfma_f32_16x16x32_bf16 v[12:15], a[232:235], v[188:191], v[12:15]\n"
    "ds_read_b64_tr_b16 a[248:249], v244 offset:27712\n"
    "v_mfma_f32_16x16x32_bf16 v[12:15], a[236:239], v[204:207], v[12:15]\n"
    "v_mfma_f32_16x16x32_bf16 v[8:11], a[236:239], v[200:203], v[8:11]\n"
    "ds_read_b64_tr_b16 a[250:251], v244 offset:32320\n"
    "v_mfma_f32_16x16x32_bf16 v[8:11], a[232:235], v[184:187], v[8:11]\n"
    "buffer_load_dwordx4 a[108:111], v254, s[20:23], s57 offen offset:3072 sc1\n"
    "v_mfma_f32_16x16x32_bf16 v[4:7], a[232:235], v[180:183], v[4:7]\n"
    "s_add_u32 s57, s72, 12288\n"
    "ds_read_b64_tr_b16 a[252:253], v244 offset:36928\n"
    "v_mfma_f32_16x16x32_bf16 v[4:7], a[236:239], v[196:199], v[4:7]\n"
    "v_mfma_f32_16x16x32_bf16 v[0:3], a[236:239], v[192:195], v[0:3]\n"
    "ds_read_b64_tr_b16 a[254:255], v244 offset:41536\n"
    "v_mfma_f32_16x16x32_bf16 v[0:3], a[232:235], v[176:179], v[0:3]\n"
    "s_waitcnt lgkmcnt(4)\n"
    "buffer_load_dwordx4 a[112:115], v254, s[20:23], s57 offen offset:0 sc1\n"
    "v_mfma_f32_16x16x32_bf16 v[16:19], a[240:243], v[176:179], v[16:19]\n"
    "ds_read_b64_tr_b16 a[224:225], v244 offset:27744\n"
    "v_mfma_f32_16x16x32_bf16 v[16:19], a[244:247], v[192:195], v[16:19]\n"
    "v_mfma_f32_16x16x32_bf16 v[20:23], a[244:247], v[196:199], v[20:23]\n"
    "ds_read_b64_tr_b16 a[226:227], v244 offset:32352\n"
    "v_mfma_f32_16x16x32_bf16 v[20:23], a[240:243], v[180:183], v[20:23]\n"
    "buffer_load_dwordx4 a[116:119], v254, s[20:23], s57 offen offset:1024 sc1\n"
    "v_mfma_f32_16x16x32_bf16 v[24:27], a[240:243], v[184:187], v[24:27]\n"
    "ds_read_b64_tr_b16 a[228:229], v244 offset:36960\n"
    "v_mfma_f32_16x16x32_bf16 v[24:27], a[244:247], v[200:203], v[24:27]\n"
    "v_mfma_f32_16x16x32_bf16 v[28:31], a[244:247], v[204:207], v[28:31]\n"
    "ds_read_b64_tr_b16 a[230:231], v244 offset:41568\n"
    "v_mfma_f32_16x16x32_bf16 v[28:31], a[240:243], v[188:191], v[28:31]\n"
    "s_waitcnt lgkmcnt(4)\n"
    "buffer_load_dwordx4 a[120:123], v254, s[20:23], s57 offen offset:2048 sc1\n"
    "v_mfma_f32_16x16x32_bf16 v[44:47], a[248:251], v[188:191], v[44:47]\n"
    "ds_read_b64_tr_b16 a[232:233], v244 offset:27776\n"
    "v_mfma_f32_16x16x32_bf16 v[44:47], a[252:255], v[204:207], v[44:47]\n"
    "v_mfma_f32_16x16x32_bf16 v[40:43], a[252:255], v[200:203], v[40:43]\n"
    "ds_read_b64_tr_b16 a[234:235], v244 offset:32384\n"
    "v_mfma_f32_16x16x32_bf16 v[40:43], a[248:251], v[184:187], v[40:43]\n"
    "buffer_load_dwordx4 a[124:127], v254, s[20:23], s57 offen offset:3072 sc1\n"
    "v_mfma_f32_16x16x32_bf16 v[36:39], a[248:251], v[180:183], v[36:39]\n"
    "s_add_u32 s57, s73, 8192\n"
    "ds_read_b64_tr_b16 a[236:237], v244 offset:36992\n"
    "v_mfma_f32_16x16x32_bf16 v[36:39], a[252:255], v[196:199], v[36:39]\n"
    "v_mfma_f32_16x16x32_bf16 v[32:35], a[252:255], v[192:195], v[32:35]\n"
    "ds_read_b64_tr_b16 a[238:239], v244 offset:41600\n"
    "v_mfma_f32_16x16x32_bf16 v[32:35], a[248:251], v[176:179], v[32:35]\n"
    "s_waitcnt lgkmcnt(4)\n"
    "buffer_load_dwordx4 a[128:131], v254, s[20:23], s57 offen offset:0 sc1\n"
    "v_mfma_f32_16x16x32_bf16 v[48:51], a[224:227], v[176:179], v[48:51]\n"
    "ds_read_b64_tr_b16 a[240:241], v244 offset:27808\n"
    "v_mfma_f32_16x16x32_bf16 v[48:51], a[228:231], v[192:195], v[48:51]\n"
    "v_mfma_f32_16x16x32_bf16 v[52:55], a[228:231], v[196:199], v[52:55]\n"
    "ds_read_b64_tr_b16 a[242:243], v244 offset:32416\n"
    "v_mfma_f32_16x16x32_bf16 v[52:55], a[224:227], v[180:183], v[52:55]\n"
    "buffer_load_dwordx4 a[132:135], v254, s[20:23], s57 offen offset:1024 sc1\n"
    "v_mfma_f32_16x16x32_bf16 v[56:59], a[224:227], v[184:187], v[56:59]\n"
    "ds_read_b64_tr_b16 a[244:245], v244 offset:37024\n"
    "v_mfma_f32_16x16x32_bf16 v[56:59], a[228:231], v[200:203], v[56:59]\n"
    "v_mfma_f32_16x16x32_bf16 v[60:63], a[228:231], v[204:207], v[60:63]\n"
    "ds_read_b64_tr_b16 a[246:247], v244 offset:41632\n"
    "v_mfma_f32_16x16x32_bf16 v[60:63], a[224:227], v[188:191], v[60:63]\n"
    "s_waitcnt lgkmcnt(4)\n"
    "buffer_load_dwordx4 a[136:139], v254, s[20:23], s57 offen offset:2048 sc1\n"
    "v_mfma_f32_16x16x32_bf16 v[76:79], a[232:235], v[188:191], v[76:79]\n"
    "ds_read_b64_tr_b16 a[248:249], v244 offset:27840\n"
    "v_mfma_f32_16x16x32_bf16 v[76:79], a[236:239], v[204:207], v[76:79]\n"
    "v_mfma_f32_16x16x32_bf16 v[72:75], a[236:239], v[200:203], v[72:75]\n"
    "ds_read_b64_tr_b16 a[250:251], v244 offset:32448\n"
    "v_mfma_f32_16x16x32_bf16 v[72:75], a[232:235], v[184:187], v[72:75]\n"
    "buffer_load_dwordx4 a[140:143], v254, s[20:23], s57 offen offset:3072 sc1\n"
    "v_mfma_f32_16x16x32_bf16 v[68:71], a[232:235], v[180:183], v[68:71]\n"
    "s_add_u32 s57, s73, 12288\n"
    "ds_read_b64_tr_b16 a[252:253], v244 offset:37056\n"
    "v_mfma_f32_16x16x32_bf16 v[68:71], a[236:239], v[196:199], v[68:71]\n"
    "v_mfma_f32_16x16x32_bf16 v[64:67], a[236:239], v[192:195], v[64:67]\n"
    "ds_read_b64_tr_b16 a[254:255], v244 offset:41664\n"
    "v_mfma_f32_16x16x32_bf16 v[64:67], a[232:235], v[176:179], v[64:67]\n"
    "s_waitcnt lgkmcnt(4)\n"
    "buffer_load_dwordx4 a[144:147], v254, s[20:23], s57 offen offset:0 sc1\n"
    "v_mfma_f32_16x16x32_bf16 v[80:83], a[240:243], v[176:179], v[80:83]\n"
    "ds_read_b64_tr_b16 a[224:225], v244 offset:27872\n"
    "v_mfma_f32_16x16x32_bf16 v[80:83], a[244:247], v[192:195], v[80:83]\n"
    "v_mfma_f32_16x16x32_bf16 v[84:87], a[244:247], v[196:199], v[84:87]\n"
    "ds_read_b64_tr_b16 a[226:227], v244 offset:32480\n"
    "v_mfma_f32_16x16x32_bf16 v[84:87], a[240:243], v[180:183], v[84:87]\n"
    "buffer_load_dwordx4 a[148:151], v254, s[20:23], s57 offen offset:1024 sc1\n"
    "v_mfma_f32_16x16x32_bf16 v[88:91], a[240:243], v[184:187], v[88:91]\n"
    "ds_read_b64_tr_b16 a[228:229], v244 offset:37088\n"
    "v_mfma_f32_16x16x32_bf16 v[88:91], a[244:247], v[200:203], v[88:91]\n"
    "v_mfma_f32_16x16x32_bf16 v[92:95], a[244:247], v[204:207], v[92:95]\n"
    "ds_read_b64_tr_b16 a[230:231], v244 offset:41696\n"
    "v_mfma_f32_16x16x32_bf16 v[92:95], a[240:243], v[188:191], v[92:95]\n"
    "s_waitcnt lgkmcnt(4)\n"
    "buffer_load_dwordx4 a[152:155], v254, s[20:23], s57 offen offset:2048 sc1\n"
    "v_mfma_f32_16x16x32_bf16 v[108:111], a[248:251], v[188:191], v[108:111]\n"
    "ds_read_b64_tr_b16 a[232:233], v244 offset:27904\n"
    "v_mfma_f32_16x16x32_bf16 v[108:111], a[252:255], v[204:207], v[108:111]\n"
    "v_mfma_f32_16x16x32_bf16 v[104:107], a[252:255], v[200:203], v[104:107]\n"
    "ds_read_b64_tr_b16 a[234:235], v244 offset:32512\n"
    "v_mfma_f32_16x16x32_bf16 v[104:107], a[248:251], v[184:187], v[104:107]\n"
    "buffer_load_dwordx4 a[156:159], v254, s[20:23], s57 offen offset:3072 sc1\n"
    "v_mfma_f32_16x16x32_bf16 v[100:103], a[248:251], v[180:183], v[100:103]\n"
    "ds_read_b64_tr_b16 a[236:237], v244 offset:37120\n"
    "v_mfma_f32_16x16x32_bf16 v[100:103], a[252:255], v[196:199], v[100:103]\n"
    "v_mfma_f32_16x16x32_bf16 v[96:99], a[252:255], v[192:195], v[96:99]\n"
    "ds_read_b64_tr_b16 a[238:239], v244 offset:41728\n"
    "v_mfma_f32_16x16x32_bf16 v[96:99], a[248:251], v[176:179], v[96:99]\n"
    "s_waitcnt lgkmcnt(4)\n"
    "buffer_load_dword v248, v247, s[20:23], s77 offen offset:512 sc1\n"
    "v_mfma_f32_16x16x32_bf16 v[112:115], a[224:227], v[176:179], v[112:115]\n"
    "ds_read_b64_tr_b16 a[240:241], v244 offset:46080\n"
    "v_mfma_f32_16x16x32_bf16 v[112:115], a[228:231], v[192:195], v[112:115]\n"
    "v_mfma_f32_16x16x32_bf16 v[116:119], a[228:231], v[196:199], v[116:119]\n"
    "ds_read_b64_tr_b16 a[242:243], v244 offset:50688\n"
    "v_mfma_f32_16x16x32_bf16 v[116:119], a[224:227], v[180:183], v[116:119]\n"
    "v_mfma_f32_16x16x32_bf16 v[120:123], a[224:227], v[184:187], v[120:123]\n"
    "ds_read_b64_tr_b16 a[244:245], v245 offset:0\n"
    "v_mfma_f32_16x16x32_bf16 v[120:123], a[228:231], v[200:203], v[120:123]\n"
    "v_mfma_f32_16x16x32_bf16 v[124:127], a[228:231], v[204:207], v[124:127]\n"
    "ds_read_b64_tr_b16 a[246:247], v245 offset:4608\n"
    "v_mfma_f32_16x16x32_bf16 v[124:127], a[224:227], v[188:191], v[124:127]\n"
    "s_waitcnt lgkmcnt(4)\n"
    "v_mfma_f32_16x16x32_bf16 v[140:143], a[232:235], v[188:191], v[140:143]\n"
    "ds_read_b64_tr_b16 a[248:249], v244 offset:46112\n"
    "v_mfma_f32_16x16x32_bf16 v[140:143], a[236:239], v[204:207], v[140:143]\n"
    "v_mfma_f32_16x16x32_bf16 v[136:139], a[236:239], v[200:203], v[136:139]\n"
    "ds_read_b64_tr_b16 a[250:251], v244 offset:50720\n"
    "v_mfma_f32_16x16x32_bf16 v[136:139], a[232:235], v[184:187], v[136:139]\n"
    "v_mfma_f32_16x16x32_bf16 v[132:135], a[232:235], v[180:183], v[132:135]\n"
    "ds_read_b64_tr_b16 a[252:253], v245 offset:32\n"
    "v_mfma_f32_16x16x32_bf16 v[132:135], a[236:239], v[196:199], v[132:135]\n"
    "v_mfma_f32_16x16x32_bf16 v[128:131], a[236:239], v[192:195], v[128:131]\n"
    "ds_read_b64_tr_b16 a[254:255], v245 offset:4640\n"
    "v_mfma_f32_16x16x32_bf16 v[128:131], a[232:235], v[176:179], v[128:131]\n"
    "s_waitcnt lgkmcnt(4)\n"
    "v_mfma_f32_16x16x32_bf16 v[0:3], a[240:243], a[0:3], v[0:3]\n"
    "s_barrier\n"
    "ds_read_b64_tr_b16 a[224:225], v244 offset:46144\n"
    "v_mfma_f32_16x16x32_bf16 v[0:3], a[244:247], a[16:19], v[0:3]\n"
    "s_add_u32 m0, s78, 0\n"
    "v_mfma_f32_16x16x32_bf16 v[4:7], a[244:247], a[20:23], v[4:7]\n"
    "s_mov_b32 s56, s36\n"
    "ds_read_b64_tr_b16 a[226:227], v244 offset:50752\n"
    "v_mfma_f32_16x16x32_bf16 v[4:7], a[240:243], a[4:7], v[4:7]\n"
    "buffer_load_dwordx4 v251, s[28:31], s56 offen offset:0 lds\n"
    "v_mfma_f32_16x16x32_bf16 v[8:11], a[240:243], a[8:11], v[8:11]\n"
    "ds_read_b64_tr_b16 a[228:229], v245 offset:64\n"
    "v_mfma_f32_16x16x32_bf16 v[8:11], a[244:247], a[24:27], v[8:11]\n"
    "v_mfma_f32_16x16x32_bf16 v[12:15], a[244:247], a[28:31], v[12:15]\n"
    "ds_read_b64_tr_b16 a[230:231], v245 offset:4672\n"
    "v_mfma_f32_16x16x32_bf16 v[12:15], a[240:243], a[12:15], v[12:15]\n"
    "s_waitcnt lgkmcnt(4)\n"
    "buffer_load_dwordx4 v252, s[28:31], s56 offen offset:1024 lds\n"
    "v_mfma_f32_16x16x32_bf16 v[28:31], a[248:251], a[12:15], v[28:31]\n"
    "s_add_u32 m0, s79, 0\n"
    "ds_read_b64_tr_b16 a[232:233], v244 offset:46176\n"
    "v_mfma_f32_16x16x32_bf16 v[28:31], a[252:255], a[28:31], v[28:31]\n"
    "v_mfma_f32_16x16x32_bf16 v[24:27], a[252:255], a[24:27], v[24:27]\n"
    "ds_read_b64_tr_b16 a[234:235], v244 offset:50784\n"
    "v_mfma_f32_16x16x32_bf16 v[24:27], a[248:251], a[8:11], v[24:27]\n"
    "buffer_load_dwordx4 v253, s[28:31], s56 offen offset:0 lds\n"
    "v_mfma_f32_16x16x32_bf16 v[20:23], a[248:251], a[4:7], v[20:23]\n"
    "s_add_u32 m0, s78, 9216\n"
    "ds_read_b64_tr_b16 a[236:237], v245 offset:96\n"
    "v_mfma_f32_16x16x32_bf16 v[20:23], a[252:255], a[20:23], v[20:23]\n"
    "s_add_u32 s56, s56, 36864\n"
    "v_mfma_f32_16x16x32_bf16 v[16:19], a[252:255], a[16:19], v[16:19]\n"
    "ds_read_b64_tr_b16 a[238:239], v245 offset:4704\n"
    "v_mfma_f32_16x16x32_bf16 v[16:19], a[248:251], a[0:3], v[16:19]\n"
    "s_waitcnt lgkmcnt(4)\n"
    "buffer_load_dwordx4 v251, s[28:31], s56 offen offset:0 lds\n"
    "v_mfma_f32_16x16x32_bf16 v[32:35], a[224:227], a[0:3], v[32:35]\n"
    "ds_read_b64_tr_b16 a[240:241], v244 offset:46208\n"
    "v_mfma_f32_16x16x32_bf16 v[32:35], a[228:231], a[16:19], v[32:35]\n"
    "v_mfma_f32_16x16x32_bf16 v[36:39], a[228:231], a[20:23], v[36:39]\n"
    "ds_read_b64_tr_b16 a[242:243], v244 offset:50816\n"
    "v_mfma_f32_16x16x32_bf16 v[36:39], a[224:227], a[4:7], v[36:39]\n"
    "buffer_load_dwordx4 v252, s[28:31], s56 offen offset:1024 lds\n"
    "v_mfma_f32_16x16x32_bf16 v[40:43], a[224:227], a[8:11], v[40:43]\n"
    "s_add_u32 m0, s78, 18432\n"
    "ds_read_b64_tr_b16 a[244:245], v245 offset:128\n"
    "v_mfma_f32_16x16x32_bf16 v[40:43], a[228:231], a[24:27], v[40:43]\n"
    "s_add_u32 s56, s56, 36864\n"
    "v_mfma_f32_16x16x32_bf16 v[44:47], a[228:231], a[28:31], v[44:47]\n"
    "ds_read_b64_tr_b16 a[246:247], v245 offset:4736\n"
    "v_mfma_f32_16x16x32_bf16 v[44:47], a[224:227], a[12:15], v[44:47]\n"
    "s_waitcnt lgkmcnt(4)\n"
    "buffer_load_dwordx4 v251, s[28:31], s56 offen offset:0 lds\n"
    "v_mfma_f32_16x16x32_bf16 v[60:63], a[232:235], a[12:15], v[60:63]\n"
    "ds_read_b64_tr_b16 a[248:249], v244 offset:46240\n"
    "v_mfma_f32_16x16x32_bf16 v[60:63], a[236:239], a[28:31], v[60:63]\n"
    "v_mfma_f32_16x16x32_bf16 v[56:59], a[236:239], a[24:27], v[56:59]\n"
    "ds_read_b64_tr_b16 a[250:251], v244 offset:50848\n"
    "v_mfma_f32_16x16x32_bf16 v[56:59], a[232:235], a[8:11], v[56:59]\n"
    "buffer_load_dwordx4 v252, s[28:31], s56 offen offset:1024 lds\n"
    "v_mfma_f32_16x16x32_bf16 v[52:55], a[232:235], a[4:7], v[52:55]\n"
    "s_add_u32 m0, s78, 27648\n"
    "ds_read_b64_tr_b16 a[252:253], v245 offset:160\n"
    "v_mfma_f32_16x16x32_bf16 v[52:55], a[236:239], a[20:23], v[52:55]\n"
    "s_add_u32 s56, s70, 0\n"
    "v_mfma_f32_16x16x32_bf16 v[48:51], a[236:239], a[16:19], v[48:51]\n"
    "ds_read_b64_tr_b16 a[254:255], v245 offset:4768\n"
    "v_mfma_f32_16x16x32_bf16 v[48:51], a[232:235], a[0:3], v[48:51]\n"
    "s_waitcnt lgkmcnt(4)\n"
    "buffer_load_dwordx4 v251, s[24:27], s56 offen offset:0 lds\n"
    "v_mfma_f32_16x16x32_bf16 v[64:67], a[240:243], a[0:3], v[64:67]\n"
    "ds_read_b64_tr_b16 a[224:225], v244 offset:46272\n"
    "v_mfma_f32_16x16x32_bf16 v[64:67], a[244:247], a[16:19], v[64:67]\n"
    "v_mfma_f32_16x16x32_bf16 v[68:71], a[244:247], a[20:23], v[68:71]\n"
    "ds_read_b64_tr_b16 a[226:227], v244 offset:50880\n"
    "v_mfma_f32_16x16x32_bf16 v[68:71], a[240:243], a[4:7], v[68:71]\n"
    "buffer_load_dwordx4 v252, s[24:27], s56 offen offset:1024 lds\n"
    "v_mfma_f32_16x16x32_bf16 v[72:75], a[240:243], a[8:11], v[72:75]\n"
    "s_add_u32 m0, s79, 27648\n"
    "ds_read_b64_tr_b16 a[228:229], v245 offset:192\n"
    "v_mfma_f32_16x16x32_bf16 v[72:75], a[244:247], a[24:27], v[72:75]\n"
    "v_mfma_f32_16x16x32_bf16 v[76:79], a[244:247], a[28:31], v[76:79]\n"
    "ds_read_b64_tr_b16 a[230:231], v245 offset:4800\n"
    "v_mfma_f32_16x16x32_bf16 v[76:79], a[240:243], a[12:15], v[76:79]\n"
    "s_waitcnt lgkmcnt(4)\n"
    "buffer_load_dwordx4 v253, s[24:27], s56 offen offset:0 lds\n"
    "v_mfma_f32_16x16x32_bf16 v[92:95], a[248:251], a[12:15], v[92:95]\n"
    "s_add_u32 m0, s78, 36864\n"
    "ds_read_b64_tr_b16 a[232:233], v244 offset:46304\n"
    "v_mfma_f32_16x16x32_bf16 v[92:95], a[252:255], a[28:31], v[92:95]\n"
    "s_add_u32 s56, s56, 36864\n"
    "v_mfma_f32_16x16x32_bf16 v[88:91], a[252:255], a[24:27], v[88:91]\n"
    "ds_read_b64_tr_b16 a[234:235], v244 offset:50912\n"
    "v_mfma_f32_16x16x32_bf16 v[88:91], a[248:251], a[8:11], v[88:91]\n"
    "buffer_load_dwordx4 v251, s[24:27], s56 offen offset:0 lds\n"
    "v_mfma_f32_16x16x32_bf16 v[84:87], a[248:251], a[4:7], v[84:87]\n"
    "ds_read_b64_tr_b16 a[236:237], v245 offset:224\n"
    "v_mfma_f32_16x16x32_bf16 v[84:87], a[252:255], a[20:23], v[84:87]\n"
    "v_mfma_f32_16x16x32_bf16 v[80:83], a[252:255], a[16:19], v[80:83]\n"
    "ds_read_b64_tr_b16 a[238:239], v245 offset:4832\n"
    "v_mfma_f32_16x16x32_bf16 v[80:83], a[248:251], a[0:3], v[80:83]\n"
    "s_waitcnt lgkmcnt(4)\n"
    "buffer_load_dwordx4 v252, s[24:27], s56 offen offset:1024 lds\n"
    "v_mfma_f32_16x16x32_bf16 v[96:99], a[224:227], a[0:3], v[96:99]\n"
    "s_waitcnt vmcnt(12)\n"
    "ds_read_b64_tr_b16 a[240:241], v244 offset:46336\n"
    "v_cmp_ne_u32 vcc, s66, v248\n"
    "v_mfma_f32_16x16x32_bf16 v[96:99], a[228:231], a[16:19], v[96:99]\n"
    "s_nop 0\n"
    "s_cbranch_vccnz RT4_%=\n"
    "RB4_%=:\n"
    "v_mfma_f32_16x16x32_bf16 v[100:103], a[228:231], a[20:23], v[100:103]\n"
    "s_add_u32 s57, s72, 16384\n"
    "ds_read_b64_tr_b16 a[242:243], v244 offset:50944\n"
    "v_mfma_f32_16x16x32_bf16 v[100:103], a[224:227], a[4:7], v[100:103]\n"
    "buffer_load_dwordx4 a[160:163], v254, s[20:23], s57 offen offset:0 sc1\n"
    "v_mfma_f32_16x16x32_bf16 v[104:107], a[224:227], a[8:11], v[104:107]\n"
    "ds_read_b64_tr_b16 a[244:245], v245 offset:256\n"
    "v_mfma_f32_16x16x32_bf16 v[104:107], a[228:231], a[24:27], v[104:107]\n"
    "v_mfma_f32_16x16x32_bf16 v[108:111], a[228:231], a[28:31], v[108:111]\n"
    "ds_read_b64_tr_b16 a[246:247], v245 offset:4864\n"
    "v_mfma_f32_16x16x32_bf16 v[108:111], a[224:227], a[12:15], v[108:111]\n"
    "s_waitcnt lgkmcnt(4)\n"
    "buffer_load_dwordx4 a[164:167], v254, s[20:23], s57 offen offset:1024 sc1\n"
    "v_mfma_f32_16x16x32_bf16 v[124:127], a[232:235], a[12:15], v[124:127]\n"
    "ds_read_b64_tr_b16 a[248:249], v245 offset:9216\n"
    "v_mfma_f32_16x16x32_bf16 v[124:127], a[236:239], a[28:31], v[124:127]\n"
    "v_mfma_f32_16x16x32_bf16 v[120:123], a[236:239], a[24:27], v[120:123]\n"
    "ds_read_b64_tr_b16 a[250:251], v245 offset:13824\n"
    "v_mfma_f32_16x16x32_bf16 v[120:123], a[232:235], a[8:11], v[120:123]\n"
    "buffer_load_dwordx4 a[168:171], v254, s[20:23], s57 offen offset:2048 sc1\n"
    "v_mfma_f32_16x16x32_bf16 v[116:119], a[232:235], a[4:7], v[116:119]\n"
    "ds_read_b64_tr_b16 a[252:253], v245 offset:18432\n"
    "v_mfma_f32_16x16x32_bf16 v[116:119], a[236:239], a[20:23], v[116:119]\n"
    "v_mfma_f32_16x16x32_bf16 v[112:115], a[236:239], a[16:19], v[112:115]\n"
    "ds_read_b64_tr_b16 a[254:255], v245 offset:23040\n"
    "v_mfma_f32_16x16x32_bf16 v[112:115], a[232:235], a[0:3], v[112:115]\n"
    "s_waitcnt lgkmcnt(4)\n"
    "buffer_load_dwordx4 a[172:175], v254, s[20:23], s57 offen offset:3072 sc1\n"
    "v_mfma_f32_16x16x32_bf16 v[128:131], a[240:243], a[0:3], v[128:131]\n"
    "s_add_u32 s57, s72, 20480\n"
    "ds_read_b64_tr_b16 a[224:225], v245 offset:9248\n"
    "v_mfma_f32_16x16x32_bf16 v[128:131], a[244:247], a[16:19], v[128:131]\n"
    "v_mfma_f32_16x16x32_bf16 v[132:135], a[244:247], a[20:23], v[132:135]\n"
    "ds_read_b64_tr_b16 a[226:227], v245 offset:13856\n"
    "v_mfma_f32_16x16x32_bf16 v[132:135], a[240:243], a[4:7], v[132:135]\n"
    "buffer_load_dwordx4 a[176:179], v254, s[20:23], s57 offen offset:0 sc1\n"
    "v_mfma_f32_16x16x32_bf16 v[136:139], a[240:243], a[8:11], v[136:139]\n"
    "ds_read_b64_tr_b16 a[228:229], v245 offset:18464\n"
    "v_mfma_f32_16x16x32_bf16 v[136:139], a[244:247], a[24:27], v[136:139]\n"
    "v_mfma_f32_16x16x32_bf16 v[140:143], a[244:247], a[28:31], v[140:143]\n"
    "ds_read_b64_tr_b16 a[230:231], v245 offset:23072\n"
    "v_mfma_f32_16x16x32_bf16 v[140:143], a[240:243], a[12:15], v[140:143]\n"
    "s_waitcnt lgkmcnt(4)\n"
    "buffer_load_dwordx4 a[180:183], v254, s[20:23], s57 offen offset:1024 sc1\n"
    "v_mfma_f32_16x16x32_bf16 v[12:15], a[248:251], a[44:47], v[12:15]\n"
    "ds_read_b64_tr_b16 a[232:233], v245 offset:9280\n"
    "v_mfma_f32_16x16x32_bf16 v[12:15], a[252:255], a[60:63], v[12:15]\n"
    "v_mfma_f32_16x16x32_bf16 v[8:11], a[252:255], a[56:59], v[8:11]\n"
    "ds_read_b64_tr_b16 a[234:235], v245 offset:13888\n"
    "v_mfma_f32_16x16x32_bf16 v[8:11], a[248:251], a[40:43], v[8:11]\n"
    "buffer_load_dwordx4 a[184:187], v254, s[20:23], s57 offen offset:2048 sc1\n"
    "v_mfma_f32_16x16x32_bf16 v[4:7], a[248:251], a[36:39], v[4:7]\n"
    "ds_read_b64_tr_b16 a[236:237], v245 offset:18496\n"
    "v_mfma_f32_16x16x32_bf16 v[4:7], a[252:255], a[52:55], v[4:7]\n"
    "v_mfma_f32_16x16x32_bf16 v[0:3], a[252:255], a[48:51], v[0:3]\n"
    "ds_read_b64_tr_b16 a[238:239], v245 offset:23104\n"
    "v_mfma_f32_16x16x32_bf16 v[0:3], a[248:251], a[32:35], v[0:3]\n"
    "s_waitcnt lgkmcnt(4)\n"
    "buffer_load_dwordx4 a[188:191], v254, s[20:23], s57 offen offset:3072 sc1\n"
    "v_mfma_f32_16x16x32_bf16 v[16:19], a[224:227], a[32:35], v[16:19]\n"
    "s_add_u32 s57, s73, 16384\n"
    "ds_read_b64_tr_b16 a[240:241], v245 offset:9312\n"
    "v_mfma_f32_16x16x32_bf16 v[16:19], a[228:231], a[48:51], v[16:19]\n"
    "v_mfma_f32_16x16x32_bf16 v[20:23], a[228:231], a[52:55], v[20:23]\n"
    "ds_read_b64_tr_b16 a[242:243], v245 offset:13920\n"
    "v_lshlrev_b32 v228, 16, v224\n"
    "v_mfma_f32_16x16x32_bf16 v[20:23], a[224:227], a[36:39], v[20:23]\n"
    "buffer_load_dwordx4 a[192:195], v254, s[20:23], s57 offen offset:0 sc1\n"
    "v_mfma_f32_16x16x32_bf16 v[24:27], a[224:227], a[40:43], v[24:27]\n"
    "ds_read_b64_tr_b16 a[244:245], v245 offset:18528\n"
    "v_and_b32 v229, 0xffff0000, v224\n"
    "v_mfma_f32_16x16x32_bf16 v[24:27], a[228:231], a[56:59], v[24:27]\n"
    "v_lshlrev_b32 v230, 16, v225\n"
    "v_and_b32 v231, 0xffff0000, v225\n"
    "v_mfma_f32_16x16x32_bf16 v[28:31], a[228:231], a[60:63], v[28:31]\n"
    "ds_read_b64_tr_b16 a[246:247], v245 offset:23136\n"
    "v_lshlrev_b32 v232, 16, v226\n"
    "v_mfma_f32_16x16x32_bf16 v[28:31], a[224:227], a[44:47], v[28:31]\n"
    "s_waitcnt lgkmcnt(4)\n"
    "buffer_load_dwordx4 a[196:199], v254, s[20:23], s57 offen offset:1024 sc1\n"
    "v_mfma_f32_16x16x32_bf16 v[44:47], a[232:235], a[44:47], v[44:47]\n"
    "ds_read_b64_tr_b16 a[248:249], v245 offset:9344\n"
    "v_and_b32 v233, 0xffff0000, v226\n"
    "v_mfma_f32_16x16x32_bf16 v[44:47], a[236:239], a[60:63], v[44:47]\n"
    "v_lshlrev_b32 v234, 16, v227\n"
    "v_and_b32 v235, 0xffff0000, v227\n"
    "v_mfma_f32_16x16x32_bf16 v[40:43], a[236:239], a[56:59], v[40:43]\n"
    "ds_read_b64_tr_b16 a[250:251], v245 offset:13952\n"
    "v_mfma_f32_16x16x32_bf16 v[40:43], a[232:235], a[40:43], v[40:43]\n"
    "buffer_load_dwordx4 a[200:203], v254, s[20:23], s57 offen offset:2048 sc1\n"
    "v_mfma_f32_16x16x32_bf16 v[36:39], a[232:235], a[36:39], v[36:39]\n"
    "ds_read_b64_tr_b16 a[252:253], v245 offset:18560\n"
    "v_mfma_f32_16x16x32_bf16 v[36:39], a[236:239], a[52:55], v[36:39]\n"
    "v_rcp_f32 v224, v210\n"
    "v_mfma_f32_16x16x32_bf16 v[32:35], a[236:239], a[48:51], v[32:35]\n"
    "ds_read_b64_tr_b16 a[254:255], v245 offset:23168\n"
    "v_mfma_f32_16x16x32_bf16 v[32:35], a[232:235], a[32:35], v[32:35]\n"
    "s_waitcnt lgkmcnt(4)\n"
    "buffer_load_dwordx4 a[204:207], v254, s[20:23], s57 offen offset:3072 sc1\n"
    "v_mfma_f32_16x16x32_bf16 v[48:51], a[240:243], a[32:35], v[48:51]\n"
    "s_add_u32 s57, s73, 20480\n"
    "ds_read_b64_tr_b16 a[224:225], v245 offset:9376\n"
    "v_mfma_f32_16x16x32_bf16 v[48:51], a[244:247], a[48:51], v[48:51]\n"
    "v_rcp_f32 v225, v214\n"
    "v_mfma_f32_16x16x32_bf16 v[52:55], a[244:247], a[52:55], v[52:55]\n"
    "ds_read_b64_tr_b16 a[226:227], v245 offset:13984\n"
    "v_mfma_f32_16x16x32_bf16 v[52:55], a[240:243], a[36:39], v[52:55]\n"
    "buffer_load_dwordx4 a[208:211], v254, s[20:23], s57 offen offset:0 sc1\n"
    "v_mfma_f32_16x16x32_bf16 v[56:59], a[240:243], a[40:43], v[56:59]\n"
    "ds_read_b64_tr_b16 a[228:229], v245 offset:18592\n"
    "v_mfma_f32_16x16x32_bf16 v[56:59], a[244:247], a[56:59], v[56:59]\n"
    "v_rcp_f32 v226, v218\n"
    "v_mfma_f32_16x16x32_bf16 v[60:63], a[244:247], a[60:63], v[60:63]\n"
    "ds_read_b64_tr_b16 a[230:231], v245 offset:23200\n"
    "v_mfma_f32_16x16x32_bf16 v[60:63], a[240:243], a[44:47], v[60:63]\n"
    "s_waitcnt lgkmcnt(4)\n"
    "buffer_load_dwordx4 a[212:215], v254, s[20:23], s57 offen offset:1024 sc1\n"
    "v_mfma_f32_16x16x32_bf16 v[76:79], a[248:251], a[44:47], v[76:79]\n"
    "ds_read_b64_tr_b16 a[232:233], v245 offset:9408\n"
    "v_mfma_f32_16x16x32_bf16 v[76:79], a[252:255], a[60:63], v[76:79]\n"
    "v_rcp_f32 v227, v222\n"
    "v_mfma_f32_16x16x32_bf16 v[72:75], a[252:255], a[56:59], v[72:75]\n"
    "ds_read_b64_tr_b16 a[234:235], v245 offset:14016\n"
    "v_fmac_f32 v0, v224, v232\n"
    "v_mfma_f32_16x16x32_bf16 v[72:75], a[248:251], a[40:43], v[72:75]\n"
    "buffer_load_dwordx4 a[216:219], v254, s[20:23], s57 offen offset:2048 sc1\n"
    "v_mfma_f32_16x16x32_bf16 v[68:71], a[248:251], a[36:39], v[68:71]\n"
    "ds_read_b64_tr_b16 a[236:237], v245 offset:18624\n"
    "v_fmac_f32 v1, v224, v233\n"
    "v_mfma_f32_16x16x32_bf16 v[68:71], a[252:255], a[52:55], v[68:71]\n"
    "v_fmac_f32 v2, v224, v234\n"
    "v_fmac_f32 v3, v224, v235\n"
    "v_mfma_f32_16x16x32_bf16 v[64:67], a[252:255], a[48:51], v[64:67]\n"
    "ds_read_b64_tr_b16 a[238:239], v245 offset:23232\n"
    "v_mul_f32 v208, v224, v228\n"
    "v_mfma_f32_16x16x32_bf16 v[64:67], a[248:251], a[32:35], v[64:67]\n"
    "s_waitcnt lgkmcnt(4)\n"
    "buffer_load_dwordx4 a[220:223], v254, s[20:23], s57 offen offset:3072 sc1\n"
    "v_mfma_f32_16x16x32_bf16 v[80:83], a[224:227], a[32:35], v[80:83]\n"
    "s_add_u32 s80, s77, 98304\n"
    "ds_read_b64_tr_b16 a[240:241], v245 offset:9440\n"
    "v_mul_f32 v209, v224, v229\n"
    "v_mfma_f32_16x16x32_bf16 v[80:83], a[228:231], a[48:51], v[80:83]\n"
    "v_mul_f32 v210, v224, v230\n"
    "v_mul_f32 v211, v224, v231\n"
    "v_mfma_f32_16x16x32_bf16 v[84:87], a[228:231], a[52:55], v[84:87]\n"
    "ds_read_b64_tr_b16 a[242:243], v245 offset:14048\n"
    "v_fmac_f32 v4, v225, v232\n"
    "v_mfma_f32_16x16x32_bf16 v[84:87], a[224:227], a[36:39], v[84:87]\n"
    "buffer_load_dword v248, off, s[20:23], s80 sc1\n"
    "v_mfma_f32_16x16x32_bf16 v[88:91], a[224:227], a[40:43], v[88:91]\n"
    "ds_read_b64_tr_b16 a[244:245], v245 offset:18656\n"
    "v_fmac_f32 v5, v225, v233\n"
    "v_mfma_f32_16x16x32_bf16 v[88:91], a[228:231], a[56:59], v[88:91]\n"
    "v_fmac_f32 v6, v225, v234\n"
    "v_fmac_f32 v7, v225, v235\n"
    "v_mfma_f32_16x16x32_bf16 v[92:95], a[228:231], a[60:63], v[92:95]\n"
    "ds_read_b64_tr_b16 a[246:247], v245 offset:23264\n"
    "v_mul_f32 v212, v225, v228\n"
    "v_mfma_f32_16x16x32_bf16 v[92:95], a[224:227], a[44:47], v[92:95]\n"
    "s_waitcnt lgkmcnt(4)\n"
    "v_mul_f32 v213, v225, v229\n"
    "v_mul_f32 v214, v225, v230\n"
    "v_mfma_f32_16x16x32_bf16 v[108:111], a[232:235], a[44:47], v[108:111]\n"
    "ds_read_b64_tr_b16 a[248:249], v245 offset:9472\n"
    "v_mul_f32 v215, v225, v231\n"
    "v_mfma_f32_16x16x32_bf16 v[108:111], a[236:239], a[60:63], v[108:111]\n"
    "v_fmac_f32 v8, v226, v232\n"
    "v_fmac_f32 v9, v226, v233\n"
    "v_mfma_f32_16x16x32_bf16 v[104:107], a[236:239], a[56:59], v[104:107]\n"
    "ds_read_b64_tr_b16 a[250:251], v245 offset:14080\n"
    "v_fmac_f32 v10, v226, v234\n"
    "v_mfma_f32_16x16x32_bf16 v[104:107], a[232:235], a[40:43], v[104:107]\n"
    "v_fmac_f32 v11, v226, v235\n"
    "v_mul_f32 v216, v226, v228\n"
    "v_mfma_f32_16x16x32_bf16 v[100:103], a[232:235], a[36:39], v[100:103]\n"
    "ds_read_b64_tr_b16 a[252:253], v245 offset:18688\n"
    "v_mul_f32 v217, v226, v229\n"
    "v_mfma_f32_16x16x32_bf16 v[100:103], a[236:239], a[52:55], v[100:103]\n"
    "v_mul_f32 v218, v226, v230\n"
    "v_mul_f32 v219, v226, v231\n"
    "v_mfma_f32_16x16x32_bf16 v[96:99], a[236:239], a[48:51], v[96:99]\n"
    "ds_read_b64_tr_b16 a[254:255], v245 offset:23296\n"
    "v_fmac_f32 v12, v227, v232\n"
    "v_mfma_f32_16x16x32_bf16 v[96:99], a[232:235], a[32:35], v[96:99]\n"
    "s_waitcnt lgkmcnt(4)\n"
    "v_fmac_f32 v13, v227, v233\n"
    "v_fmac_f32 v14, v227, v234\n"
    "v_mfma_f32_16x16x32_bf16 v[112:115], a[240:243], a[32:35], v[112:115]\n"
    "ds_read_b64_tr_b16 a[224:225], v245 offset:27648\n"
    "v_fmac_f32 v15, v227, v235\n"
    "v_mfma_f32_16x16x32_bf16 v[112:115], a[244:247], a[48:51], v[112:115]\n"
    "v_mul_f32 v220, v227, v228\n"
    "v_mul_f32 v221, v227, v229\n"
    "v_mfma_f32_16x16x32_bf16 v[116:119], a[244:247], a[52:55], v[116:119]\n"
    "ds_read_b64_tr_b16 a[226:227], v245 offset:32256\n"
    "v_mul_f32 v222, v227, v230\n"
    "v_mfma_f32_16x16x32_bf16 v[116:119], a[240:243], a[36:39], v[116:119]\n"
    "v_mul_f32 v223, v227, v231\n"
    "v_mfma_f32_16x16x32_bf16 v[120:123], a[240:243], a[40:43], v[120:123]\n"
    "ds_read_b64_tr_b16 a[228:229], v245 offset:36864\n"
    "v_mfma_f32_16x16x32_bf16 v[120:123], a[244:247], a[56:59], v[120:123]\n"
    "v_mfma_f32_16x16x32_bf16 v[124:127], a[244:247], a[60:63], v[124:127]\n"
    "ds_read_b64_tr_b16 a[230:231], v245 offset:41472\n"
    "v_mfma_f32_16x16x32_bf16 v[124:127], a[240:243], a[44:47], v[124:127]\n"
    "s_waitcnt lgkmcnt(4)\n"
    "v_mfma_f32_16x16x32_bf16 v[140:143], a[248:251], a[44:47], v[140:143]\n"
    "s_waitcnt vmcnt(0)\n"
    "ds_read_b64_tr_b16 a[232:233], v245 offset:27680\n"
    "v_cmp_ne_u32 vcc, s66, v248\n"
    "v_mfma_f32_16x16x32_bf16 v[140:143], a[252:255], a[60:63], v[140:143]\n"
    "s_nop 0\n"
    "s_cbranch_vccnz RT5_%=\n"
    "RB5_%=:\n"
    "v_mfma_f32_16x16x32_bf16 v[136:139], a[252:255], a[56:59], v[136:139]\n"
    "s_add_u32 s57, s75, 0\n"
    "ds_read_b64_tr_b16 a[234:235], v245 offset:32288\n"
    "v_mfma_f32_16x16x32_bf16 v[136:139], a[248:251], a[40:43], v[136:139]\n"
    "buffer_load_dwordx4 v[144:147], v254, s[20:23], s57 offen offset:0 sc1\n"
    "v_mfma_f32_16x16x32_bf16 v[132:135], a[248:251], a[36:39], v[132:135]\n"
    "ds_read_b64_tr_b16 a[236:237], v245 offset:36896\n"
    "v_mfma_f32_16x16x32_bf16 v[132:135], a[252:255], a[52:55], v[132:135]\n"
    "v_mfma_f32_16x16x32_bf16 v[128:131], a[252:255], a[48:51], v[128:131]\n"
    "ds_read_b64_tr_b16 a[238:239], v245 offset:41504\n"
    "v_mfma_f32_16x16x32_bf16 v[128:131], a[248:251], a[32:35], v[128:131]\n"
    "s_waitcnt lgkmcnt(4)\n"
    "buffer_load_dwordx4 v[148:151], v254, s[20:23], s57 offen offset:1024 sc1\n"
    "v_mfma_f32_16x16x32_bf16 v[0:3], a[224:227], a[64:67], v[0:3]\n"
    "ds_read_b64_tr_b16 a[240:241], v245 offset:27712\n"
    "v_mfma_f32_16x16x32_bf16 v[0:3], a[228:231], a[80:83], v[0:3]\n"
    "v_mfma_f32_16x16x32_bf16 v[4:7], a[228:231], a[84:87], v[4:7]\n"
    "ds_read_b64_tr_b16 a[242:243], v245 offset:32320\n"
    "v_bfe_u32 v224, v255, 4, 1\n"
    "v_mfma_f32_16x16x32_bf16 v[4:7], a[224:227], a[68:71], v[4:7]\n"
    "buffer_load_dwordx4 v[152:155], v254, s[20:23], s57 offen offset:2048 sc1\n"
    "v_mfma_f32_16x16x32_bf16 v[8:11], a[224:227], a[72:75], v[8:11]\n"
    "ds_read_b64_tr_b16 a[244:245], v245 offset:36928\n"
    "v_bfe_u32 v225, v255, 2, 2\n"
    "v_mfma_f32_16x16x32_bf16 v[8:11], a[228:231], a[88:91], v[8:11]\n"
    "v_mad_u32_u24 v225, v224, 36, v225\n"
    "v_mul_u32_u24 v225, 288, v225\n"
    "v_mfma_f32_16x16x32_bf16 v[12:15], a[228:231], a[92:95], v[12:15]\n"
    "ds_read_b64_tr_b16 a[246:247], v245 offset:41536\n"
    "v_and_b32 v226, 3, v255\n"
    "v_mfma_f32_16x16x32_bf16 v[12:15], a[224:227], a[76:79], v[12:15]\n"
    "s_waitcnt lgkmcnt(4)\n"
    "buffer_load_dwordx4 v[156:159], v254, s[20:23], s57 offen offset:3072 sc1\n"
    "v_mfma_f32_16x16x32_bf16 v[28:31], a[232:235], a[76:79], v[28:31]\n"
    "s_add_u32 s57, s75, 4096\n"
    "ds_read_b64_tr_b16 a[248:249], v245 offset:27744\n"
    "v_lshl_add_u32 v225, v226, 3, v225\n"
    "v_mfma_f32_16x16x32_bf16 v[28:31], a[236:239], a[92:95], v[28:31]\n"
    "v_add_u32 v247, 4608, v225\n"
    "v_and_b32 v224, 31, v255\n"
    "v_mfma_f32_16x16x32_bf16 v[24:27], a[236:239], a[88:91], v[24:27]\n"
    "ds_read_b64_tr_b16 a[250:251], v245 offset:32352\n"
    "v_mul_u32_u24 v224, 1152, v224\n"
    "v_mfma_f32_16x16x32_bf16 v[24:27], a[232:235], a[72:75], v[24:27]\n"
    "buffer_load_dwordx4 v[160:163], v254, s[20:23], s57 offen offset:0 sc1\n"
    "v_mfma_f32_16x16x32_bf16 v[20:23], a[232:235], a[68:71], v[20:23]\n"
    "ds_read_b64_tr_b16 a[252:253], v245 offset:36960\n"
    "v_lshrrev_b32 v225, 5, v255\n"
    "v_mfma_f32_16x16x32_bf16 v[20:23], a[236:239], a[84:87], v[20:23]\n"
    "v_lshl_add_u32 v251, v225, 4, v224\n"
    "v_lshlrev_b32 v252, 3, v255\n"
    "v_mfma_f32_16x16x32_bf16 v[16:19], a[236:239], a[80:83], v[16:19]\n"
    "ds_read_b64_tr_b16 a[254:255], v245 offset:41568\n"
    "v_and_b32 v250, 0x1fff, v250\n"
    "v_mfma_f32_16x16x32_bf16 v[16:19], a[232:235], a[64:67], v[16:19]\n"
    "s_waitcnt lgkmcnt(4)\n"
    "buffer_load_dwordx4 v[164:167], v254, s[20:23], s57 offen offset:1024 sc1\n"
    "v_mfma_f32_16x16x32_bf16 v[32:35], a[240:243], a[64:67], v[32:35]\n"
    "ds_read_b64_tr_b16 a[224:225], v245 offset:27776\n"
    "v_or_b32 v250, 0x4000, v250\n"
    "v_mfma_f32_16x16x32_bf16 v[32:35], a[244:247], a[80:83], v[32:35]\n"
    "v_lshlrev_b32 v250, 16, v250\n"
    "v_mfma_f32_16x16x32_bf16 v[36:39], a[244:247], a[84:87], v[36:39]\n"
    "ds_read_b64_tr_b16 a[226:227], v245 offset:32384\n"
    "v_mfma_f32_16x16x32_bf16 v[36:39], a[240:243], a[68:71], v[36:39]\n"
    "buffer_load_dwordx4 v[168:171], v254, s[20:23], s57 offen offset:2048 sc1\n"
    "v_mfma_f32_16x16x32_bf16 v[40:43], a[240:243], a[72:75], v[40:43]\n"
    "ds_read_b64_tr_b16 a[228:229], v245 offset:36992\n"
    "v_mfma_f32_16x16x32_bf16 v[40:43], a[244:247], a[88:91], v[40:43]\n"
    "v_mfma_f32_16x16x32_bf16 v[44:47], a[244:247], a[92:95], v[44:47]\n"
    "ds_read_b64_tr_b16 a[230:231], v245 offset:41600\n"
    "v_mfma_f32_16x16x32_bf16 v[44:47], a[240:243], a[76:79], v[44:47]\n"
    "s_waitcnt lgkmcnt(4)\n"
    "buffer_load_dwordx4 v[172:175], v254, s[20:23], s57 offen offset:3072 sc1\n"
    "v_mfma_f32_16x16x32_bf16 v[60:63], a[248:251], a[76:79], v[60:63]\n"
    "s_add_u32 s57, s75, 8192\n"
    "ds_read_b64_tr_b16 a[232:233], v245 offset:27808\n"
    "v_mfma_f32_16x16x32_bf16 v[60:63], a[252:255], a[92:95], v[60:63]\n"
    "v_mfma_f32_16x16x32_bf16 v[56:59], a[252:255], a[88:91], v[56:59]\n"
    "ds_read_b64_tr_b16 a[234:235], v245 offset:32416\n"
    "v_mfma_f32_16x16x32_bf16 v[56:59], a[248:251], a[72:75], v[56:59]\n"
    "buffer_load_dwordx4 v[176:179], v254, s[20:23], s57 offen offset:0 sc1\n"
    "v_mfma_f32_16x16x32_bf16 v[52:55], a[248:251], a[68:71], v[52:55]\n"
    "ds_read_b64_tr_b16 a[236:237], v245 offset:37024\n"
    "v_mfma_f32_16x16x32_bf16 v[52:55], a[252:255], a[84:87], v[52:55]\n"
    "v_mfma_f32_16x16x32_bf16 v[48:51], a[252:255], a[80:83], v[48:51]\n"
    "ds_read_b64_tr_b16 a[238:239], v245 offset:41632\n"
    "v_mfma_f32_16x16x32_bf16 v[48:51], a[248:251], a[64:67], v[48:51]\n"
    "s_waitcnt lgkmcnt(4)\n"
    "buffer_load_dwordx4 v[180:183], v254, s[20:23], s57 offen offset:1024 sc1\n"
    "v_mfma_f32_16x16x32_bf16 v[64:67], a[224:227], a[64:67], v[64:67]\n"
    "ds_read_b64_tr_b16 a[240:241], v245 offset:27840\n"
    "v_mfma_f32_16x16x32_bf16 v[64:67], a[228:231], a[80:83], v[64:67]\n"
    "v_mfma_f32_16x16x32_bf16 v[68:71], a[228:231], a[84:87], v[68:71]\n"
    "ds_read_b64_tr_b16 a[242:243], v245 offset:32448\n"
    "v_mfma_f32_16x16x32_bf16 v[68:71], a[224:227], a[68:71], v[68:71]\n"
    "buffer_load_dwordx4 v[184:187], v254, s[20:23], s57 offen offset:2048 sc1\n"
    "v_mfma_f32_16x16x32_bf16 v[72:75], a[224:227], a[72:75], v[72:75]\n"
    "ds_read_b64_tr_b16 a[244:245], v245 offset:37056\n"
    "v_mfma_f32_16x16x32_bf16 v[72:75], a[228:231], a[88:91], v[72:75]\n"
    "v_mfma_f32_16x16x32_bf16 v[76:79], a[228:231], a[92:95], v[76:79]\n"
    "ds_read_b64_tr_b16 a[246:247], v245 offset:41664\n"
    "v_mfma_f32_16x16x32_bf16 v[76:79], a[224:227], a[76:79], v[76:79]\n"
    "s_waitcnt lgkmcnt(4)\n"
    "buffer_load_dwordx4 v[188:191], v254, s[20:23], s57 offen offset:3072 sc1\n"
    "v_mfma_f32_16x16x32_bf16 v[92:95], a[232:235], a[76:79], v[92:95]\n"
    "ds_read_b64_tr_b16 a[248:249], v245 offset:27872\n"
    "v_mfma_f32_16x16x32_bf16 v[92:95], a[236:239], a[92:95], v[92:95]\n"
    "v_mfma_f32_16x16x32_bf16 v[88:91], a[236:239], a[88:91], v[88:91]\n"
    "ds_read_b64_tr_b16 a[250:251], v245 offset:32480\n"
    "v_mfma_f32_16x16x32_bf16 v[88:91], a[232:235], a[72:75], v[88:91]\n"
    "v_mfma_f32_16x16x32_bf16 v[84:87], a[232:235], a[68:71], v[84:87]\n"
    "ds_read_b64_tr_b16 a[252:253], v245 offset:37088\n"
    "v_mfma_f32_16x16x32_bf16 v[84:87], a[236:239], a[84:87], v[84:87]\n"
    "v_mfma_f32_16x16x32_bf16 v[80:83], a[236:239], a[80:83], v[80:83]\n"
    "ds_read_b64_tr_b16 a[254:255], v245 offset:41696\n"
    "v_mfma_f32_16x16x32_bf16 v[80:83], a[232:235], a[64:67], v[80:83]\n"
    "s_waitcnt lgkmcnt(4)\n"
    "v_mfma_f32_16x16x32_bf16 v[96:99], a[240:243], a[64:67], v[96:99]\n"
    "ds_read_b64_tr_b16 a[224:225], v245 offset:27904\n"
    "v_mfma_f32_16x16x32_bf16 v[96:99], a[244:247], a[80:83], v[96:99]\n"
    "v_mfma_f32_16x16x32_bf16 v[100:103], a[244:247], a[84:87], v[100:103]\n"
    "ds_read_b64_tr_b16 a[226:227], v245 offset:32512\n"
    "v_mfma_f32_16x16x32_bf16 v[100:103], a[240:243], a[68:71], v[100:103]\n"
    "v_mfma_f32_16x16x32_bf16 v[104:107], a[240:243], a[72:75], v[104:107]\n"
    "ds_read_b64_tr_b16 a[228:229], v245 offset:37120\n"
    "v_mfma_f32_16x16x32_bf16 v[104:107], a[244:247], a[88:91], v[104:107]\n"
    "v_mfma_f32_16x16x32_bf16 v[108:111], a[244:247], a[92:95], v[108:111]\n"
    "ds_read_b64_tr_b16 a[230:231], v245 offset:41728\n"
    "v_mfma_f32_16x16x32_bf16 v[108:111], a[240:243], a[76:79], v[108:111]\n"
    "s_waitcnt lgkmcnt(4)\n"
    "v_mfma_f32_16x16x32_bf16 v[124:127], a[248:251], a[76:79], v[124:127]\n"
    "s_barrier\n"
    "ds_read_b64_tr_b16 a[232:233], v245 offset:46080\n"
    "v_mfma_f32_16x16x32_bf16 v[124:127], a[252:255], a[92:95], v[124:127]\n"
    "v_mfma_f32_16x16x32_bf16 v[120:123], a[252:255], a[88:91], v[120:123]\n"
    "ds_read_b64_tr_b16 a[234:235], v245 offset:50688\n"
    "v_mfma_f32_16x16x32_bf16 v[120:123], a[248:251], a[72:75], v[120:123]\n"
    "v_mfma_f32_16x16x32_bf16 v[116:119], a[248:251], a[68:71], v[116:119]\n"
    "ds_read_b64_tr_b16 a[236:237], v246 offset:0\n"
    "v_mfma_f32_16x16x32_bf16 v[116:119], a[252:255], a[84:87], v[116:119]\n"
    "v_mfma_f32_16x16x32_bf16 v[112:115], a[252:255], a[80:83], v[112:115]\n"
    "ds_read_b64_tr_b16 a[238:239], v246 offset:4608\n"
    "v_mfma_f32_16x16x32_bf16 v[112:115], a[248:251], a[64:67], v[112:115]\n"
    "s_waitcnt lgkmcnt(4)\n"
    "v_mfma_f32_16x16x32_bf16 v[128:131], a[224:227], a[64:67], v[128:131]\n"
    "ds_read_b64_tr_b16 a[240:241], v246 offset:9216\n"
    "v_mfma_f32_16x16x32_bf16 v[128:131], a[228:231], a[80:83], v[128:131]\n"
    "v_mfma_f32_16x16x32_bf16 v[132:135], a[228:231], a[84:87], v[132:135]\n"
    "ds_read_b64_tr_b16 a[242:243], v246 offset:13824\n"
    "v_mfma_f32_16x16x32_bf16 v[132:135], a[224:227], a[68:71], v[132:135]\n"
    "v_mfma_f32_16x16x32_bf16 v[136:139], a[224:227], a[72:75], v[136:139]\n"
    "ds_read_b64_tr_b16 a[244:245], v246 offset:18432\n"
    "v_mfma_f32_16x16x32_bf16 v[136:139], a[228:231], a[88:91], v[136:139]\n"
    "v_mfma_f32_16x16x32_bf16 v[140:143], a[228:231], a[92:95], v[140:143]\n"
    "ds_read_b64_tr_b16 a[246:247], v246 offset:23040\n"
    "v_mfma_f32_16x16x32_bf16 v[140:143], a[224:227], a[76:79], v[140:143]\n"
    "s_waitcnt lgkmcnt(4)\n"
    "JOIN_%=:\n"
    "v_mfma_f32_16x16x32_bf16 v[0:3], a[232:235], a[96:99], v[0:3]\n"
    "ds_read_b64_tr_b16 a[248:249], v246 offset:27648\n"
    "v_mfma_f32_16x16x32_bf16 v[0:3], a[236:239], a[112:115], v[0:3]\n"
    "v_mfma_f32_16x16x32_bf16 v[4:7], a[236:239], a[116:119], v[4:7]\n"
    "ds_read_b64_tr_b16 a[250:251], v246 offset:32256\n"
    "v_mfma_f32_16x16x32_bf16 v[4:7], a[232:235], a[100:103], v[4:7]\n"
    "v_mfma_f32_16x16x32_bf16 v[8:11], a[232:235], a[104:107], v[8:11]\n"
    "ds_read_b64_tr_b16 a[252:253], v246 offset:36864\n"
    "v_mfma_f32_16x16x32_bf16 v[8:11], a[236:239], a[120:123], v[8:11]\n"
    "v_mfma_f32_16x16x32_bf16 v[12:15], a[236:239], a[124:127], v[12:15]\n"
    "ds_read_b64_tr_b16 a[254:255], v246 offset:41472\n"
    "v_mfma_f32_16x16x32_bf16 v[12:15], a[232:235], a[108:111], v[12:15]\n"
    "s_waitcnt lgkmcnt(4)\n"
    "s_waitcnt vmcnt(32)\n"
    "v_mfma_f32_16x16x32_bf16 v[12:15], a[240:243], a[140:143], v[12:15]\n"
    "ds_read_b64_tr_b16 a[224:225], v244 offset:27648\n"
    "s_waitcnt vmcnt(28)\n"
    "v_mfma_f32_16x16x32_bf16 v[12:15], a[244:247], a[156:159], v[12:15]\n"
    "v_mfma_f32_16x16x32_bf16 v[8:11], a[244:247], a[152:155], v[8:11]\n"
    "ds_read_b64_tr_b16 a[226:227], v244 offset:32256\n"
    "v_mfma_f32_16x16x32_bf16 v[8:11], a[240:243], a[136:139], v[8:11]\n"
    "v_mfma_f32_16x16x32_bf16 v[4:7], a[240:243], a[132:135], v[4:7]\n"
    "ds_read_b64_tr_b16 a[228:229], v244 offset:36864\n"
    "v_mfma_f32_16x16x32_bf16 v[4:7], a[244:247], a[148:151], v[4:7]\n"
    "v_mfma_f32_16x16x32_bf16 v[0:3], a[244:247], a[144:147], v[0:3]\n"
    "ds_read_b64_tr_b16 a[230:231], v244 offset:41472\n"
    "v_mfma_f32_16x16x32_bf16 v[0:3], a[240:243], a[128:131], v[0:3]\n"
    "s_waitcnt lgkmcnt(4)\n"
    "s_waitcnt vmcnt(27)\n"
    "v_mfma_f32_16x16x32_bf16 v[0:3], a[248:251], a[160:163], v[0:3]\n"
    "ds_read_b64_tr_b16 a[232:233], v244 offset:0\n"
    "s_waitcnt vmcnt(23)\n"
    "v_mfma_f32_16x16x32_bf16 v[0:3], a[252:255], a[176:179], v[0:3]\n"
    "s_waitcnt vmcnt(22)\n"
    "v_mfma_f32_16x16x32_bf16 v[4:7], a[252:255], a[180:183], v[4:7]\n"
    "ds_read_b64_tr_b16 a[234:235], v244 offset:5760\n"
    "v_mfma_f32_16x16x32_bf16 v[4:7], a[248:251], a[164:167], v[4:7]\n"
    "v_mfma_f32_16x16x32_bf16 v[8:11], a[248:251], a[168:171], v[8:11]\n"
    "ds_read_b64_tr_b16 a[236:237], v244 offset:10368\n"
    "s_waitcnt vmcnt(21)\n"
    "v_mfma_f32_16x16x32_bf16 v[8:11], a[252:255], a[184:187], v[8:11]\n"
    "s_waitcnt vmcnt(20)\n"
    "v_mfma_f32_16x16x32_bf16 v[12:15], a[252:255], a[188:191], v[12:15]\n"
    "ds_read_b64_tr_b16 a[238:239], v244 offset:16128\n"
    "v_mfma_f32_16x16x32_bf16 v[12:15], a[248:251], a[172:175], v[12:15]\n"
    "s_waitcnt lgkmcnt(4)\n"
    "s_waitcnt vmcnt(16)\n"
    "v_mfma_f32_16x16x32_bf16 v[12:15], a[224:227], a[204:207], v[12:15]\n"
    "ds_read_b64_tr_b16 a[240:241], v247 offset:0\n"
    "s_waitcnt vmcnt(12)\n"
    "v_mfma_f32_16x16x32_bf16 v[12:15], a[228:231], a[220:223], v[12:15]\n"
    "v_mfma_f32_16x16x32_bf16 v[8:11], a[228:231], a[216:219], v[8:11]\n"
    "v_mfma_f32_16x16x32_bf16 v[8:11], a[224:227], a[200:203], v[8:11]\n"
    "v_mfma_f32_16x16x32_bf16 v[4:7], a[224:227], a[196:199], v[4:7]\n"
    "ds_read_b64_tr_b16 a[244:245], v245 offset:46112\n"
    "v_mfma_f32_16x16x32_bf16 v[4:7], a[228:231], a[212:215], v[4:7]\n"
    "v_mfma_f32_16x16x32_bf16 v[0:3], a[228:231], a[208:211], v[0:3]\n"
    "ds_read_b64_tr_b16 a[246:247], v245 offset:50720\n"
    "v_mfma_f32_16x16x32_bf16 v[0:3], a[224:227], a[192:195], v[0:3]\n"
    "s_waitcnt lgkmcnt(2)\n"
    "s_waitcnt vmcnt(11)\n"
    "v_mfma_f32_16x16x32_bf16 v[0:3], a[232:235], v[144:147], v[0:3]\n"
    "ds_read_b64_tr_b16 a[248:249], v246 offset:32\n"
    "s_waitcnt vmcnt(7)\n"
    "v_mfma_f32_16x16x32_bf16 v[0:3], a[236:239], v[160:163], v[0:3]\n"
    "s_waitcnt vmcnt(3)\n"
    "v_mfma_f32_16x16x32_bf16 v[0:3], a[240:243], v[176:179], v[0:3]\n"
    "ds_read_b64_tr_b16 a[250:251], v246 offset:4640\n"
    "s_waitcnt vmcnt(2)\n"
    "v_mfma_f32_16x16x32_bf16 v[4:7], a[240:243], v[180:183], v[4:7]\n"
    "v_mfma_f32_16x16x32_bf16 v[4:7], a[236:239], v[164:167], v[4:7]\n"
    "ds_read_b64_tr_b16 a[252:253], v246 offset:9248\n"
    "v_mfma_f32_16x16x32_bf16 v[4:7], a[232:235], v[148:151], v[4:7]\n"
    "v_mfma_f32_16x16x32_bf16 v[8:11], a[232:235], v[152:155], v[8:11]\n"
    "ds_read_b64_tr_b16 a[254:255], v246 offset:13856\n"
    "v_mfma_f32_16x16x32_bf16 v[8:11], a[236:239], v[168:171], v[8:11]\n"
    "s_waitcnt vmcnt(1)\n"
    "v_mfma_f32_16x16x32_bf16 v[8:11], a[240:243], v[184:187], v[8:11]\n"
    "ds_read_b64_tr_b16 a[224:225], v246 offset:18464\n"
    "s_waitcnt vmcnt(0)\n"
    "v_mfma_f32_16x16x32_bf16 v[12:15], a[240:243], v[188:191], v[12:15]\n"
    "v_mfma_f32_16x16x32_bf16 v[12:15], a[236:239], v[172:175], v[12:15]\n"
    "ds_read_b64_tr_b16 a[226:227], v246 offset:23072\n"
    "v_mfma_f32_16x16x32_bf16 v[12:15], a[232:235], v[156:159], v[12:15]\n"
    "s_waitcnt lgkmcnt(4)\n"
    "v_mfma_f32_16x16x32_bf16 v[28:31], a[244:247], a[108:111], v[28:31]\n"
    "ds_read_b64_tr_b16 a[228:229], v246 offset:27680\n"
    "v_mfma_f32_16x16x32_bf16 v[28:31], a[248:251], a[124:127], v[28:31]\n"
    "v_mfma_f32_16x16x32_bf16 v[24:27], a[248:251], a[120:123], v[24:27]\n"
    "s_add_u32 s56, s60, 81920\n"
    "ds_read_b64_tr_b16 a[230:231], v246 offset:32288\n"
    "v_cvt_pk_bf16_f32 v0, v0, v1\n"
    "v_mfma_f32_16x16x32_bf16 v[24:27], a[244:247], a[104:107], v[24:27]\n"
    "v_cvt_pk_bf16_f32 v1, v2, v3\n"
    "v_cvt_pk_bf16_f32 v2, v4, v5\n"
    "v_mfma_f32_16x16x32_bf16 v[20:23], a[244:247], a[100:103], v[20:23]\n"
    "ds_read_b64_tr_b16 a[232:233], v246 offset:36896\n"
    "v_cvt_pk_bf16_f32 v3, v6, v250\n"
    "v_mfma_f32_16x16x32_bf16 v[20:23], a[248:251], a[116:119], v[20:23]\n"
    "v_cvt_pk_bf16_f32 v4, v7, v250\n"
    "v_cvt_pk_bf16_f32 v8, v8, v9\n"
    "v_mfma_f32_16x16x32_bf16 v[16:19], a[248:251], a[112:115], v[16:19]\n"
    "ds_read_b64_tr_b16 a[234:235], v246 offset:41504\n"
    "v_cvt_pk_bf16_f32 v9, v10, v11\n"
    "v_mfma_f32_16x16x32_bf16 v[16:19], a[244:247], a[96:99], v[16:19]\n"
    "s_waitcnt lgkmcnt(4)\n"
    "v_cvt_pk_bf16_f32 v10, v12, v13\n"
    "v_cvt_pk_bf16_f32 v11, v14, v250\n"
    "v_mfma_f32_16x16x32_bf16 v[16:19], a[252:255], a[128:131], v[16:19]\n"
    "ds_read_b64_tr_b16 a[236:237], v244 offset:27680\n"
    "v_cvt_pk_bf16_f32 v5, v15, v250\n"
    "v_mfma_f32_16x16x32_bf16 v[16:19], a[224:227], a[144:147], v[16:19]\n"
    "buffer_store_dwordx4 v[0:3], v254, s[20:23], s60 offen offset:0 sc1\n"
    "v_mfma_f32_16x16x32_bf16 v[20:23], a[224:227], a[148:151], v[20:23]\n"
    "ds_read_b64_tr_b16 a[238:239], v244 offset:32288\n"
    "v_mfma_f32_16x16x32_bf16 v[20:23], a[252:255], a[132:135], v[20:23]\n"
    "buffer_store_dwordx4 v[8:11], v254, s[20:23], s56 offen offset:0 sc1\n"
    "v_mfma_f32_16x16x32_bf16 v[24:27], a[252:255], a[136:139], v[24:27]\n"
    "ds_read_b64_tr_b16 a[240:241], v244 offset:36896\n"
    "v_mfma_f32_16x16x32_bf16 v[24:27], a[224:227], a[152:155], v[24:27]\n"
    "buffer_store_dword v4, v249, s[20:23], s60 offen offset:1024 sc1\n"
    "v_mfma_f32_16x16x32_bf16 v[28:31], a[224:227], a[156:159], v[28:31]\n"
    "ds_read_b64_tr_b16 a[242:243], v244 offset:41504\n"
    "v_mfma_f32_16x16x32_bf16 v[28:31], a[252:255], a[140:143], v[28:31]\n"
    "s_waitcnt lgkmcnt(4)\n"
    "buffer_store_dword v5, v249, s[20:23], s56 offen offset:1024 sc1\n"
    "v_mfma_f32_16x16x32_bf16 v[28:31], a[228:231], a[172:175], v[28:31]\n"
    "ds_read_b64_tr_b16 a[244:245], v244 offset:32\n"
    "v_mfma_f32_16x16x32_bf16 v[28:31], a[232:235], a[188:191], v[28:31]\n"
    "v_mfma_f32_16x16x32_bf16 v[24:27], a[232:235], a[184:187], v[24:27]\n"
    "ds_read_b64_tr_b16 a[246:247], v244 offset:5792\n"
    "v_mfma_f32_16x16x32_bf16 v[24:27], a[228:231], a[168:171], v[24:27]\n"
    "v_mfma_f32_16x16x32_bf16 v[20:23], a[228:231], a[164:167], v[20:23]\n"
    "ds_read_b64_tr_b16 a[248:249], v244 offset:10400\n"
    "v_mfma_f32_16x16x32_bf16 v[20:23], a[232:235], a[180:183], v[20:23]\n"
    "v_mfma_f32_16x16x32_bf16 v[16:19], a[232:235], a[176:179], v[16:19]\n"
    "ds_read_b64_tr_b16 a[250:251], v244 offset:16160\n"
    "v_mfma_f32_16x16x32_bf16 v[16:19], a[228:231], a[160:163], v[16:19]\n"
    "s_waitcnt lgkmcnt(4)\n"
    "v_mfma_f32_16x16x32_bf16 v[16:19], a[236:239], a[192:195], v[16:19]\n"
    "ds_read_b64_tr_b16 a[252:253], v247 offset:32\n"
    "v_mfma_f32_16x16x32_bf16 v[16:19], a[240:243], a[208:211], v[16:19]\n"
    "v_mfma_f32_16x16x32_bf16 v[20:23], a[240:243], a[212:215], v[20:23]\n"
    "v_mfma_f32_16x16x32_bf16 v[20:23], a[236:239], a[196:199], v[20:23]\n"
    "v_mfma_f32_16x16x32_bf16 v[24:27], a[236:239], a[200:203], v[24:27]\n"
    "ds_read_b64_tr_b16 a[224:225], v245 offset:46144\n"
    "v_mfma_f32_16x16x32_bf16 v[24:27], a[240:243], a[216:219], v[24:27]\n"
    "v_mfma_f32_16x16x32_bf16 v[28:31], a[240:243], a[220:223], v[28:31]\n"
    "ds_read_b64_tr_b16 a[226:227], v245 offset:50752\n"
    "v_mfma_f32_16x16x32_bf16 v[28:31], a[236:239], a[204:207], v[28:31]\n"
    "s_waitcnt lgkmcnt(2)\n"
    "v_mfma_f32_16x16x32_bf16 v[28:31], a[244:247], v[156:159], v[28:31]\n"
    "ds_read_b64_tr_b16 a[228:229], v246 offset:64\n"
    "v_mfma_f32_16x16x32_bf16 v[28:31], a[248:251], v[172:175], v[28:31]\n"
    "v_mfma_f32_16x16x32_bf16 v[28:31], a[252:255], v[188:191], v[28:31]\n"
    "ds_read_b64_tr_b16 a[230:231], v246 offset:4672\n"
    "v_mfma_f32_16x16x32_bf16 v[24:27], a[252:255], v[184:187], v[24:27]\n"
    "v_mfma_f32_16x16x32_bf16 v[24:27], a[248:251], v[168:171], v[24:27]\n"
    "s_add_u32 s82, s4, 1\n"
    "ds_read_b64_tr_b16 a[232:233], v246 offset:9280\n"
    "v_mfma_f32_16x16x32_bf16 v[24:27], a[244:247], v[152:155], v[24:27]\n"
    "s_and_b32 s82, s82, 7\n"
    "v_mfma_f32_16x16x32_bf16 v[20:23], a[244:247], v[148:151], v[20:23]\n"
    "s_mul_i32 s82, s82, 81920\n"
    "ds_read_b64_tr_b16 a[234:235], v246 offset:13888\n"
    "v_mfma_f32_16x16x32_bf16 v[20:23], a[248:251], v[164:167], v[20:23]\n"
    "s_add_u32 s82, s82, s58\n"
    "v_mfma_f32_16x16x32_bf16 v[20:23], a[252:255], v[180:183], v[20:23]\n"
    "s_add_u32 s56, s82, 0\n"
    "ds_read_b64_tr_b16 a[236:237], v246 offset:18496\n"
    "v_mfma_f32_16x16x32_bf16 v[16:19], a[252:255], v[176:179], v[16:19]\n"
    "v_mfma_f32_16x16x32_bf16 v[16:19], a[248:251], v[160:163], v[16:19]\n"
    "ds_read_b64_tr_b16 a[238:239], v246 offset:23104\n"
    "v_mfma_f32_16x16x32_bf16 v[16:19], a[244:247], v[144:147], v[16:19]\n"
    "s_waitcnt lgkmcnt(4)\n"
    "v_mfma_f32_16x16x32_bf16 v[32:35], a[224:227], a[96:99], v[32:35]\n"
    "ds_read_b64_tr_b16 a[240:241], v246 offset:27712\n"
    "v_mfma_f32_16x16x32_bf16 v[32:35], a[228:231], a[112:115], v[32:35]\n"
    "v_mfma_f32_16x16x32_bf16 v[36:39], a[228:231], a[116:119], v[36:39]\n"
    "ds_read_b64_tr_b16 a[242:243], v246 offset:32320\n"
    "s_nop 1\n"
    "v_cvt_pk_bf16_f32 v16, v16, v17\n"
    "v_mfma_f32_16x16x32_bf16 v[36:39], a[224:227], a[100:103], v[36:39]\n"
    "v_cvt_pk_bf16_f32 v17, v18, v19\n"
    "v_cvt_pk_bf16_f32 v18, v20, v21\n"
    "v_mfma_f32_16x16x32_bf16 v[40:43], a[224:227], a[104:107], v[40:43]\n"
    "ds_read_b64_tr_b16 a[244:245], v246 offset:36928\n"
    "v_cvt_pk_bf16_f32 v19, v22, v250\n"
    "v_mfma_f32_16x16x32_bf16 v[40:43], a[228:231], a[120:123], v[40:43]\n"
    "v_cvt_pk_bf16_f32 v20, v23, v250\n"
    "v_cvt_pk_bf16_f32 v24, v24, v25\n"
    "v_mfma_f32_16x16x32_bf16 v[44:47], a[228:231], a[124:127], v[44:47]\n"
    "ds_read_b64_tr_b16 a[246:247], v246 offset:41536\n"
    "v_cvt_pk_bf16_f32 v25, v26, v27\n"
    "v_mfma_f32_16x16x32_bf16 v[44:47], a[224:227], a[108:111], v[44:47]\n"
    "s_waitcnt lgkmcnt(4)\n"
    "v_cvt_pk_bf16_f32 v26, v28, v29\n"
    "v_cvt_pk_bf16_f32 v27, v30, v250\n"
    "v_mfma_f32_16x16x32_bf16 v[44:47], a[232:235], a[140:143], v[44:47]\n"
    "ds_read_b64_tr_b16 a[248:249], v244 offset:27712\n"
    "v_cvt_pk_bf16_f32 v21, v31, v250\n"
    "v_mfma_f32_16x16x32_bf16 v[44:47], a[236:239], a[156:159], v[44:47]\n"
    "buffer_store_dwordx4 v[16:19], v254, s[20:23], s56 offen offset:0 sc1\n"
    "v_mfma_f32_16x16x32_bf16 v[40:43], a[236:239], a[152:155], v[40:43]\n"
    "ds_read_b64_tr_b16 a[250:251], v244 offset:32320\n"
    "v_mfma_f32_16x16x32_bf16 v[40:43], a[232:235], a[136:139], v[40:43]\n"
    "buffer_store_dwordx4 v[24:27], v254, s[20:23], s56 offen offset:1024 sc1\n"
    "v_mfma_f32_16x16x32_bf16 v[36:39], a[232:235], a[132:135], v[36:39]\n"
    "ds_read_b64_tr_b16 a[252:253], v244 offset:36928\n"
    "v_mfma_f32_16x16x32_bf16 v[36:39], a[236:239], a[148:151], v[36:39]\n"
    "buffer_store_dwordx2 v[20:21], v252, s[20:23], s56 offen offset:2048 sc1\n"
    "v_mfma_f32_16x16x32_bf16 v[32:35], a[236:239], a[144:147], v[32:35]\n"
    "ds_read_b64_tr_b16 a[254:255], v244 offset:41536\n"
    "v_mfma_f32_16x16x32_bf16 v[32:35], a[232:235], a[128:131], v[32:35]\n"
    "s_waitcnt lgkmcnt(4)\n"
    "v_mfma_f32_16x16x32_bf16 v[32:35], a[240:243], a[160:163], v[32:35]\n"
    "ds_read_b64_tr_b16 a[224:225], v244 offset:64\n"
    "v_mfma_f32_16x16x32_bf16 v[32:35], a[244:247], a[176:179], v[32:35]\n"
    "v_mfma_f32_16x16x32_bf16 v[36:39], a[244:247], a[180:183], v[36:39]\n"
    "ds_read_b64_tr_b16 a[226:227], v244 offset:5824\n"
    "v_mfma_f32_16x16x32_bf16 v[36:39], a[240:243], a[164:167], v[36:39]\n"
    "v_mfma_f32_16x16x32_bf16 v[40:43], a[240:243], a[168:171], v[40:43]\n"
    "ds_read_b64_tr_b16 a[228:229], v244 offset:10432\n"
    "v_mfma_f32_16x16x32_bf16 v[40:43], a[244:247], a[184:187], v[40:43]\n"
    "v_mfma_f32_16x16x32_bf16 v[44:47], a[244:247], a[188:191], v[44:47]\n"
    "ds_read_b64_tr_b16 a[230:231], v244 offset:16192\n"
    "v_mfma_f32_16x16x32_bf16 v[44:47], a[240:243], a[172:175], v[44:47]\n"
    "s_waitcnt lgkmcnt(4)\n"
    "v_mfma_f32_16x16x32_bf16 v[44:47], a[248:251], a[204:207], v[44:47]\n"
    "ds_read_b64_tr_b16 a[232:233], v247 offset:64\n"
    "v_mfma_f32_16x16x32_bf16 v[44:47], a[252:255], a[220:223], v[44:47]\n"
    "v_mfma_f32_16x16x32_bf16 v[40:43], a[252:255], a[216:219], v[40:43]\n"
    "v_mfma_f32_16x16x32_bf16 v[40:43], a[248:251], a[200:203], v[40:43]\n"
    "v_mfma_f32_16x16x32_bf16 v[36:39], a[248:251], a[196:199], v[36:39]\n"
    "ds_read_b64_tr_b16 a[236:237], v245 offset:46176\n"
    "v_mfma_f32_16x16x32_bf16 v[36:39], a[252:255], a[212:215], v[36:39]\n"
    "v_mfma_f32_16x16x32_bf16 v[32:35], a[252:255], a[208:211], v[32:35]\n"
    "ds_read_b64_tr_b16 a[238:239], v245 offset:50784\n"
    "v_mfma_f32_16x16x32_bf16 v[32:35], a[248:251], a[192:195], v[32:35]\n"
    "s_waitcnt lgkmcnt(2)\n"
    "v_mfma_f32_16x16x32_bf16 v[32:35], a[224:227], v[144:147], v[32:35]\n"
    "ds_read_b64_tr_b16 a[240:241], v246 offset:96\n"
    "v_mfma_f32_16x16x32_bf16 v[32:35], a[228:231], v[160:163], v[32:35]\n"
    "v_mfma_f32_16x16x32_bf16 v[32:35], a[232:235], v[176:179], v[32:35]\n"
    "ds_read_b64_tr_b16 a[242:243], v246 offset:4704\n"
    "v_mfma_f32_16x16x32_bf16 v[36:39], a[232:235], v[180:183], v[36:39]\n"
    "v_mfma_f32_16x16x32_bf16 v[36:39], a[228:231], v[164:167], v[36:39]\n"
    "s_add_u32 s82, s4, 2\n"
    "ds_read_b64_tr_b16 a[244:245], v246 offset:9312\n"
    "v_mfma_f32_16x16x32_bf16 v[36:39], a[224:227], v[148:151], v[36:39]\n"
    "s_and_b32 s82, s82, 7\n"
    "v_mfma_f32_16x16x32_bf16 v[40:43], a[224:227], v[152:155], v[40:43]\n"
    "s_mul_i32 s82, s82, 81920\n"
    "ds_read_b64_tr_b16 a[246:247], v246 offset:13920\n"
    "v_mfma_f32_16x16x32_bf16 v[40:43], a[228:231], v[168:171], v[40:43]\n"
    "s_add_u32 s82, s82, s58\n"
    "v_mfma_f32_16x16x32_bf16 v[40:43], a[232:235], v[184:187], v[40:43]\n"
    "s_add_u32 s56, s82, 2560\n"
    "ds_read_b64_tr_b16 a[248:249], v246 offset:18528\n"
    "v_mfma_f32_16x16x32_bf16 v[44:47], a[232:235], v[188:191], v[44:47]\n"
    "s_add_u32 s57, s61, 0\n"
    "v_mfma_f32_16x16x32_bf16 v[44:47], a[228:231], v[172:175], v[44:47]\n"
    "ds_read_b64_tr_b16 a[250:251], v246 offset:23136\n"
    "v_mfma_f32_16x16x32_bf16 v[44:47], a[224:227], v[156:159], v[44:47]\n"
    "s_waitcnt lgkmcnt(4)\n"
    "v_mfma_f32_16x16x32_bf16 v[60:63], a[236:239], a[108:111], v[60:63]\n"
    "ds_read_b64_tr_b16 a[252:253], v246 offset:27744\n"
    "v_mfma_f32_16x16x32_bf16 v[60:63], a[240:243], a[124:127], v[60:63]\n"
    "buffer_load_dwordx4 v[0:3], v254, s[48:51], s57 offen offset:0 sc1\n"
    "v_mfma_f32_16x16x32_bf16 v[56:59], a[240:243], a[120:123], v[56:59]\n"
    "ds_read_b64_tr_b16 a[254:255], v246 offset:32352\n"
    "v_cvt_pk_bf16_f32 v32, v32, v33\n"
    "v_mfma_f32_16x16x32_bf16 v[56:59], a[236:239], a[104:107], v[56:59]\n"
    "v_cvt_pk_bf16_f32 v33, v34, v35\n"
    "v_cvt_pk_bf16_f32 v34, v36, v37\n"
    "v_mfma_f32_16x16x32_bf16 v[52:55], a[236:239], a[100:103], v[52:55]\n"
    "ds_read_b64_tr_b16 a[224:225], v246 offset:36960\n"
    "v_cvt_pk_bf16_f32 v35, v38, v250\n"
    "v_mfma_f32_16x16x32_bf16 v[52:55], a[240:243], a[116:119], v[52:55]\n"
    "v_cvt_pk_bf16_f32 v36, v39, v250\n"
    "v_cvt_pk_bf16_f32 v40, v40, v41\n"
    "v_mfma_f32_16x16x32_bf16 v[48:51], a[240:243], a[112:115], v[48:51]\n"
    "ds_read_b64_tr_b16 a[226:227], v246 offset:41568\n"
    "v_cvt_pk_bf16_f32 v41, v42, v43\n"
    "v_mfma_f32_16x16x32_bf16 v[48:51], a[236:239], a[96:99], v[48:51]\n"
    "s_waitcnt lgkmcnt(4)\n"
    "v_cvt_pk_bf16_f32 v42, v44, v45\n"
    "v_cvt_pk_bf16_f32 v43, v46, v250\n"
    "v_mfma_f32_16x16x32_bf16 v[48:51], a[244:247], a[128:131], v[48:51]\n"
    "ds_read_b64_tr_b16 a[228:229], v244 offset:27744\n"
    "v_cvt_pk_bf16_f32 v37, v47, v250\n"
    "v_mfma_f32_16x16x32_bf16 v[48:51], a[248:251], a[144:147], v[48:51]\n"
    "buffer_store_dwordx4 v[32:35], v254, s[20:23], s56 offen offset:0 sc1\n"
    "v_mfma_f32_16x16x32_bf16 v[52:55], a[248:251], a[148:151], v[52:55]\n"
    "ds_read_b64_tr_b16 a[230:231], v244 offset:32352\n"
    "v_mfma_f32_16x16x32_bf16 v[52:55], a[244:247], a[132:135], v[52:55]\n"
    "buffer_store_dwordx4 v[40:43], v254, s[20:23], s56 offen offset:1024 sc1\n"
    "v_mfma_f32_16x16x32_bf16 v[56:59], a[244:247], a[136:139], v[56:59]\n"
    "ds_read_b64_tr_b16 a[232:233], v244 offset:36960\n"
    "v_mfma_f32_16x16x32_bf16 v[56:59], a[248:251], a[152:155], v[56:59]\n"
    "buffer_store_dwordx2 v[36:37], v252, s[20:23], s56 offen offset:2048 sc1\n"
    "v_mfma_f32_16x16x32_bf16 v[60:63], a[248:251], a[156:159], v[60:63]\n"
    "ds_read_b64_tr_b16 a[234:235], v244 offset:41568\n"
    "v_mfma_f32_16x16x32_bf16 v[60:63], a[244:247], a[140:143], v[60:63]\n"
    "s_waitcnt lgkmcnt(4)\n"
    "buffer_load_dword v236, v249, s[48:51], s57 offen offset:1024 sc1\n"
    "v_mfma_f32_16x16x32_bf16 v[60:63], a[252:255], a[172:175], v[60:63]\n"
    "ds_read_b64_tr_b16 a[236:237], v244 offset:96\n"
    "v_mfma_f32_16x16x32_bf16 v[60:63], a[224:227], a[188:191], v[60:63]\n"
    "buffer_load_dwordx4 v[4:7], v254, s[48:51], s57 offen offset:1280 sc1\n"
    "v_mfma_f32_16x16x32_bf16 v[56:59], a[224:227], a[184:187], v[56:59]\n"
    "ds_read_b64_tr_b16 a[238:239], v244 offset:5856\n"
    "v_mfma_f32_16x16x32_bf16 v[56:59], a[252:255], a[168:171], v[56:59]\n"
    "buffer_load_dword v237, v249, s[48:51], s57 offen offset:2304 sc1\n"
    "v_mfma_f32_16x16x32_bf16 v[52:55], a[252:255], a[164:167], v[52:55]\n"
    "ds_read_b64_tr_b16 a[240:241], v244 offset:10464\n"
    "v_mfma_f32_16x16x32_bf16 v[52:55], a[224:227], a[180:183], v[52:55]\n"
    "buffer_load_dwordx4 v[8:11], v254, s[48:51], s57 offen offset:2560 sc1\n"
    "v_mfma_f32_16x16x32_bf16 v[48:51], a[224:227], a[176:179], v[48:51]\n"
    "ds_read_b64_tr_b16 a[242:243], v244 offset:16224\n"
    "v_mfma_f32_16x16x32_bf16 v[48:51], a[252:255], a[160:163], v[48:51]\n"
    "s_waitcnt lgkmcnt(4)\n"
    "buffer_load_dword v238, v249, s[48:51], s57 offen offset:3584 sc1\n"
    "v_mfma_f32_16x16x32_bf16 v[48:51], a[228:231], a[192:195], v[48:51]\n"
    "s_add_u32 s57, s61, 3840\n"
    "ds_read_b64_tr_b16 a[244:245], v247 offset:96\n"
    "v_mfma_f32_16x16x32_bf16 v[48:51], a[232:235], a[208:211], v[48:51]\n"
    "buffer_load_dwordx4 v[12:15], v254, s[48:51], s57 offen offset:0 sc1\n"
    "v_mfma_f32_16x16x32_bf16 v[52:55], a[232:235], a[212:215], v[52:55]\n"
    "v_mfma_f32_16x16x32_bf16 v[52:55], a[228:231], a[196:199], v[52:55]\n"
    "buffer_load_dword v239, v249, s[48:51], s57 offen offset:1024 sc1\n"
    "v_mfma_f32_16x16x32_bf16 v[56:59], a[228:231], a[200:203], v[56:59]\n"
    "ds_read_b64_tr_b16 a[248:249], v245 offset:46208\n"
    "v_mfma_f32_16x16x32_bf16 v[56:59], a[232:235], a[216:219], v[56:59]\n"
    "buffer_load_dwordx4 v[16:19], v254, s[48:51], s57 offen offset:1280 sc1\n"
    "v_mfma_f32_16x16x32_bf16 v[60:63], a[232:235], a[220:223], v[60:63]\n"
    "ds_read_b64_tr_b16 a[250:251], v245 offset:50816\n"
    "v_mfma_f32_16x16x32_bf16 v[60:63], a[228:231], a[204:207], v[60:63]\n"
    "s_waitcnt lgkmcnt(2)\n"
    "buffer_load_dword v240, v249, s[48:51], s57 offen offset:2304 sc1\n"
    "v_mfma_f32_16x16x32_bf16 v[60:63], a[236:239], v[156:159], v[60:63]\n"
    "ds_read_b64_tr_b16 a[252:253], v246 offset:128\n"
    "v_mfma_f32_16x16x32_bf16 v[60:63], a[240:243], v[172:175], v[60:63]\n"
    "buffer_load_dwordx4 v[20:23], v254, s[48:51], s57 offen offset:2560 sc1\n"
    "v_mfma_f32_16x16x32_bf16 v[60:63], a[244:247], v[188:191], v[60:63]\n"
    "ds_read_b64_tr_b16 a[254:255], v246 offset:4736\n"
    "v_mfma_f32_16x16x32_bf16 v[56:59], a[244:247], v[184:187], v[56:59]\n"
    "buffer_load_dword v241, v249, s[48:51], s57 offen offset:3584 sc1\n"
    "v_mfma_f32_16x16x32_bf16 v[56:59], a[240:243], v[168:171], v[56:59]\n"
    "s_add_u32 s82, s4, 3\n"
    "ds_read_b64_tr_b16 a[224:225], v246 offset:9344\n"
    "v_mfma_f32_16x16x32_bf16 v[56:59], a[236:239], v[152:155], v[56:59]\n"
    "s_and_b32 s82, s82, 7\n"
    "v_mfma_f32_16x16x32_bf16 v[52:55], a[236:239], v[148:151], v[52:55]\n"
    "s_mul_i32 s82, s82, 81920\n"
    "ds_read_b64_tr_b16 a[226:227], v246 offset:13952\n"
    "v_mfma_f32_16x16x32_bf16 v[52:55], a[240:243], v[164:167], v[52:55]\n"
    "s_add_u32 s82, s82, s58\n"
    "v_mfma_f32_16x16x32_bf16 v[52:55], a[244:247], v[180:183], v[52:55]\n"
    "s_add_u32 s56, s82, 5120\n"
    "ds_read_b64_tr_b16 a[228:229], v246 offset:18560\n"
    "v_mfma_f32_16x16x32_bf16 v[48:51], a[244:247], v[176:179], v[48:51]\n"
    "s_add_u32 s57, s61, 7680\n"
    "buffer_load_dwordx4 v[24:27], v254, s[48:51], s57 offen offset:0 sc1\n"
    "v_mfma_f32_16x16x32_bf16 v[48:51], a[240:243], v[160:163], v[48:51]\n"
    "ds_read_b64_tr_b16 a[230:231], v246 offset:23168\n"
    "v_mfma_f32_16x16x32_bf16 v[48:51], a[236:239], v[144:147], v[48:51]\n"
    "s_waitcnt lgkmcnt(4)\n"
    "buffer_load_dword v242, v249, s[48:51], s57 offen offset:1024 sc1\n"
    "v_mfma_f32_16x16x32_bf16 v[64:67], a[248:251], a[96:99], v[64:67]\n"
    "ds_read_b64_tr_b16 a[232:233], v246 offset:27776\n"
    "v_mfma_f32_16x16x32_bf16 v[64:67], a[252:255], a[112:115], v[64:67]\n"
    "buffer_load_dwordx4 v[28:31], v254, s[48:51], s57 offen offset:1280 sc1\n"
    "v_mfma_f32_16x16x32_bf16 v[68:71], a[252:255], a[116:119], v[68:71]\n"
    "ds_read_b64_tr_b16 a[234:235], v246 offset:32384\n"
    "v_cvt_pk_bf16_f32 v48, v48, v49\n"
    "v_mfma_f32_16x16x32_bf16 v[68:71], a[248:251], a[100:103], v[68:71]\n"
    "v_cvt_pk_bf16_f32 v49, v50, v51\n"
    "v_cvt_pk_bf16_f32 v50, v52, v53\n"
    "v_mfma_f32_16x16x32_bf16 v[72:75], a[248:251], a[104:107], v[72:75]\n"
    "ds_read_b64_tr_b16 a[236:237], v246 offset:36992\n"
    "v_cvt_pk_bf16_f32 v51, v54, v250\n"
    "v_mfma_f32_16x16x32_bf16 v[72:75], a[252:255], a[120:123], v[72:75]\n"
    "v_cvt_pk_bf16_f32 v52, v55, v250\n"
    "v_cvt_pk_bf16_f32 v56, v56, v57\n"
    "v_mfma_f32_16x16x32_bf16 v[76:79], a[252:255], a[124:127], v[76:79]\n"
    "ds_read_b64_tr_b16 a[238:239], v246 offset:41600\n"
    "v_cvt_pk_bf16_f32 v57, v58, v59\n"
    "v_mfma_f32_16x16x32_bf16 v[76:79], a[248:251], a[108:111], v[76:79]\n"
    "s_waitcnt lgkmcnt(4)\n"
    "v_cvt_pk_bf16_f32 v58, v60, v61\n"
    "v_cvt_pk_bf16_f32 v59, v62, v250\n"
    "v_mfma_f32_16x16x32_bf16 v[76:79], a[224:227], a[140:143], v[76:79]\n"
    "ds_read_b64_tr_b16 a[240:241], v244 offset:27776\n"
    "v_cvt_pk_bf16_f32 v53, v63, v250\n"
    "v_mfma_f32_16x16x32_bf16 v[76:79], a[228:231], a[156:159], v[76:79]\n"
    "buffer_store_dwordx4 v[48:51], v254, s[20:23], s56 offen offset:0 sc1\n"
    "v_mfma_f32_16x16x32_bf16 v[72:75], a[228:231], a[152:155], v[72:75]\n"
    "ds_read_b64_tr_b16 a[242:243], v244 offset:32384\n"
    "v_mfma_f32_16x16x32_bf16 v[72:75], a[224:227], a[136:139], v[72:75]\n"
    "buffer_store_dwordx4 v[56:59], v254, s[20:23], s56 offen offset:1024 sc1\n"
    "v_mfma_f32_16x16x32_bf16 v[68:71], a[224:227], a[132:135], v[68:71]\n"
    "ds_read_b64_tr_b16 a[244:245], v244 offset:36992\n"
    "v_mfma_f32_16x16x32_bf16 v[68:71], a[228:231], a[148:151], v[68:71]\n"
    "buffer_store_dwordx2 v[52:53], v252, s[20:23], s56 offen offset:2048 sc1\n"
    "v_mfma_f32_16x16x32_bf16 v[64:67], a[228:231], a[144:147], v[64:67]\n"
    "ds_read_b64_tr_b16 a[246:247], v244 offset:41600\n"
    "v_mfma_f32_16x16x32_bf16 v[64:67], a[224:227], a[128:131], v[64:67]\n"
    "s_waitcnt lgkmcnt(4)\n"
    "buffer_load_dword v243, v249, s[48:51], s57 offen offset:2304 sc1\n"
    "v_mfma_f32_16x16x32_bf16 v[64:67], a[232:235], a[160:163], v[64:67]\n"
    "ds_read_b64_tr_b16 a[248:249], v244 offset:128\n"
    "v_mfma_f32_16x16x32_bf16 v[64:67], a[236:239], a[176:179], v[64:67]\n"
    "s_add_u32 s57, s59, 0\n"
    "v_mfma_f32_16x16x32_bf16 v[68:71], a[236:239], a[180:183], v[68:71]\n"
    "ds_read_b64_tr_b16 a[250:251], v244 offset:5888\n"
    "v_mfma_f32_16x16x32_bf16 v[68:71], a[232:235], a[164:167], v[68:71]\n"
    "v_mfma_f32_16x16x32_bf16 v[72:75], a[232:235], a[168:171], v[72:75]\n"
    "ds_read_b64_tr_b16 a[252:253], v244 offset:10496\n"
    "v_mfma_f32_16x16x32_bf16 v[72:75], a[236:239], a[184:187], v[72:75]\n"
    "v_mfma_f32_16x16x32_bf16 v[76:79], a[236:239], a[188:191], v[76:79]\n"
    "ds_read_b64_tr_b16 a[254:255], v244 offset:16256\n"
    "v_mfma_f32_16x16x32_bf16 v[76:79], a[232:235], a[172:175], v[76:79]\n"
    "s_waitcnt lgkmcnt(4)\n"
    "buffer_load_dwordx4 v[192:195], v254, s[20:23], s57 offen sc1\n"
    "v_mfma_f32_16x16x32_bf16 v[76:79], a[240:243], a[204:207], v[76:79]\n"
    "ds_read_b64_tr_b16 a[224:225], v247 offset:128\n"
    "v_mfma_f32_16x16x32_bf16 v[76:79], a[244:247], a[220:223], v[76:79]\n"
    "buffer_load_dwordx4 v[196:199], v254, s[20:23], s57 offen offset:1024 sc1\n"
    "v_mfma_f32_16x16x32_bf16 v[72:75], a[244:247], a[216:219], v[72:75]\n"
    "v_mfma_f32_16x16x32_bf16 v[72:75], a[240:243], a[200:203], v[72:75]\n"
    "buffer_load_dwordx2 v[200:201], v252, s[20:23], s57 offen offset:2048 sc1\n"
    "v_mfma_f32_16x16x32_bf16 v[68:71], a[240:243], a[196:199], v[68:71]\n"
    "ds_read_b64_tr_b16 a[228:229], v245 offset:46240\n"
    "v_mfma_f32_16x16x32_bf16 v[68:71], a[244:247], a[212:215], v[68:71]\n"
    "s_add_u32 s57, s59, 2560\n"
    "v_mfma_f32_16x16x32_bf16 v[64:67], a[244:247], a[208:211], v[64:67]\n"
    "s_waitcnt vmcnt(3)\n"
    "ds_read_b64_tr_b16 a[230:231], v245 offset:50848\n"
    "v_xor_b32 v248, v3, v250\n"
    "v_mfma_f32_16x16x32_bf16 v[64:67], a[240:243], a[192:195], v[64:67]\n"
    "s_waitcnt lgkmcnt(2)\n"
    "v_bitop3_b32 v248, v248, v236, v250 bitop3:0xf6\n"
    "v_bitop3_b32 v248, v248, v7, v250 bitop3:0xf6\n"
    "v_mfma_f32_16x16x32_bf16 v[64:67], a[248:251], v[144:147], v[64:67]\n"
    "ds_read_b64_tr_b16 a[232:233], v246 offset:160\n"
    "v_bitop3_b32 v248, v248, v237, v250 bitop3:0xf6\n"
    "v_mfma_f32_16x16x32_bf16 v[64:67], a[252:255], v[160:163], v[64:67]\n"
    "v_bitop3_b32 v248, v248, v11, v250 bitop3:0xf6\n"
    "v_bitop3_b32 v248, v248, v238, v250 bitop3:0xf6\n"
    "v_mfma_f32_16x16x32_bf16 v[64:67], a[224:227], v[176:179], v[64:67]\n"
    "ds_read_b64_tr_b16 a[234:235], v246 offset:4768\n"
    "v_bitop3_b32 v248, v248, v15, v250 bitop3:0xf6\n"
    "v_mfma_f32_16x16x32_bf16 v[68:71], a[224:227], v[180:183], v[68:71]\n"
    "buffer_load_dwordx4 v[32:35], v254, s[20:23], s57 offen sc1\n"
    "v_mfma_f32_16x16x32_bf16 v[68:71], a[252:255], v[164:167], v[68:71]\n"
    "s_add_u32 s82, s4, 4\n"
    "ds_read_b64_tr_b16 a[236:237], v246 offset:9376\n"
    "v_bitop3_b32 v248, v248, v239, v250 bitop3:0xf6\n"
    "v_mfma_f32_16x16x32_bf16 v[68:71], a[248:251], v[148:151], v[68:71]\n"
    "s_and_b32 s82, s82, 7\n"
    "buffer_load_dwordx4 v[36:39], v254, s[20:23], s57 offen offset:1024 sc1\n"
    "v_mfma_f32_16x16x32_bf16 v[72:75], a[248:251], v[152:155], v[72:75]\n"
    "s_mul_i32 s82, s82, 81920\n"
    "ds_read_b64_tr_b16 a[238:239], v246 offset:13984\n"
    "v_bitop3_b32 v248, v248, v19, v250 bitop3:0xf6\n"
    "v_mfma_f32_16x16x32_bf16 v[72:75], a[252:255], v[168:171], v[72:75]\n"
    "s_add_u32 s82, s82, s58\n"
    "buffer_load_dwordx2 v[40:41], v252, s[20:23], s57 offen offset:2048 sc1\n"
    "v_mfma_f32_16x16x32_bf16 v[72:75], a[224:227], v[184:187], v[72:75]\n"
    "s_add_u32 s56, s82, 7680\n"
    "ds_read_b64_tr_b16 a[240:241], v246 offset:18592\n"
    "v_bitop3_b32 v248, v248, v240, v250 bitop3:0xf6\n"
    "v_mfma_f32_16x16x32_bf16 v[76:79], a[224:227], v[188:191], v[76:79]\n"
    "v_bitop3_b32 v248, v248, v23, v250 bitop3:0xf6\n"
    "v_bitop3_b32 v248, v248, v241, v250 bitop3:0xf6\n"
    "v_mfma_f32_16x16x32_bf16 v[76:79], a[252:255], v[172:175], v[76:79]\n"
    "ds_read_b64_tr_b16 a[242:243], v246 offset:23200\n"
    "v_bitop3_b32 v248, v248, v27, v250 bitop3:0xf6\n"
    "v_mfma_f32_16x16x32_bf16 v[76:79], a[248:251], v[156:159], v[76:79]\n"
    "s_waitcnt lgkmcnt(4)\n"
    "v_bitop3_b32 v248, v248, v242, v250 bitop3:0xf6\n"
    "v_bitop3_b32 v248, v248, v31, v250 bitop3:0xf6\n"
    "v_mfma_f32_16x16x32_bf16 v[92:95], a[228:231], a[108:111], v[92:95]\n"
    "ds_read_b64_tr_b16 a[244:245], v246 offset:27808\n"
    "v_bitop3_b32 v248, v248, v243, v250 bitop3:0xf6\n"
    "v_mfma_f32_16x16x32_bf16 v[92:95], a[232:235], a[124:127], v[92:95]\n"
    "v_cmp_lt_u32 vcc, 0xffff, v248\n"
    "v_mfma_f32_16x16x32_bf16 v[88:91], a[232:235], a[120:123], v[88:91]\n"
    "s_nop 0\n"
    "s_and_b64 vcc, vcc, s[64:65]\n"
    "ds_read_b64_tr_b16 a[246:247], v246 offset:32416\n"
    "v_cvt_pk_bf16_f32 v64, v64, v65\n"
    "v_mfma_f32_16x16x32_bf16 v[88:91], a[228:231], a[104:107], v[88:91]\n"
    "s_cbranch_vccnz RT11_%=\n"
    "RB11_%=:\n"
    "v_cvt_pk_bf16_f32 v65, v66, v67\n"
    "v_cvt_pk_bf16_f32 v66, v68, v69\n"
    "v_mfma_f32_16x16x32_bf16 v[84:87], a[228:231], a[100:103], v[84:87]\n"
    "ds_read_b64_tr_b16 a[248:249], v246 offset:37024\n"
    "v_cvt_pk_bf16_f32 v67, v70, v250\n"
    "v_mfma_f32_16x16x32_bf16 v[84:87], a[232:235], a[116:119], v[84:87]\n"
    "v_cvt_pk_bf16_f32 v68, v71, v250\n"
    "v_cvt_pk_bf16_f32 v72, v72, v73\n"
    "v_mfma_f32_16x16x32_bf16 v[80:83], a[232:235], a[112:115], v[80:83]\n"
    "ds_read_b64_tr_b16 a[250:251], v246 offset:41632\n"
    "v_cvt_pk_bf16_f32 v73, v74, v75\n"
    "v_mfma_f32_16x16x32_bf16 v[80:83], a[228:231], a[96:99], v[80:83]\n"
    "s_waitcnt lgkmcnt(4)\n"
    "v_cvt_pk_bf16_f32 v74, v76, v77\n"
    "v_cvt_pk_bf16_f32 v75, v78, v250\n"
    "v_mfma_f32_16x16x32_bf16 v[80:83], a[236:239], a[128:131], v[80:83]\n"
    "ds_read_b64_tr_b16 a[252:253], v244 offset:27808\n"
    "v_cvt_pk_bf16_f32 v69, v79, v250\n"
    "v_mfma_f32_16x16x32_bf16 v[80:83], a[240:243], a[144:147], v[80:83]\n"
    "buffer_store_dwordx4 v[64:67], v254, s[20:23], s56 offen offset:0 sc1\n"
    "v_mfma_f32_16x16x32_bf16 v[84:87], a[240:243], a[148:151], v[84:87]\n"
    "ds_read_b64_tr_b16 a[254:255], v244 offset:32416\n"
    "v_lshlrev_b32 v228, 16, v0\n"
    "v_mfma_f32_16x16x32_bf16 v[84:87], a[236:239], a[132:135], v[84:87]\n"
    "buffer_store_dwordx4 v[72:75], v254, s[20:23], s56 offen offset:1024 sc1\n"
    "v_mfma_f32_16x16x32_bf16 v[88:91], a[236:239], a[136:139], v[88:91]\n"
    "ds_read_b64_tr_b16 a[224:225], v244 offset:37024\n"
    "v_and_b32 v229, 0xffff0000, v0\n"
    "v_mfma_f32_16x16x32_bf16 v[88:91], a[240:243], a[152:155], v[88:91]\n"
    "buffer_store_dwordx2 v[68:69], v252, s[20:23], s56 offen offset:2048 sc1\n"
    "v_mfma_f32_16x16x32_bf16 v[92:95], a[240:243], a[156:159], v[92:95]\n"
    "ds_read_b64_tr_b16 a[226:227], v244 offset:41632\n"
    "v_lshlrev_b32 v230, 16, v1\n"
    "v_mfma_f32_16x16x32_bf16 v[92:95], a[236:239], a[140:143], v[92:95]\n"
    "s_waitcnt lgkmcnt(4)\n"
    "v_and_b32 v231, 0xffff0000, v1\n"
    "v_lshlrev_b32 v232, 16, v2\n"
    "v_mfma_f32_16x16x32_bf16 v[92:95], a[244:247], a[172:175], v[92:95]\n"
    "ds_read_b64_tr_b16 a[228:229], v244 offset:160\n"
    "v_and_b32 v233, 0xffff0000, v2\n"
    "v_mfma_f32_16x16x32_bf16 v[92:95], a[248:251], a[188:191], v[92:95]\n"
    "s_add_u32 s57, s59, 5120\n"
    "v_lshlrev_b32 v234, 16, v3\n"
    "v_lshlrev_b32 v235, 16, v236\n"
    "v_mfma_f32_16x16x32_bf16 v[88:91], a[248:251], a[184:187], v[88:91]\n"
    "ds_read_b64_tr_b16 a[230:231], v244 offset:5920\n"
    "v_lshlrev_b32 v224, 16, v4\n"
    "v_mfma_f32_16x16x32_bf16 v[88:91], a[244:247], a[168:171], v[88:91]\n"
    "v_and_b32 v225, 0xffff0000, v4\n"
    "v_add_f32 v228, v228, v224\n"
    "v_mfma_f32_16x16x32_bf16 v[84:87], a[244:247], a[164:167], v[84:87]\n"
    "ds_read_b64_tr_b16 a[232:233], v244 offset:10528\n"
    "v_add_f32 v229, v229, v225\n"
    "v_mfma_f32_16x16x32_bf16 v[84:87], a[248:251], a[180:183], v[84:87]\n"
    "v_lshlrev_b32 v224, 16, v5\n"
    "v_and_b32 v225, 0xffff0000, v5\n"
    "v_mfma_f32_16x16x32_bf16 v[80:83], a[248:251], a[176:179], v[80:83]\n"
    "ds_read_b64_tr_b16 a[234:235], v244 offset:16288\n"
    "v_add_f32 v230, v230, v224\n"
    "v_mfma_f32_16x16x32_bf16 v[80:83], a[244:247], a[160:163], v[80:83]\n"
    "s_waitcnt lgkmcnt(4)\n"
    "buffer_load_dwordx4 v[48:51], v254, s[20:23], s57 offen sc1\n"
    "v_mfma_f32_16x16x32_bf16 v[80:83], a[252:255], a[192:195], v[80:83]\n"
    "ds_read_b64_tr_b16 a[236:237], v247 offset:160\n"
    "v_add_f32 v231, v231, v225\n"
    "v_mfma_f32_16x16x32_bf16 v[80:83], a[224:227], a[208:211], v[80:83]\n"
    "buffer_load_dwordx4 v[52:55], v254, s[20:23], s57 offen offset:1024 sc1\n"
    "v_mfma_f32_16x16x32_bf16 v[84:87], a[224:227], a[212:215], v[84:87]\n"
    "v_lshlrev_b32 v224, 16, v6\n"
    "v_and_b32 v225, 0xffff0000, v6\n"
    "v_mfma_f32_16x16x32_bf16 v[84:87], a[252:255], a[196:199], v[84:87]\n"
    "buffer_load_dwordx2 v[56:57], v252, s[20:23], s57 offen offset:2048 sc1\n"
    "v_mfma_f32_16x16x32_bf16 v[88:91], a[252:255], a[200:203], v[88:91]\n"
    "ds_read_b64_tr_b16 a[240:241], v245 offset:46272\n"
    "v_add_f32 v232, v232, v224\n"
    "v_mfma_f32_16x16x32_bf16 v[88:91], a[224:227], a[216:219], v[88:91]\n"
    "v_add_f32 v233, v233, v225\n"
    "v_lshlrev_b32 v224, 16, v7\n"
    "v_mfma_f32_16x16x32_bf16 v[92:95], a[224:227], a[220:223], v[92:95]\n"
    "ds_read_b64_tr_b16 a[242:243], v245 offset:50880\n"
    "v_lshlrev_b32 v225, 16, v237\n"
    "v_mfma_f32_16x16x32_bf16 v[92:95], a[252:255], a[204:207], v[92:95]\n"
    "s_waitcnt lgkmcnt(2)\n"
    "v_add_f32 v234, v234, v224\n"
    "v_add_f32 v235, v235, v225\n"
    "v_mfma_f32_16x16x32_bf16 v[92:95], a[228:231], v[156:159], v[92:95]\n"
    "ds_read_b64_tr_b16 a[244:245], v246 offset:192\n"
    "v_lshlrev_b32 v224, 16, v8\n"
    "v_mfma_f32_16x16x32_bf16 v[92:95], a[232:235], v[172:175], v[92:95]\n"
    "v_and_b32 v225, 0xffff0000, v8\n"
    "v_add_f32 v228, v228, v224\n"
    "v_mfma_f32_16x16x32_bf16 v[92:95], a[236:239], v[188:191], v[92:95]\n"
    "ds_read_b64_tr_b16 a[246:247], v246 offset:4800\n"
    "v_add_f32 v229, v229, v225\n"
    "v_mfma_f32_16x16x32_bf16 v[88:91], a[236:239], v[184:187], v[88:91]\n"
    "v_lshlrev_b32 v224, 16, v9\n"
    "v_and_b32 v225, 0xffff0000, v9\n"
    "v_mfma_f32_16x16x32_bf16 v[88:91], a[232:235], v[168:171], v[88:91]\n"
    "s_add_u32 s82, s4, 5\n"
    "ds_read_b64_tr_b16 a[248:249], v246 offset:9408\n"
    "v_add_f32 v230, v230, v224\n"
    "v_mfma_f32_16x16x32_bf16 v[88:91], a[228:231], v[152:155], v[88:91]\n"
    "s_and_b32 s82, s82, 7\n"
    "v_add_f32 v231, v231, v225\n"
    "v_lshlrev_b32 v224, 16, v10\n"
    "v_mfma_f32_16x16x32_bf16 v[84:87], a[228:231], v[148:151], v[84:87]\n"
    "s_mul_i32 s82, s82, 81920\n"
    "ds_read_b64_tr_b16 a[250:251], v246 offset:14016\n"
    "v_and_b32 v225, 0xffff0000, v10\n"
    "v_mfma_f32_16x16x32_bf16 v[84:87], a[232:235], v[164:167], v[84:87]\n"
    "s_add_u32 s82, s82, s58\n"
    "v_add_f32 v232, v232, v224\n"
    "v_add_f32 v233, v233, v225\n"
    "v_mfma_f32_16x16x32_bf16 v[84:87], a[236:239], v[180:183], v[84:87]\n"
    "s_add_u32 s56, s82, 10240\n"
    "ds_read_b64_tr_b16 a[252:253], v246 offset:18624\n"
    "v_lshlrev_b32 v224, 16, v11\n"
    "v_mfma_f32_16x16x32_bf16 v[80:83], a[236:239], v[176:179], v[80:83]\n"
    "v_lshlrev_b32 v225, 16, v238\n"
    "v_add_f32 v234, v234, v224\n"
    "v_mfma_f32_16x16x32_bf16 v[80:83], a[232:235], v[160:163], v[80:83]\n"
    "ds_read_b64_tr_b16 a[254:255], v246 offset:23232\n"
    "v_add_f32 v235, v235, v225\n"
    "v_mfma_f32_16x16x32_bf16 v[80:83], a[228:231], v[144:147], v[80:83]\n"
    "s_waitcnt lgkmcnt(4)\n"
    "v_lshlrev_b32 v224, 16, v12\n"
    "v_and_b32 v225, 0xffff0000, v12\n"
    "v_mfma_f32_16x16x32_bf16 v[96:99], a[240:243], a[96:99], v[96:99]\n"
    "ds_read_b64_tr_b16 a[224:225], v246 offset:27840\n"
    "v_add_f32 v228, v228, v224\n"
    "v_mfma_f32_16x16x32_bf16 v[96:99], a[244:247], a[112:115], v[96:99]\n"
    "v_add_f32 v229, v229, v225\n"
    "v_lshlrev_b32 v224, 16, v13\n"
    "v_mfma_f32_16x16x32_bf16 v[100:103], a[244:247], a[116:119], v[100:103]\n"
    "ds_read_b64_tr_b16 a[226:227], v246 offset:32448\n"
    "v_cvt_pk_bf16_f32 v80, v80, v81\n"
    "v_mfma_f32_16x16x32_bf16 v[100:103], a[240:243], a[100:103], v[100:103]\n"
    "v_cvt_pk_bf16_f32 v81, v82, v83\n"
    "v_cvt_pk_bf16_f32 v82, v84, v85\n"
    "v_mfma_f32_16x16x32_bf16 v[104:107], a[240:243], a[104:107], v[104:107]\n"
    "ds_read_b64_tr_b16 a[228:229], v246 offset:37056\n"
    "v_cvt_pk_bf16_f32 v83, v86, v250\n"
    "v_mfma_f32_16x16x32_bf16 v[104:107], a[244:247], a[120:123], v[104:107]\n"
    "v_cvt_pk_bf16_f32 v84, v87, v250\n"
    "v_cvt_pk_bf16_f32 v88, v88, v89\n"
    "v_mfma_f32_16x16x32_bf16 v[108:111], a[244:247], a[124:127], v[108:111]\n"
    "ds_read_b64_tr_b16 a[230:231], v246 offset:41664\n"
    "v_cvt_pk_bf16_f32 v89, v90, v91\n"
    "v_mfma_f32_16x16x32_bf16 v[108:111], a[240:243], a[108:111], v[108:111]\n"
    "s_waitcnt lgkmcnt(4)\n"
    "v_cvt_pk_bf16_f32 v90, v92, v93\n"
    "v_cvt_pk_bf16_f32 v91, v94, v250\n"
    "v_mfma_f32_16x16x32_bf16 v[108:111], a[248:251], a[140:143], v[108:111]\n"
    "ds_read_b64_tr_b16 a[232:233], v244 offset:27840\n"
    "v_cvt_pk_bf16_f32 v85, v95, v250\n"
    "v_mfma_f32_16x16x32_bf16 v[108:111], a[252:255], a[156:159], v[108:111]\n"
    "buffer_store_dwordx4 v[80:83], v254, s[20:23], s56 offen offset:0 sc1\n"
    "v_mfma_f32_16x16x32_bf16 v[104:107], a[252:255], a[152:155], v[104:107]\n"
    "ds_read_b64_tr_b16 a[234:235], v244 offset:32448\n"
    "v_and_b32 v225, 0xffff0000, v13\n"
    "v_mfma_f32_16x16x32_bf16 v[104:107], a[248:251], a[136:139], v[104:107]\n"
    "buffer_store_dwordx4 v[88:91], v254, s[20:23], s56 offen offset:1024 sc1\n"
    "v_mfma_f32_16x16x32_bf16 v[100:103], a[248:251], a[132:135], v[100:103]\n"
    "ds_read_b64_tr_b16 a[236:237], v244 offset:37056\n"
    "v_add_f32 v230, v230, v224\n"
    "v_mfma_f32_16x16x32_bf16 v[100:103], a[252:255], a[148:151], v[100:103]\n"
    "buffer_store_dwordx2 v[84:85], v252, s[20:23], s56 offen offset:2048 sc1\n"
    "v_mfma_f32_16x16x32_bf16 v[96:99], a[252:255], a[144:147], v[96:99]\n"
    "ds_read_b64_tr_b16 a[238:239], v244 offset:41664\n"
    "v_add_f32 v231, v231, v225\n"
    "v_mfma_f32_16x16x32_bf16 v[96:99], a[248:251], a[128:131], v[96:99]\n"
    "s_waitcnt lgkmcnt(4)\n"
    "v_lshlrev_b32 v224, 16, v14\n"
    "v_and_b32 v225, 0xffff0000, v14\n"
    "v_mfma_f32_16x16x32_bf16 v[96:99], a[224:227], a[160:163], v[96:99]\n"
    "ds_read_b64_tr_b16 a[240:241], v244 offset:192\n"
    "v_add_f32 v232, v232, v224\n"
    "v_mfma_f32_16x16x32_bf16 v[96:99], a[228:231], a[176:179], v[96:99]\n"
    "s_add_u32 s57, s59, 7680\n"
    "v_add_f32 v233, v233, v225\n"
    "v_lshlrev_b32 v224, 16, v15\n"
    "v_mfma_f32_16x16x32_bf16 v[100:103], a[228:231], a[180:183], v[100:103]\n"
    "ds_read_b64_tr_b16 a[242:243], v244 offset:5952\n"
    "v_lshlrev_b32 v225, 16, v239\n"
    "v_mfma_f32_16x16x32_bf16 v[100:103], a[224:227], a[164:167], v[100:103]\n"
    "v_add_f32 v234, v234, v224\n"
    "v_add_f32 v235, v235, v225\n"
    "v_mfma_f32_16x16x32_bf16 v[104:107], a[224:227], a[168:171], v[104:107]\n"
    "ds_read_b64_tr_b16 a[244:245], v244 offset:10560\n"
    "v_lshlrev_b32 v224, 16, v16\n"
    "v_mfma_f32_16x16x32_bf16 v[104:107], a[228:231], a[184:187], v[104:107]\n"
    "v_and_b32 v225, 0xffff0000, v16\n"
    "v_add_f32 v228, v228, v224\n"
    "v_mfma_f32_16x16x32_bf16 v[108:111], a[228:231], a[188:191], v[108:111]\n"
    "ds_read_b64_tr_b16 a[246:247], v244 offset:16320\n"
    "v_add_f32 v229, v229, v225\n"
    "v_mfma_f32_16x16x32_bf16 v[108:111], a[224:227], a[172:175], v[108:111]\n"
    "s_waitcnt lgkmcnt(4)\n"
    "buffer_load_dwordx4 v[64:67], v254, s[20:23], s57 offen sc1\n"
    "v_mfma_f32_16x16x32_bf16 v[108:111], a[232:235], a[204:207], v[108:111]\n"
    "ds_read_b64_tr_b16 a[248:249], v247 offset:192\n"
    "v_lshlrev_b32 v224, 16, v17\n"
    "v_mfma_f32_16x16x32_bf16 v[108:111], a[236:239], a[220:223], v[108:111]\n"
    "buffer_load_dwordx4 v[68:71], v254, s[20:23], s57 offen offset:1024 sc1\n"
    "v_mfma_f32_16x16x32_bf16 v[104:107], a[236:239], a[216:219], v[104:107]\n"
    "v_and_b32 v225, 0xffff0000, v17\n"
    "v_add_f32 v230, v230, v224\n"
    "v_mfma_f32_16x16x32_bf16 v[104:107], a[232:235], a[200:203], v[104:107]\n"
    "buffer_load_dwordx2 v[72:73], v252, s[20:23], s57 offen offset:2048 sc1\n"
    "v_mfma_f32_16x16x32_bf16 v[100:103], a[232:235], a[196:199], v[100:103]\n"
    "ds_read_b64_tr_b16 a[252:253], v245 offset:46304\n"
    "v_add_f32 v231, v231, v225\n"
    "v_mfma_f32_16x16x32_bf16 v[100:103], a[236:239], a[212:215], v[100:103]\n"
    "v_lshlrev_b32 v224, 16, v18\n"
    "v_and_b32 v225, 0xffff0000, v18\n"
    "v_mfma_f32_16x16x32_bf16 v[96:99], a[236:239], a[208:211], v[96:99]\n"
    "ds_read_b64_tr_b16 a[254:255], v245 offset:50912\n"
    "v_add_f32 v232, v232, v224\n"
    "v_mfma_f32_16x16x32_bf16 v[96:99], a[232:235], a[192:195], v[96:99]\n"
    "s_waitcnt lgkmcnt(2)\n"
    "v_add_f32 v233, v233, v225\n"
    "v_lshlrev_b32 v224, 16, v19\n"
    "v_mfma_f32_16x16x32_bf16 v[96:99], a[240:243], v[144:147], v[96:99]\n"
    "ds_read_b64_tr_b16 a[224:225], v246 offset:224\n"
    "v_lshlrev_b32 v225, 16, v240\n"
    "v_mfma_f32_16x16x32_bf16 v[96:99], a[244:247], v[160:163], v[96:99]\n"
    "v_add_f32 v234, v234, v224\n"
    "v_add_f32 v235, v235, v225\n"
    "v_mfma_f32_16x16x32_bf16 v[96:99], a[248:251], v[176:179], v[96:99]\n"
    "ds_read_b64_tr_b16 a[226:227], v246 offset:4832\n"
    "v_lshlrev_b32 v224, 16, v20\n"
    "v_mfma_f32_16x16x32_bf16 v[100:103], a[248:251], v[180:183], v[100:103]\n"
    "v_and_b32 v225, 0xffff0000, v20\n"
    "v_add_f32 v228, v228, v224\n"
    "v_mfma_f32_16x16x32_bf16 v[100:103], a[244:247], v[164:167], v[100:103]\n"
    "s_add_u32 s82, s4, 6\n"
    "ds_read_b64_tr_b16 a[228:229], v246 offset:9440\n"
    "v_add_f32 v229, v229, v225\n"
    "v_mfma_f32_16x16x32_bf16 v[100:103], a[240:243], v[148:151], v[100:103]\n"
    "s_and_b32 s82, s82, 7\n"
    "v_lshlrev_b32 v224, 16, v21\n"
    "v_and_b32 v225, 0xffff0000, v21\n"
    "v_mfma_f32_16x16x32_bf16 v[104:107], a[240:243], v[152:155], v[104:107]\n"
    "s_mul_i32 s82, s82, 81920\n"
    "ds_read_b64_tr_b16 a[230:231], v246 offset:14048\n"
    "v_add_f32 v230, v230, v224\n"
    "v_mfma_f32_16x16x32_bf16 v[104:107], a[244:247], v[168:171], v[104:107]\n"
    "s_add_u32 s82, s82, s58\n"
    "v_add_f32 v231, v231, v225\n"
    "v_lshlrev_b32 v224, 16, v22\n"
    "v_mfma_f32_16x16x32_bf16 v[104:107], a[248:251], v[184:187], v[104:107]\n"
    "s_add_u32 s56, s82, 12800\n"
    "ds_read_b64_tr_b16 a[232:233], v246 offset:18656\n"
    "v_and_b32 v225, 0xffff0000, v22\n"
    "v_mfma_f32_16x16x32_bf16 v[108:111], a[248:251], v[188:191], v[108:111]\n"
    "v_add_f32 v232, v232, v224\n"
    "v_add_f32 v233, v233, v225\n"
    "v_mfma_f32_16x16x32_bf16 v[108:111], a[244:247], v[172:175], v[108:111]\n"
    "ds_read_b64_tr_b16 a[234:235], v246 offset:23264\n"
    "v_lshlrev_b32 v224, 16, v23\n"
    "v_mfma_f32_16x16x32_bf16 v[108:111], a[240:243], v[156:159], v[108:111]\n"
    "s_waitcnt lgkmcnt(4)\n"
    "v_lshlrev_b32 v225, 16, v241\n"
    "v_add_f32 v234, v234, v224\n"
    "v_mfma_f32_16x16x32_bf16 v[124:127], a[252:255], a[108:111], v[124:127]\n"
    "ds_read_b64_tr_b16 a[236:237], v246 offset:27872\n"
    "v_add_f32 v235, v235, v225\n"
    "v_mfma_f32_16x16x32_bf16 v[124:127], a[224:227], a[124:127], v[124:127]\n"
    "v_lshlrev_b32 v224, 16, v24\n"
    "v_and_b32 v225, 0xffff0000, v24\n"
    "v_mfma_f32_16x16x32_bf16 v[120:123], a[224:227], a[120:123], v[120:123]\n"
    "ds_read_b64_tr_b16 a[238:239], v246 offset:32480\n"
    "v_cvt_pk_bf16_f32 v96, v96, v97\n"
    "v_mfma_f32_16x16x32_bf16 v[120:123], a[252:255], a[104:107], v[120:123]\n"
    "v_cvt_pk_bf16_f32 v97, v98, v99\n"
    "v_cvt_pk_bf16_f32 v98, v100, v101\n"
    "v_mfma_f32_16x16x32_bf16 v[116:119], a[252:255], a[100:103], v[116:119]\n"
    "ds_read_b64_tr_b16 a[240:241], v246 offset:37088\n"
    "v_cvt_pk_bf16_f32 v99, v102, v250\n"
    "v_mfma_f32_16x16x32_bf16 v[116:119], a[224:227], a[116:119], v[116:119]\n"
    "v_cvt_pk_bf16_f32 v100, v103, v250\n"
    "v_cvt_pk_bf16_f32 v104, v104, v105\n"
    "v_mfma_f32_16x16x32_bf16 v[112:115], a[224:227], a[112:115], v[112:115]\n"
    "ds_read_b64_tr_b16 a[242:243], v246 offset:41696\n"
    "v_cvt_pk_bf16_f32 v105, v106, v107\n"
    "v_mfma_f32_16x16x32_bf16 v[112:115], a[252:255], a[96:99], v[112:115]\n"
    "s_waitcnt lgkmcnt(4)\n"
    "v_cvt_pk_bf16_f32 v106, v108, v109\n"
    "v_cvt_pk_bf16_f32 v107, v110, v250\n"
    "v_mfma_f32_16x16x32_bf16 v[112:115], a[228:231], a[128:131], v[112:115]\n"
    "ds_read_b64_tr_b16 a[244:245], v244 offset:27872\n"
    "v_cvt_pk_bf16_f32 v101, v111, v250\n"
    "v_mfma_f32_16x16x32_bf16 v[112:115], a[232:235], a[144:147], v[112:115]\n"
    "buffer_store_dwordx4 v[96:99], v254, s[20:23], s56 offen offset:0 sc1\n"
    "v_mfma_f32_16x16x32_bf16 v[116:119], a[232:235], a[148:151], v[116:119]\n"
    "ds_read_b64_tr_b16 a[246:247], v244 offset:32480\n"
    "v_add_f32 v228, v228, v224\n"
    "v_mfma_f32_16x16x32_bf16 v[116:119], a[228:231], a[132:135], v[116:119]\n"
    "buffer_store_dwordx4 v[104:107], v254, s[20:23], s56 offen offset:1024 sc1\n"
    "v_mfma_f32_16x16x32_bf16 v[120:123], a[228:231], a[136:139], v[120:123]\n"
    "ds_read_b64_tr_b16 a[248:249], v244 offset:37088\n"
    "v_add_f32 v229, v229, v225\n"
    "v_mfma_f32_16x16x32_bf16 v[120:123], a[232:235], a[152:155], v[120:123]\n"
    "buffer_store_dwordx2 v[100:101], v252, s[20:23], s56 offen offset:2048 sc1\n"
    "v_mfma_f32_16x16x32_bf16 v[124:127], a[232:235], a[156:159], v[124:127]\n"
    "ds_read_b64_tr_b16 a[250:251], v244 offset:41696\n"
    "v_lshlrev_b32 v224, 16, v25\n"
    "v_mfma_f32_16x16x32_bf16 v[124:127], a[228:231], a[140:143], v[124:127]\n"
    "s_waitcnt lgkmcnt(4)\n"
    "v_and_b32 v225, 0xffff0000, v25\n"
    "v_add_f32 v230, v230, v224\n"
    "v_mfma_f32_16x16x32_bf16 v[124:127], a[236:239], a[172:175], v[124:127]\n"
    "ds_read_b64_tr_b16 a[252:253], v244 offset:224\n"
    "v_add_f32 v231, v231, v225\n"
    "v_mfma_f32_16x16x32_bf16 v[124:127], a[240:243], a[188:191], v[124:127]\n"
    "s_add_u32 s57, s59, 10240\n"
    "v_lshlrev_b32 v224, 16, v26\n"
    "v_and_b32 v225, 0xffff0000, v26\n"
    "v_mfma_f32_16x16x32_bf16 v[120:123], a[240:243], a[184:187], v[120:123]\n"
    "ds_read_b64_tr_b16 a[254:255], v244 offset:5984\n"
    "v_add_f32 v232, v232, v224\n"
    "v_mfma_f32_16x16x32_bf16 v[120:123], a[236:239], a[168:171], v[120:123]\n"
    "v_add_f32 v233, v233, v225\n"
    "v_lshlrev_b32 v224, 16, v27\n"
    "v_mfma_f32_16x16x32_bf16 v[116:119], a[236:239], a[164:167], v[116:119]\n"
    "ds_read_b64_tr_b16 a[224:225], v244 offset:10592\n"
    "v_lshlrev_b32 v225, 16, v242\n"
    "v_mfma_f32_16x16x32_bf16 v[116:119], a[240:243], a[180:183], v[116:119]\n"
    "v_add_f32 v234, v234, v224\n"
    "v_add_f32 v235, v235, v225\n"
    "v_mfma_f32_16x16x32_bf16 v[112:115], a[240:243], a[176:179], v[112:115]\n"
    "ds_read_b64_tr_b16 a[226:227], v244 offset:16352\n"
    "v_lshlrev_b32 v224, 16, v28\n"
    "v_mfma_f32_16x16x32_bf16 v[112:115], a[236:239], a[160:163], v[112:115]\n"
    "s_waitcnt lgkmcnt(4)\n"
    "buffer_load_dwordx4 v[80:83], v254, s[20:23], s57 offen sc1\n"
    "v_mfma_f32_16x16x32_bf16 v[112:115], a[244:247], a[192:195], v[112:115]\n"
    "ds_read_b64_tr_b16 a[228:229], v247 offset:224\n"
    "v_and_b32 v225, 0xffff0000, v28\n"
    "v_mfma_f32_16x16x32_bf16 v[112:115], a[248:251], a[208:211], v[112:115]\n"
    "buffer_load_dwordx4 v[84:87], v254, s[20:23], s57 offen offset:1024 sc1\n"
    "v_mfma_f32_16x16x32_bf16 v[116:119], a[248:251], a[212:215], v[116:119]\n"
    "v_add_f32 v228, v228, v224\n"
    "v_add_f32 v229, v229, v225\n"
    "v_mfma_f32_16x16x32_bf16 v[116:119], a[244:247], a[196:199], v[116:119]\n"
    "buffer_load_dwordx2 v[88:89], v252, s[20:23], s57 offen offset:2048 sc1\n"
    "v_mfma_f32_16x16x32_bf16 v[120:123], a[244:247], a[200:203], v[120:123]\n"
    "ds_read_b64_tr_b16 a[232:233], v245 offset:46336\n"
    "v_lshlrev_b32 v224, 16, v29\n"
    "v_mfma_f32_16x16x32_bf16 v[120:123], a[248:251], a[216:219], v[120:123]\n"
    "v_and_b32 v225, 0xffff0000, v29\n"
    "v_add_f32 v230, v230, v224\n"
    "v_mfma_f32_16x16x32_bf16 v[124:127], a[248:251], a[220:223], v[124:127]\n"
    "ds_read_b64_tr_b16 a[234:235], v245 offset:50944\n"
    "v_add_f32 v231, v231, v225\n"
    "v_mfma_f32_16x16x32_bf16 v[124:127], a[244:247], a[204:207], v[124:127]\n"
    "s_waitcnt lgkmcnt(2)\n"
    "v_lshlrev_b32 v224, 16, v30\n"
    "v_and_b32 v225, 0xffff0000, v30\n"
    "v_mfma_f32_16x16x32_bf16 v[124:127], a[252:255], v[156:159], v[124:127]\n"
    "ds_read_b64_tr_b16 a[236:237], v246 offset:256\n"
    "v_add_f32 v232, v232, v224\n"
    "v_mfma_f32_16x16x32_bf16 v[124:127], a[224:227], v[172:175], v[124:127]\n"
    "v_add_f32 v233, v233, v225\n"
    "v_lshlrev_b32 v224, 16, v31\n"
    "v_mfma_f32_16x16x32_bf16 v[124:127], a[228:231], v[188:191], v[124:127]\n"
    "ds_read_b64_tr_b16 a[238:239], v246 offset:4864\n"
    "v_lshlrev_b32 v225, 16, v243\n"
    "v_mfma_f32_16x16x32_bf16 v[120:123], a[228:231], v[184:187], v[120:123]\n"
    "v_add_f32 v234, v234, v224\n"
    "v_add_f32 v235, v235, v225\n"
    "v_mfma_f32_16x16x32_bf16 v[120:123], a[224:227], v[168:171], v[120:123]\n"
    "s_add_u32 s82, s4, 7\n"
    "ds_read_b64_tr_b16 a[240:241], v246 offset:9472\n"
    "v_cvt_pk_bf16_f32 v236, v228, v229\n"
    "v_mfma_f32_16x16x32_bf16 v[120:123], a[252:255], v[152:155], v[120:123]\n"
    "s_and_b32 s82, s82, 7\n"
    "v_cvt_pk_bf16_f32 v237, v230, v231\n"
    "v_cvt_pk_bf16_f32 v238, v232, v233\n"
    "v_mfma_f32_16x16x32_bf16 v[116:119], a[252:255], v[148:151], v[116:119]\n"
    "s_mul_i32 s82, s82, 81920\n"
    "ds_read_b64_tr_b16 a[242:243], v246 offset:14080\n"
    "v_cvt_pk_bf16_f32 v239, v234, v235\n"
    "v_mfma_f32_16x16x32_bf16 v[116:119], a[224:227], v[164:167], v[116:119]\n"
    "s_add_u32 s82, s82, s58\n"
    "v_permlane16_swap_b32 v236, v238\n"
    "v_permlane16_swap_b32 v237, v239\n"
    "v_mfma_f32_16x16x32_bf16 v[116:119], a[228:231], v[180:183], v[116:119]\n"
    "s_add_u32 s56, s82, 15360\n"
    "ds_read_b64_tr_b16 a[244:245], v246 offset:18688\n"
    "v_mfma_f32_16x16x32_bf16 v[112:115], a[228:231], v[176:179], v[112:115]\n"
    "s_waitcnt vmcnt(21)\n"
    "buffer_store_dwordx4 v[236:239], v251, s[52:55], s63 offen offset:1024\n"
    "v_mfma_f32_16x16x32_bf16 v[112:115], a[224:227], v[160:163], v[112:115]\n"
    "ds_read_b64_tr_b16 a[246:247], v246 offset:23296\n"
    "v_xor_b32 v248, v195, v250\n"
    "v_mfma_f32_16x16x32_bf16 v[112:115], a[252:255], v[144:147], v[112:115]\n"
    "s_waitcnt lgkmcnt(4)\n"
    "v_bitop3_b32 v248, v248, v199, v250 bitop3:0xf6\n"
    "v_bitop3_b32 v248, v248, v200, v250 bitop3:0xf6\n"
    "v_mfma_f32_16x16x32_bf16 v[128:131], a[232:235], a[96:99], v[128:131]\n"
    "ds_read_b64_tr_b16 a[248:249], v246 offset:27904\n"
    "v_bitop3_b32 v248, v248, v201, v250 bitop3:0xf6\n"
    "v_mfma_f32_16x16x32_bf16 v[128:131], a[236:239], a[112:115], v[128:131]\n"
    "v_cmp_lt_u32 vcc, 0xffff, v248\n"
    "v_mfma_f32_16x16x32_bf16 v[132:135], a[236:239], a[116:119], v[132:135]\n"
    "s_nop 0\n"
    "s_cbranch_vccnz RT12_%=\n"
    "RB12_%=:\n"
    "ds_read_b64_tr_b16 a[250:251], v246 offset:32512\n"
    "v_cvt_pk_bf16_f32 v112, v112, v113\n"
    "v_mfma_f32_16x16x32_bf16 v[132:135], a[232:235], a[100:103], v[132:135]\n"
    "v_cvt_pk_bf16_f32 v113, v114, v115\n"
    "v_cvt_pk_bf16_f32 v114, v116, v117\n"
    "v_mfma_f32_16x16x32_bf16 v[136:139], a[232:235], a[104:107], v[136:139]\n"
    "ds_read_b64_tr_b16 a[252:253], v246 offset:37120\n"
    "v_cvt_pk_bf16_f32 v115, v118, v250\n"
    "v_mfma_f32_16x16x32_bf16 v[136:139], a[236:239], a[120:123], v[136:139]\n"
    "v_cvt_pk_bf16_f32 v116, v119, v250\n"
    "v_cvt_pk_bf16_f32 v120, v120, v121\n"
    "v_mfma_f32_16x16x32_bf16 v[140:143], a[236:239], a[124:127], v[140:143]\n"
    "ds_read_b64_tr_b16 a[254:255], v246 offset:41728\n"
    "v_cvt_pk_bf16_f32 v121, v122, v123\n"
    "v_mfma_f32_16x16x32_bf16 v[140:143], a[232:235], a[108:111], v[140:143]\n"
    "s_waitcnt lgkmcnt(4)\n"
    "v_cvt_pk_bf16_f32 v122, v124, v125\n"
    "v_cvt_pk_bf16_f32 v123, v126, v250\n"
    "v_mfma_f32_16x16x32_bf16 v[140:143], a[240:243], a[140:143], v[140:143]\n"
    "ds_read_b64_tr_b16 a[224:225], v244 offset:27904\n"
    "v_cvt_pk_bf16_f32 v117, v127, v250\n"
    "v_mfma_f32_16x16x32_bf16 v[140:143], a[244:247], a[156:159], v[140:143]\n"
    "buffer_store_dwordx4 v[112:115], v254, s[20:23], s56 offen offset:0 sc1\n"
    "v_mfma_f32_16x16x32_bf16 v[136:139], a[244:247], a[152:155], v[136:139]\n"
    "ds_read_b64_tr_b16 a[226:227], v244 offset:32512\n"
    "v_lshlrev_b32 v224, 16, v192\n"
    "v_mfma_f32_16x16x32_bf16 v[136:139], a[240:243], a[136:139], v[136:139]\n"
    "buffer_store_dwordx4 v[120:123], v254, s[20:23], s56 offen offset:1024 sc1\n"
    "v_mfma_f32_16x16x32_bf16 v[132:135], a[240:243], a[132:135], v[132:135]\n"
    "ds_read_b64_tr_b16 a[228:229], v244 offset:37120\n"
    "v_and_b32 v225, 0xffff0000, v192\n"
    "v_mfma_f32_16x16x32_bf16 v[132:135], a[244:247], a[148:151], v[132:135]\n"
    "buffer_store_dwordx2 v[116:117], v252, s[20:23], s56 offen offset:2048 sc1\n"
    "v_mfma_f32_16x16x32_bf16 v[128:131], a[244:247], a[144:147], v[128:131]\n"
    "ds_read_b64_tr_b16 a[230:231], v244 offset:41728\n"
    "v_add_f32 v208, v208, v224\n"
    "v_mfma_f32_16x16x32_bf16 v[128:131], a[240:243], a[128:131], v[128:131]\n"
    "s_waitcnt lgkmcnt(4)\n"
    "v_add_f32 v209, v209, v225\n"
    "v_lshlrev_b32 v224, 16, v193\n"
    "v_mfma_f32_16x16x32_bf16 v[128:131], a[248:251], a[160:163], v[128:131]\n"
    "ds_read_b64_tr_b16 a[232:233], v244 offset:256\n"
    "v_and_b32 v225, 0xffff0000, v193\n"
    "v_mfma_f32_16x16x32_bf16 v[128:131], a[252:255], a[176:179], v[128:131]\n"
    "s_add_u32 s57, s59, 12800\n"
    "v_add_f32 v210, v210, v224\n"
    "v_add_f32 v211, v211, v225\n"
    "v_mfma_f32_16x16x32_bf16 v[132:135], a[252:255], a[180:183], v[132:135]\n"
    "ds_read_b64_tr_b16 a[234:235], v244 offset:6016\n"
    "v_lshlrev_b32 v224, 16, v194\n"
    "v_mfma_f32_16x16x32_bf16 v[132:135], a[248:251], a[164:167], v[132:135]\n"
    "v_and_b32 v225, 0xffff0000, v194\n"
    "v_add_f32 v212, v212, v224\n"
    "v_mfma_f32_16x16x32_bf16 v[136:139], a[248:251], a[168:171], v[136:139]\n"
    "ds_read_b64_tr_b16 a[236:237], v244 offset:10624\n"
    "v_add_f32 v213, v213, v225\n"
    "v_mfma_f32_16x16x32_bf16 v[136:139], a[252:255], a[184:187], v[136:139]\n"
    "v_lshlrev_b32 v224, 16, v195\n"
    "v_lshlrev_b32 v225, 16, v200\n"
    "v_mfma_f32_16x16x32_bf16 v[140:143], a[252:255], a[188:191], v[140:143]\n"
    "ds_read_b64_tr_b16 a[238:239], v244 offset:16384\n"
    "v_add_f32 v214, v214, v224\n"
    "v_mfma_f32_16x16x32_bf16 v[140:143], a[248:251], a[172:175], v[140:143]\n"
    "s_waitcnt lgkmcnt(4)\n"
    "buffer_load_dwordx4 v[96:99], v254, s[20:23], s57 offen sc1\n"
    "v_mfma_f32_16x16x32_bf16 v[140:143], a[224:227], a[204:207], v[140:143]\n"
    "ds_read_b64_tr_b16 a[240:241], v247 offset:256\n"
    "v_add_f32 v215, v215, v225\n"
    "v_mfma_f32_16x16x32_bf16 v[140:143], a[228:231], a[220:223], v[140:143]\n"
    "buffer_load_dwordx4 v[100:103], v254, s[20:23], s57 offen offset:1024 sc1\n"
    "v_mfma_f32_16x16x32_bf16 v[136:139], a[228:231], a[216:219], v[136:139]\n"
    "v_lshlrev_b32 v224, 16, v196\n"
    "v_and_b32 v225, 0xffff0000, v196\n"
    "v_mfma_f32_16x16x32_bf16 v[136:139], a[224:227], a[200:203], v[136:139]\n"
    "buffer_load_dwordx2 v[104:105], v252, s[20:23], s57 offen offset:2048 sc1\n"
    "v_mfma_f32_16x16x32_bf16 v[132:135], a[224:227], a[196:199], v[132:135]\n"
    "v_add_f32 v216, v216, v224\n"
    "v_add_f32 v217, v217, v225\n"
    "v_mfma_f32_16x16x32_bf16 v[132:135], a[228:231], a[212:215], v[132:135]\n"
    "v_lshlrev_b32 v224, 16, v197\n"
    "v_and_b32 v225, 0xffff0000, v197\n"
    "v_mfma_f32_16x16x32_bf16 v[128:131], a[228:231], a[208:211], v[128:131]\n"
    "v_add_f32 v218, v218, v224\n"
    "v_add_f32 v219, v219, v225\n"
    "v_mfma_f32_16x16x32_bf16 v[128:131], a[224:227], a[192:195], v[128:131]\n"
    "s_waitcnt lgkmcnt(0)\n"
    "v_lshlrev_b32 v224, 16, v198\n"
    "v_and_b32 v225, 0xffff0000, v198\n"
    "v_mfma_f32_16x16x32_bf16 v[128:131], a[232:235], v[144:147], v[128:131]\n"
    "v_add_f32 v220, v220, v224\n"
    "v_add_f32 v221, v221, v225\n"
    "v_mfma_f32_16x16x32_bf16 v[128:131], a[236:239], v[160:163], v[128:131]\n"
    "v_lshlrev_b32 v224, 16, v199\n"
    "v_lshlrev_b32 v225, 16, v201\n"
    "v_mfma_f32_16x16x32_bf16 v[128:131], a[240:243], v[176:179], v[128:131]\n"
    "v_add_f32 v222, v222, v224\n"
    "v_add_f32 v223, v223, v225\n"
    "v_mfma_f32_16x16x32_bf16 v[132:135], a[240:243], v[180:183], v[132:135]\n"
    "v_mfma_f32_16x16x32_bf16 v[132:135], a[236:239], v[164:167], v[132:135]\n"
    "v_mfma_f32_16x16x32_bf16 v[132:135], a[232:235], v[148:151], v[132:135]\n"
    "v_mfma_f32_16x16x32_bf16 v[136:139], a[232:235], v[152:155], v[136:139]\n"
    "v_mfma_f32_16x16x32_bf16 v[136:139], a[236:239], v[168:171], v[136:139]\n"
    "v_mfma_f32_16x16x32_bf16 v[136:139], a[240:243], v[184:187], v[136:139]\n"
    "v_mfma_f32_16x16x32_bf16 v[140:143], a[240:243], v[188:191], v[140:143]\n"
    "v_mfma_f32_16x16x32_bf16 v[140:143], a[236:239], v[172:175], v[140:143]\n"
    "v_mfma_f32_16x16x32_bf16 v[140:143], a[232:235], v[156:159], v[140:143]\n"
    "s_add_u32 s57, s59, 15360\n"
    "buffer_load_dwordx4 v[0:3], v254, s[20:23], s57 offen sc1\n"
    "buffer_load_dwordx4 v[4:7], v254, s[20:23], s57 offen offset:1024 sc1\n"
    "buffer_load_dwordx2 v[8:9], v252, s[20:23], s57 offen offset:2048 sc1\n"
    "s_or_b32 s82, %2, s7\n"
    "s_cmp_eq_u32 s82, 0\n"
    "s_cselect_b32 s82, 0, 0x7ffffff0\n"
    "v_mov_b32 v226, s66\n"
    "s_add_u32 s83, s62, 36864\n"
    "v_pk_add_f32 v[208:209], v[208:209], v[128:129]\n"
    "v_pk_add_f32 v[210:211], v[210:211], v[130:131]\n"
    "v_pk_add_f32 v[212:213], v[212:213], v[132:133]\n"
    "v_pk_add_f32 v[214:215], v[214:215], v[134:135]\n"
    "v_pk_add_f32 v[216:217], v[216:217], v[136:137]\n"
    "v_pk_add_f32 v[218:219], v[218:219], v[138:139]\n"
    "v_pk_add_f32 v[220:221], v[220:221], v[140:141]\n"
    "v_pk_add_f32 v[222:223], v[222:223], v[142:143]\n"
    "s_waitcnt vmcnt(28)\n"
    "v_xor_b32 v248, v35, v250\n"
    "v_bitop3_b32 v248, v248, v39, v250 bitop3:0xf6\n"
    "v_bitop3_b32 v248, v248, v40, v250 bitop3:0xf6\n"
    "v_bitop3_b32 v248, v248, v41, v250 bitop3:0xf6\n"
    "v_cmp_lt_u32 vcc, 0xffff, v248\n"
    "s_nop 1\n"
    "s_cbranch_vccnz RT13_%=\n"
    "RB13_%=:\n"
    "v_lshlrev_b32 v224, 16, v32\n"
    "v_and_b32 v225, 0xffff0000, v32\n"
    "v_add_f32 v208, v208, v224\n"
    "v_add_f32 v209, v209, v225\n"
    "v_lshlrev_b32 v224, 16, v33\n"
    "v_and_b32 v225, 0xffff0000, v33\n"
    "v_add_f32 v210, v210, v224\n"
    "v_add_f32 v211, v211, v225\n"
    "v_lshlrev_b32 v224, 16, v34\n"
    "v_and_b32 v225, 0xffff0000, v34\n"
    "v_add_f32 v212, v212, v224\n"
    "v_add_f32 v213, v213, v225\n"
    "v_lshlrev_b32 v224, 16, v35\n"
    "v_lshlrev_b32 v225, 16, v40\n"
    "v_add_f32 v214, v214, v224\n"
    "v_add_f32 v215, v215, v225\n"
    "v_lshlrev_b32 v224, 16, v36\n"
    "v_and_b32 v225, 0xffff0000, v36\n"
    "v_add_f32 v216, v216, v224\n"
    "v_add_f32 v217, v217, v225\n"
    "v_lshlrev_b32 v224, 16, v37\n"
    "v_and_b32 v225, 0xffff0000, v37\n"
    "v_add_f32 v218, v218, v224\n"
    "v_add_f32 v219, v219, v225\n"
    "v_lshlrev_b32 v224, 16, v38\n"
    "v_and_b32 v225, 0xffff0000, v38\n"
    "v_add_f32 v220, v220, v224\n"
    "v_add_f32 v221, v221, v225\n"
    "v_lshlrev_b32 v224, 16, v39\n"
    "v_lshlrev_b32 v225, 16, v41\n"
    "v_add_f32 v222, v222, v224\n"
    "v_add_f32 v223, v223, v225\n"
    "s_waitcnt vmcnt(22)\n"
    "v_xor_b32 v248, v51, v250\n"
    "v_bitop3_b32 v248, v248, v55, v250 bitop3:0xf6\n"
    "v_bitop3_b32 v248, v248, v56, v250 bitop3:0xf6\n"
    "v_bitop3_b32 v248, v248, v57, v250 bitop3:0xf6\n"
    "v_cmp_lt_u32 vcc, 0xffff, v248\n"
    "s_nop 1\n"
    "s_cbranch_vccnz RT14_%=\n"
    "RB14_%=:\n"
    "v_lshlrev_b32 v224, 16, v48\n"
    "v_and_b32 v225, 0xffff0000, v48\n"
    "v_add_f32 v208, v208, v224\n"
    "v_add_f32 v209, v209, v225\n"
    "v_lshlrev_b32 v224, 16, v49\n"
    "v_and_b32 v225, 0xffff0000, v49\n"
    "v_add_f32 v210, v210, v224\n"
    "v_add_f32 v211, v211, v225\n"
    "v_lshlrev_b32 v224, 16, v50\n"
    "v_and_b32 v225, 0xffff0000, v50\n"
    "v_add_f32 v212, v212, v224\n"
    "v_add_f32 v213, v213, v225\n"
    "v_lshlrev_b32 v224, 16, v51\n"
    "v_lshlrev_b32 v225, 16, v56\n"
    "v_add_f32 v214, v214, v224\n"
    "v_add_f32 v215, v215, v225\n"
    "v_lshlrev_b32 v224, 16, v52\n"
    "v_and_b32 v225, 0xffff0000, v52\n"
    "v_add_f32 v216, v216, v224\n"
    "v_add_f32 v217, v217, v225\n"
    "v_lshlrev_b32 v224, 16, v53\n"
    "v_and_b32 v225, 0xffff0000, v53\n"
    "v_add_f32 v218, v218, v224\n"
    "v_add_f32 v219, v219, v225\n"
    "v_lshlrev_b32 v224, 16, v54\n"
    "v_and_b32 v225, 0xffff0000, v54\n"
    "v_add_f32 v220, v220, v224\n"
    "v_add_f32 v221, v221, v225\n"
    "v_lshlrev_b32 v224, 16, v55\n"
    "v_lshlrev_b32 v225, 16, v57\n"
    "v_add_f32 v222, v222, v224\n"
    "v_add_f32 v223, v223, v225\n"
    "s_waitcnt vmcnt(16)\n"
    "v_xor_b32 v248, v67, v250\n"
    "v_bitop3_b32 v248, v248, v71, v250 bitop3:0xf6\n"
    "v_bitop3_b32 v248, v248, v72, v250 bitop3:0xf6\n"
    "v_bitop3_b32 v248, v248, v73, v250 bitop3:0xf6\n"
    "v_cmp_lt_u32 vcc, 0xffff, v248\n"
    "s_nop 1\n"
    "s_cbranch_vccnz RT15_%=\n"
    "RB15_%=:\n"
    "v_lshlrev_b32 v224, 16, v64\n"
    "v_and_b32 v225, 0xffff0000, v64\n"
    "v_add_f32 v208, v208, v224\n"
    "v_add_f32 v209, v209, v225\n"
    "v_lshlrev_b32 v224, 16, v65\n"
    "v_and_b32 v225, 0xffff0000, v65\n"
    "v_add_f32 v210, v210, v224\n"
    "v_add_f32 v211, v211, v225\n"
    "v_lshlrev_b32 v224, 16, v66\n"
    "v_and_b32 v225, 0xffff0000, v66\n"
    "v_add_f32 v212, v212, v224\n"
    "v_add_f32 v213, v213, v225\n"
    "v_lshlrev_b32 v224, 16, v67\n"
    "v_lshlrev_b32 v225, 16, v72\n"
    "v_add_f32 v214, v214, v224\n"
    "v_add_f32 v215, v215, v225\n"
    "v_lshlrev_b32 v224, 16, v68\n"
    "v_and_b32 v225, 0xffff0000, v68\n"
    "v_add_f32 v216, v216, v224\n"
    "v_add_f32 v217, v217, v225\n"
    "v_lshlrev_b32 v224, 16, v69\n"
    "v_and_b32 v225, 0xffff0000, v69\n"
    "v_add_f32 v218, v218, v224\n"
    "v_add_f32 v219, v219, v225\n"
    "v_lshlrev_b32 v224, 16, v70\n"
    "v_and_b32 v225, 0xffff0000, v70\n"
    "v_add_f32 v220, v220, v224\n"
    "v_add_f32 v221, v221, v225\n"
    "v_lshlrev_b32 v224, 16, v71\n"
    "v_lshlrev_b32 v225, 16, v73\n"
    "v_add_f32 v222, v222, v224\n"
    "v_add_f32 v223, v223, v225\n"
    "s_waitcnt vmcnt(10)\n"
    "v_xor_b32 v248, v83, v250\n"
    "v_bitop3_b32 v248, v248, v87, v250 bitop3:0xf6\n"
    "v_bitop3_b32 v248, v248, v88, v250 bitop3:0xf6\n"
    "v_bitop3_b32 v248, v248, v89, v250 bitop3:0xf6\n"
    "v_cmp_lt_u32 vcc, 0xffff, v248\n"
    "s_nop 1\n"
    "s_cbranch_vccnz RT16_%=\n"
    "RB16_%=:\n"
    "v_lshlrev_b32 v224, 16, v80\n"
    "v_and_b32 v225, 0xffff0000, v80\n"
    "v_add_f32 v208, v208, v224\n"
    "v_add_f32 v209, v209, v225\n"
    "v_lshlrev_b32 v224, 16, v81\n"
    "v_and_b32 v225, 0xffff0000, v81\n"
    "v_add_f32 v210, v210, v224\n"
    "v_add_f32 v211, v211, v225\n"
    "v_lshlrev_b32 v224, 16, v82\n"
    "v_and_b32 v225, 0xffff0000, v82\n"
    "v_add_f32 v212, v212, v224\n"
    "v_add_f32 v213, v213, v225\n"
    "v_lshlrev_b32 v224, 16, v83\n"
    "v_lshlrev_b32 v225, 16, v88\n"
    "v_add_f32 v214, v214, v224\n"
    "v_add_f32 v215, v215, v225\n"
    "v_lshlrev_b32 v224, 16, v84\n"
    "v_and_b32 v225, 0xffff0000, v84\n"
    "v_add_f32 v216, v216, v224\n"
    "v_add_f32 v217, v217, v225\n"
    "v_lshlrev_b32 v224, 16, v85\n"
    "v_and_b32 v225, 0xffff0000, v85\n"
    "v_add_f32 v218, v218, v224\n"
    "v_add_f32 v219, v219, v225\n"
    "v_lshlrev_b32 v224, 16, v86\n"
    "v_and_b32 v225, 0xffff0000, v86\n"
    "v_add_f32 v220, v220, v224\n"
    "v_add_f32 v221, v221, v225\n"
    "v_lshlrev_b32 v224, 16, v87\n"
    "v_lshlrev_b32 v225, 16, v89\n"
    "v_add_f32 v222, v222, v224\n"
    "v_add_f32 v223, v223, v225\n"
    "s_waitcnt vmcnt(3)\n"
    "v_xor_b32 v248, v99, v250\n"
    "v_bitop3_b32 v248, v248, v103, v250 bitop3:0xf6\n"
    "v_bitop3_b32 v248, v248, v104, v250 bitop3:0xf6\n"
    "v_bitop3_b32 v248, v248, v105, v250 bitop3:0xf6\n"
    "v_cmp_lt_u32 vcc, 0xffff, v248\n"
    "s_nop 1\n"
    "s_cbranch_vccnz RT17_%=\n"
    "RB17_%=:\n"
    "v_lshlrev_b32 v224, 16, v96\n"
    "v_and_b32 v225, 0xffff0000, v96\n"
    "v_add_f32 v208, v208, v224\n"
    "v_add_f32 v209, v209, v225\n"
    "v_lshlrev_b32 v224, 16, v97\n"
    "v_and_b32 v225, 0xffff0000, v97\n"
    "v_add_f32 v210, v210, v224\n"
    "v_add_f32 v211, v211, v225\n"
    "v_lshlrev_b32 v224, 16, v98\n"
    "v_and_b32 v225, 0xffff0000, v98\n"
    "v_add_f32 v212, v212, v224\n"
    "v_add_f32 v213, v213, v225\n"
    "v_lshlrev_b32 v224, 16, v99\n"
    "v_lshlrev_b32 v225, 16, v104\n"
    "v_add_f32 v214, v214, v224\n"
    "v_add_f32 v215, v215, v225\n"
    "v_lshlrev_b32 v224, 16, v100\n"
    "v_and_b32 v225, 0xffff0000, v100\n"
    "v_add_f32 v216, v216, v224\n"
    "v_add_f32 v217, v217, v225\n"
    "v_lshlrev_b32 v224, 16, v101\n"
    "v_and_b32 v225, 0xffff0000, v101\n"
    "v_add_f32 v218, v218, v224\n"
    "v_add_f32 v219, v219, v225\n"
    "v_lshlrev_b32 v224, 16, v102\n"
    "v_and_b32 v225, 0xffff0000, v102\n"
    "v_add_f32 v220, v220, v224\n"
    "v_add_f32 v221, v221, v225\n"
    "v_lshlrev_b32 v224, 16, v103\n"
    "v_lshlrev_b32 v225, 16, v105\n"
    "v_add_f32 v222, v222, v224\n"
    "v_add_f32 v223, v223, v225\n"
    "s_waitcnt vmcnt(0)\n"
    "v_xor_b32 v248, v3, v250\n"
    "v_bitop3_b32 v248, v248, v7, v250 bitop3:0xf6\n"
    "v_bitop3_b32 v248, v248, v8, v250 bitop3:0xf6\n"
    "v_bitop3_b32 v248, v248, v9, v250 bitop3:0xf6\n"
    "v_cmp_lt_u32 vcc, 0xffff, v248\n"
    "s_nop 1\n"
    "s_cbranch_vccnz RT18_%=\n"
    "RB18_%=:\n"
    "v_lshlrev_b32 v224, 16, v0\n"
    "v_and_b32 v225, 0xffff0000, v0\n"
    "v_add_f32 v208, v208, v224\n"
    "v_add_f32 v209, v209, v225\n"
    "v_lshlrev_b32 v224, 16, v1\n"
    "v_and_b32 v225, 0xffff0000, v1\n"
    "v_add_f32 v210, v210, v224\n"
    "v_add_f32 v211, v211, v225\n"
    "v_lshlrev_b32 v224, 16, v2\n"
    "v_and_b32 v225, 0xffff0000, v2\n"
    "v_add_f32 v212, v212, v224\n"
    "v_add_f32 v213, v213, v225\n"
    "v_lshlrev_b32 v224, 16, v3\n"
    "v_lshlrev_b32 v225, 16, v8\n"
    "v_add_f32 v214, v214, v224\n"
    "v_add_f32 v215, v215, v225\n"
    "v_lshlrev_b32 v224, 16, v4\n"
    "v_and_b32 v225, 0xffff0000, v4\n"
    "v_add_f32 v216, v216, v224\n"
    "v_add_f32 v217, v217, v225\n"
    "v_lshlrev_b32 v224, 16, v5\n"
    "v_and_b32 v225, 0xffff0000, v5\n"
    "v_add_f32 v218, v218, v224\n"
    "v_add_f32 v219, v219, v225\n"
    "v_lshlrev_b32 v224, 16, v6\n"
    "v_and_b32 v225, 0xffff0000, v6\n"
    "v_add_f32 v220, v220, v224\n"
    "v_add_f32 v221, v221, v225\n"
    "v_lshlrev_b32 v224, 16, v7\n"
    "v_lshlrev_b32 v225, 16, v9\n"
    "v_add_f32 v222, v222, v224\n"
    "v_add_f32 v223, v223, v225\n"
    "v_cvt_pk_bf16_f32 v128, v208, v209\n"
    "v_cvt_pk_bf16_f32 v129, v210, v211\n"
    "v_cvt_pk_bf16_f32 v130, v212, v213\n"
    "v_cvt_pk_bf16_f32 v131, v214, v215\n"
    "s_nop 0\n"
    "v_permlane16_swap_b32 v128, v130\n"
    "v_permlane16_swap_b32 v129, v131\n"
    "buffer_store_dwordx4 v[128:131], v251, s[32:35], s62 offen\n"
    "v_cvt_pk_bf16_f32 v132, v216, v217\n"
    "v_cvt_pk_bf16_f32 v133, v218, v219\n"
    "v_cvt_pk_bf16_f32 v134, v220, v221\n"
    "v_cvt_pk_bf16_f32 v135, v222, v223\n"
    "s_nop 0\n"
    "v_permlane16_swap_b32 v132, v134\n"
    "v_permlane16_swap_b32 v133, v135\n"
    "buffer_store_dwordx4 v[132:135], v251, s[32:35], s83 offen\n"
    "buffer_store_dword v226, off, s[20:23], s82 sc1\n"
    "s_endpgm\n"
    "ROLEB_%=:\n"
    "s_and_b32 s85, s7, 1\n"
    "s_mul_i32 s80, s4, 144\n"
    "s_add_u32 s81, s80, 1152\n"
    "s_lshl_b32 s82, s7, 7\n"
    "s_add_u32 s83, s82, 64\n"
    "s_add_u32 s84, s80, 128\n"
    "s_mul_i32 s86, s85, 110592\n"
    "s_add_u32 s86, s86, s81\n"
    "v_add_u32 v224, s82, v255\n"
    "v_mul_u32_u24 v225, 3641, v224\n"
    "v_lshrrev_b32 v225, 16, v225\n"
    "v_mul_u32_u24 v226, 18, v225\n"
    "v_sub_u32 v224, v224, v226\n"
    "v_bfe_u32 v226, v225, 2, 2\n"
    "v_and_b32 v227, 3, v225\n"
    "v_lshl_add_u32 v226, v226, 3, v227\n"
    "v_lshrrev_b32 v225, 4, v225\n"
    "v_lshl_add_u32 v226, v225, 2, v226\n"
    "v_mul_u32_u24 v226, 3456, v226\n"
    "v_cmp_le_u32 vcc, 9, v224\n"
    "v_lshl_add_u32 v226, v224, 4, v226\n"
    "v_add_u32 v226, s81, v226\n"
    "v_add_u32 v227, 1008, v226\n"
    "v_cndmask_b32 v242, v226, v227, vcc\n"
    "v_add_u32 v224, s83, v255\n"
    "v_mul_u32_u24 v225, 3641, v224\n"
    "v_lshrrev_b32 v225, 16, v225\n"
    "v_mul_u32_u24 v226, 18, v225\n"
    "v_sub_u32 v224, v224, v226\n"
    "v_bfe_u32 v226, v225, 2, 2\n"
    "v_and_b32 v227, 3, v225\n"
    "v_lshl_add_u32 v226, v226, 3, v227\n"
    "v_lshrrev_b32 v225, 4, v225\n"
    "v_lshl_add_u32 v226, v225, 2, v226\n"
    "v_mul_u32_u24 v226, 3456, v226\n"
    "v_cmp_le_u32 vcc, 9, v224\n"
    "v_lshl_add_u32 v226, v224, 4, v226\n"
    "v_add_u32 v226, s84, v226\n"
    "v_add_u32 v227, 1008, v226\n"
    "v_cndmask_b32 v243, v226, v227, vcc\n"
    "v_add_u32 v224, 512, v255\n"
    "v_mul_u32_u24 v225, 3641, v224\n"
    "v_lshrrev_b32 v225, 16, v225\n"
    "v_mul_u32_u24 v226, 18, v225\n"
    "v_sub_u32 v224, v224, v226\n"
    "v_bfe_u32 v226, v225, 2, 2\n"
    "v_and_b32 v227, 3, v225\n"
    "v_lshl_add_u32 v226, v226, 3, v227\n"
    "v_lshrrev_b32 v225, 4, v225\n"
    "v_lshl_add_u32 v226, v225, 2, v226\n"
    "v_mul_u32_u24 v226, 3456, v226\n"
    "v_cmp_le_u32 vcc, 9, v224\n"
    "v_lshl_add_u32 v226, v224, 4, v226\n"
    "v_add_u32 v226, s86, v226\n"
    "v_add_u32 v227, 1008, v226\n"
    "v_cndmask_b32 v244, v226, v227, vcc\n"
    "s_lshl_b32 s61, s7, 11\n"
    "s_add_u32 s61, s61, 65536\n"
    "s_mul_i32 s62, s85, 9216\n"
    "s_add_u32 s62, s62, 73728\n"
    "s_mul_i32 s58, s56, 221184\n"
    "s_mul_i32 s59, s56, 221184\n"
    "s_waitcnt lgkmcnt(0)\n"
    "s_add_u32 s8, s8, s57\n"
    "s_addc_u32 s9, s9, 0\n"
    "s_mov_b32 s10, 73728\n"
    "s_add_u32 m0, s61, 0\n"
    "s_nop 0\n"
    "buffer_load_dwordx4 v242, s[16:19], s58 offen offset:0 lds\n"
    "buffer_load_dwordx4 v243, s[16:19], s58 offen offset:1024 lds\n"
    "s_add_u32 m0, s62, 0\n"
    "s_nop 0\n"
    "buffer_load_dwordx4 v244, s[16:19], s58 offen offset:0 lds\n"
    "s_add_u32 m0, s61, 9216\n"
    "s_add_u32 s58, s58, 110592\n"
    "buffer_load_dwordx4 v242, s[16:19], s58 offen offset:0 lds\n"
    "buffer_load_dwordx4 v243, s[16:19], s58 offen offset:1024 lds\n"
    "s_add_u32 m0, s65, 0\n"
    "s_nop 0\n"
    "buffer_load_dwordx4 v247, s[8:11], s60 offen offset:0 lds\n"
    "buffer_load_dwordx4 v248, s[8:11], s60 offen offset:1024 lds\n"
    "buffer_load_dwordx4 v249, s[8:11], s60 offen offset:2048 lds\n"
    "buffer_load_dwordx4 v250, s[8:11], s60 offen offset:3072 lds\n"
    "s_add_u32 m0, s65, 4096\n"
    "s_add_u32 s60, s60, 36864\n"
    "buffer_load_dwordx4 v247, s[8:11], s60 offen offset:0 lds\n"
    "buffer_load_dwordx4 v248, s[8:11], s60 offen offset:1024 lds\n"
    "buffer_load_dwordx4 v249, s[8:11], s60 offen offset:2048 lds\n"
    "buffer_load_dwordx4 v250, s[8:11], s60 offen offset:3072 lds\n"
    "s_lshl_b32 s84, s7, 6\n"
    "s_sub_u32 s86, s86, 1152\n"
    "s_lshl_b32 s63, s7, 10\n"
    "s_add_u32 s63, s63, 102400\n"
    "s_mul_i32 s64, s85, 5120\n"
    "s_add_u32 s64, s64, 106496\n"
    "v_add_u32 v224, s84, v255\n"
    "v_mul_u32_u24 v225, 6554, v224\n"
    "v_lshrrev_b32 v225, 16, v225\n"
    "v_mul_u32_u24 v226, 10, v225\n"
    "v_sub_u32 v224, v224, v226\n"
    "v_bfe_u32 v226, v225, 2, 2\n"
    "v_and_b32 v227, 3, v225\n"
    "v_lshl_add_u32 v226, v226, 3, v227\n"
    "v_lshrrev_b32 v225, 4, v225\n"
    "v_lshl_add_u32 v226, v225, 2, v226\n"
    "v_mul_u32_u24 v226, 3456, v226\n"
    "v_cmp_le_u32 vcc, 9, v224\n"
    "v_lshl_add_u32 v226, v224, 4, v226\n"
    "v_add_u32 v226, s80, v226\n"
    "v_add_u32 v227, -16, v226\n"
    "v_cndmask_b32 v240, v226, v227, vcc\n"
    "v_add_u32 v224, 256, v255\n"
    "v_mul_u32_u24 v225, 6554, v224\n"
    "v_lshrrev_b32 v225, 16, v225\n"
    "v_mul_u32_u24 v226, 10, v225\n"
    "v_sub_u32 v224, v224, v226\n"
    "v_bfe_u32 v226, v225, 2, 2\n"
    "v_and_b32 v227, 3, v225\n"
    "v_lshl_add_u32 v226, v226, 3, v227\n"
    "v_lshrrev_b32 v225, 4, v225\n"
    "v_lshl_add_u32 v226, v225, 2, v226\n"
    "v_mul_u32_u24 v226, 3456, v226\n"
    "v_cmp_le_u32 vcc, 9, v224\n"
    "v_lshl_add_u32 v226, v224, 4, v226\n"
    "v_add_u32 v226, s86, v226\n"
    "v_add_u32 v227, -16, v226\n"
    "v_cndmask_b32 v241, v226, v227, vcc\n"
    "s_add_u32 m0, s63, 0\n"
    "s_nop 0\n"
    "buffer_load_dwordx4 v240, s[16:19], s59 offen lds\n"
    "s_add_u32 m0, s64, 0\n"
    "s_nop 0\n"
    "buffer_load_dwordx4 v241, s[16:19], s59 offen lds\n"
    "s_add_u32 m0, s63, 5120\n"
    "s_add_u32 s59, s59, 110592\n"
    "buffer_load_dwordx4 v240, s[16:19], s59 offen lds\n"
    "v_and_b32 v224, 15, v255\n"
    "v_lshrrev_b32 v225, 4, v255\n"
    "v_lshrrev_b32 v226, 1, v224\n"
    "v_xor_b32 v226, v225, v226\n"
    "v_lshlrev_b32 v226, 4, v226\n"
    "v_lshl_add_u32 v226, v224, 7, v226\n"
    "v_add_u32 v245, s65, v226\n"
    "v_xor_b32 v246, 64, v245\n"
    "v_lshrrev_b32 v224, 2, v255\n"
    "v_and_b32 v225, 3, v255\n"
    "v_mul_u32_u24 v226, 288, v224\n"
    "v_add_u32 v226, 65536, v226\n"
    "v_lshl_add_u32 v239, v225, 3, v226\n"
    "v_mul_u32_u24 v227, 72, v225\n"
    "v_add3_u32 v238, v227, v226, 32\n"
    "v_mul_u32_u24 v226, 160, v224\n"
    "v_add_u32 v226, 102400, v226\n"
    "v_lshl_add_u32 v237, v225, 3, v226\n"
    "v_and_b32 v225, 1, v225\n"
    "v_mul_u32_u24 v227, 72, v225\n"
    "v_add3_u32 v236, v227, v226, 32\n"
    "v_and_b32 v224, 8, v255\n"
    "v_cmp_eq_u32 vcc, 0, v224\n"
    "v_mov_b32 v228, 0x358637bd\n"
    "v_mov_b32 v229, 0x358637bd\n"
    "v_mov_b32 v230, 0x358637bd\n"
    "v_mov_b32 v231, 0x358637bd\n"
    "v_cndmask_b32 v232, 0, v228, vcc\n"
    "v_cndmask_b32 v233, 0, v228, vcc\n"
    "v_cndmask_b32 v234, 0, v228, vcc\n"
    "v_cndmask_b32 v235, 0, v228, vcc\n"
    "v_and_b32 v219, 31, v255\n"
    "v_lshrrev_b32 v224, 5, v255\n"
    "v_lshlrev_b32 v218, 4, v219\n"
    "v_lshlrev_b32 v219, 2, v219\n"
    "v_mul_u32_u24 v217, 36864, v224\n"
    "v_add_u32 v216, v217, v218\n"
    "v_add_u32 v217, v217, v219\n"
    "v_lshl_add_u32 v214, v224, 14, v218\n"
    "v_lshlrev_b32 v254, 4, v255\n"
    "v_lshlrev_b32 v224, 3, v255\n"
    "s_lshl_b32 s81, s4, 2\n"
    "s_add_u32 s81, s81, s5\n"
    "s_mul_i32 s84, s81, 1152\n"
    "s_lshl_b32 s86, s87, 6\n"
    "s_add_u32 s86, s86, s81\n"
    "s_lshl_b32 s86, s86, 9\n"
    "s_add_u32 s86, s86, 1179648\n"
    "s_add_u32 s56, s56, 1\n"
    "s_cmp_eq_u32 s56, 9\n"
    "s_cselect_b32 s56, 0, s56\n"
    "s_lshl_b32 s60, s56, 7\n"
    "s_mul_i32 s58, s56, 221184\n"
    "s_mul_i32 s59, s56, 221184\n"
    "s_mov_b32 s57, 0\n"
    "s_mov_b32 s67, 200000\n"
    "s_getpc_b64 s[82:83]\n"
    "PCXB_%=:\n"
    "s_lshr_b32 s81, %2, 3\n"
    "s_lshl_b32 s81, s81, 2\n"
    "s_add_u32 s81, s81, s7\n"
    "s_lshl_b32 s81, s81, 9\n"
    "s_min_u32 s81, s81, PCE_%=-PC0_%=-512\n"
    "s_sub_u32 s82, s82, PCXB_%=-PC0_%=\n"
    "s_subb_u32 s83, s83, 0\n"
    "s_add_u32 s82, s82, s81\n"
    "s_addc_u32 s83, s83, 0\n"
    "global_load_dwordx2 v[222:223], v224, s[82:83]\n"
    "buffer_load_dwordx4 v[192:195], v216, s[8:11], s84 offen\n"
    "buffer_load_dwordx4 v[196:199], v216, s[8:11], s84 offen offset:512\n"
    "buffer_load_dword v200, v217, s[8:11], s84 offen offset:1024\n"
    "buffer_load_dwordx4 v[204:207], v218, s[36:39], 0 offen\n"
    "buffer_load_dwordx4 v[208:211], v218, s[36:39], 0 offen offset:512\n"
    "buffer_load_dword v212, v219, s[36:39], 0 offen offset:1024\n"
    "buffer_load_dword v213, off, s[20:23], 0 sc1\n"
    "s_add_u32 m0, s65, 8192\n"
    "s_nop 0\n"
    "buffer_load_dwordx4 v247, s[8:11], s60 offen offset:0 lds\n"
    "buffer_load_dwordx4 v248, s[8:11], s60 offen offset:1024 lds\n"
    "buffer_load_dwordx4 v249, s[8:11], s60 offen offset:2048 lds\n"
    "buffer_load_dwordx4 v250, s[8:11], s60 offen offset:3072 lds\n"
    "s_add_u32 m0, s65, 12288\n"
    "s_add_u32 s60, s60, 36864\n"
    "buffer_load_dwordx4 v247, s[8:11], s60 offen offset:0 lds\n"
    "s_waitcnt vmcnt(16)\n"
    "s_barrier\n"
    "ds_read_b64_tr_b16 a[64:65], v239 offset:0\n"
    "ds_read_b64_tr_b16 a[66:67], v239 offset:4608\n"
    "ds_read_b128 a[0:3], v245 offset:0\n"
    "ds_read_b64_tr_b16 a[68:69], v239 offset:9216\n"
    "ds_read_b64_tr_b16 a[70:71], v239 offset:13824\n"
    "ds_read_b128 a[16:19], v246 offset:0\n"
    "ds_read_b128 a[20:23], v246 offset:2048\n"
    "ds_read_b128 a[4:7], v245 offset:2048\n"
    "ds_read_b128 a[24:27], v246 offset:4096\n"
    "ds_read_b128 a[8:11], v245 offset:4096\n"
    "ds_read_b128 a[28:31], v246 offset:6144\n"
    "ds_read_b128 a[12:15], v245 offset:6144\n"
    "ds_read_b64_tr_b16 a[72:73], v239 offset:40\n"
    "ds_read_b64_tr_b16 a[74:75], v239 offset:4648\n"
    "ds_read_b64_tr_b16 a[76:77], v239 offset:9256\n"
    "ds_read_b64_tr_b16 a[78:79], v239 offset:13864\n"
    "s_waitcnt lgkmcnt(13)\n"
    "v_mfma_f32_16x16x32_bf16 v[0:3], a[0:3], a[64:67], v[228:231]\n"
    "ds_read_b64_tr_b16 a[80:81], v239 offset:72\n"
    "s_waitcnt lgkmcnt(11)\n"
    "v_mfma_f32_16x16x32_bf16 v[0:3], a[16:19], a[68:71], v[0:3]\n"
    "buffer_load_dwordx4 v248, s[8:11], s60 offen offset:1024 lds\n"
    "s_waitcnt lgkmcnt(10)\n"
    "v_mfma_f32_16x16x32_bf16 v[4:7], a[20:23], a[68:71], v[228:231]\n"
    "s_lshl_b32 s48, s5, 8\n"
    "ds_read_b64_tr_b16 a[82:83], v239 offset:4680\n"
    "s_waitcnt lgkmcnt(10)\n"
    "v_mfma_f32_16x16x32_bf16 v[4:7], a[4:7], a[64:67], v[4:7]\n"
    "s_lshl_b32 s49, s5, 5\n"
    "s_waitcnt lgkmcnt(8)\n"
    "v_mfma_f32_16x16x32_bf16 v[8:11], a[8:11], a[64:67], v[228:231]\n"
    "s_add_u32 s49, s49, 1024\n"
    "ds_read_b64_tr_b16 a[84:85], v239 offset:9288\n"
    "v_mfma_f32_16x16x32_bf16 v[8:11], a[24:27], a[68:71], v[8:11]\n"
    "s_lshl_b32 s50, s7, 7\n"
    "s_waitcnt lgkmcnt(8)\n"
    "v_mfma_f32_16x16x32_bf16 v[12:15], a[28:31], a[68:71], v[228:231]\n"
    "s_add_u32 s51, s50, 64\n"
    "ds_read_b64_tr_b16 a[86:87], v239 offset:13896\n"
    "s_waitcnt lgkmcnt(8)\n"
    "v_mfma_f32_16x16x32_bf16 v[12:15], a[12:15], a[64:67], v[12:15]\n"
    "s_waitcnt lgkmcnt(4)\n"
    "buffer_load_dwordx4 v249, s[8:11], s60 offen offset:2048 lds\n"
    "v_mfma_f32_16x16x32_bf16 v[28:31], a[12:15], a[72:75], v[228:231]\n"
    "s_sub_u32 s52, s48, 1024\n"
    "ds_read_b64_tr_b16 a[88:89], v239 offset:112\n"
    "v_mfma_f32_16x16x32_bf16 v[28:31], a[28:31], a[76:79], v[28:31]\n"
    "s_sub_u32 s53, s49, 1024\n"
    "v_mfma_f32_16x16x32_bf16 v[24:27], a[24:27], a[76:79], v[228:231]\n"
    "s_and_b32 s54, s7, 1\n"
    "ds_read_b64_tr_b16 a[90:91], v239 offset:4720\n"
    "v_mfma_f32_16x16x32_bf16 v[24:27], a[8:11], a[72:75], v[24:27]\n"
    "s_mul_i32 s55, s54, 36864\n"
    "buffer_load_dwordx4 v250, s[8:11], s60 offen offset:3072 lds\n"
    "v_mfma_f32_16x16x32_bf16 v[20:23], a[4:7], a[72:75], v[228:231]\n"
    "s_add_u32 m0, s61, 18432\n"
    "ds_read_b64_tr_b16 a[92:93], v239 offset:9328\n"
    "v_mfma_f32_16x16x32_bf16 v[20:23], a[20:23], a[76:79], v[20:23]\n"
    "s_add_u32 s54, s55, s48\n"
    "v_mfma_f32_16x16x32_bf16 v[16:19], a[16:19], a[76:79], v[228:231]\n"
    "s_add_u32 s55, s55, s49\n"
    "ds_read_b64_tr_b16 a[94:95], v239 offset:13936\n"
    "v_mfma_f32_16x16x32_bf16 v[16:19], a[0:3], a[72:75], v[16:19]\n"
    "s_waitcnt lgkmcnt(4)\n"
    "v_add_u32 v224, s50, v255\n"
    "v_mul_u32_u24 v225, 3641, v224\n"
    "v_mfma_f32_16x16x32_bf16 v[32:35], a[0:3], a[80:83], v[228:231]\n"
    "s_cmp_eq_u32 s5, 3\n"
    "ds_read_b64_tr_b16 a[96:97], v238 offset:0\n"
    "v_lshrrev_b32 v225, 16, v225\n"
    "v_mfma_f32_16x16x32_bf16 v[32:35], a[16:19], a[84:87], v[32:35]\n"
    "s_cselect_b32 s81, 0, s5\n"
    "buffer_load_dwordx4 v242, s[16:19], s58 offen offset:0 lds\n"
    "v_mfma_f32_16x16x32_bf16 v[36:39], a[20:23], a[84:87], v[228:231]\n"
    "s_mul_i32 s36, s4, 82944\n"
    "ds_read_b64_tr_b16 a[98:99], v238 offset:4608\n"
    "v_mul_u32_u24 v226, 18, v225\n"
    "v_mfma_f32_16x16x32_bf16 v[36:39], a[4:7], a[80:83], v[36:39]\n"
    "s_mul_i32 s83, s4, 663552\n"
    "v_sub_u32 v224, v224, v226\n"
    "v_lshrrev_b32 v226, 1, v224\n"
    "v_mfma_f32_16x16x32_bf16 v[40:43], a[8:11], a[80:83], v[228:231]\n"
    "s_and_b32 s77, %2, 0xe7\n"
    "ds_read_b64_tr_b16 a[100:101], v238 offset:9216\n"
    "v_cmp_gt_u32 vcc, 2, v224\n"
    "v_mfma_f32_16x16x32_bf16 v[40:43], a[24:27], a[84:87], v[40:43]\n"
    "s_lshl_b32 s77, s77, 2\n"
    "buffer_load_dwordx4 v243, s[16:19], s58 offen offset:1024 lds\n"
    "v_mfma_f32_16x16x32_bf16 v[44:47], a[28:31], a[84:87], v[228:231]\n"
    "s_add_u32 m0, s62, 18432\n"
    "ds_read_b64_tr_b16 a[102:103], v238 offset:13824\n"
    "v_add_u32 v226, s4, v226\n"
    "v_mfma_f32_16x16x32_bf16 v[44:47], a[12:15], a[80:83], v[44:47]\n"
    "s_waitcnt lgkmcnt(4)\n"
    "v_and_b32 v226, 7, v226\n"
    "v_lshl_add_u32 v226, v226, 5, s48\n"
    "v_mfma_f32_16x16x32_bf16 v[60:63], a[12:15], a[88:91], v[228:231]\n"
    "s_add_u32 s77, s77, s7\n"
    "ds_read_b64_tr_b16 a[104:105], v239 offset:144\n"
    "v_mov_b32 v227, s49\n"
    "v_mfma_f32_16x16x32_bf16 v[60:63], a[28:31], a[92:95], v[60:63]\n"
    "s_mul_i32 s75, s77, 24576\n"
    "v_cndmask_b32 v226, v226, v227, vcc\n"
    "v_and_b32 v224, 1, v224\n"
    "v_mfma_f32_16x16x32_bf16 v[56:59], a[24:27], a[92:95], v[228:231]\n"
    "s_add_u32 s75, s75, 4194304\n"
    "ds_read_b64_tr_b16 a[106:107], v239 offset:4752\n"
    "v_mul_u32_u24 v225, 1152, v225\n"
    "v_mfma_f32_16x16x32_bf16 v[56:59], a[8:11], a[88:91], v[56:59]\n"
    "s_mul_i32 s82, s81, 221184\n"
    "buffer_load_dwordx4 v244, s[16:19], s58 offen offset:0 lds\n"
    "v_mfma_f32_16x16x32_bf16 v[52:55], a[4:7], a[88:91], v[228:231]\n"
    "s_add_u32 m0, s61, 27648\n"
    "ds_read_b64_tr_b16 a[108:109], v239 offset:9360\n"
    "v_lshl_add_u32 v224, v224, 4, v225\n"
    "v_mfma_f32_16x16x32_bf16 v[52:55], a[20:23], a[92:95], v[52:55]\n"
    "s_add_u32 s58, s58, 110592\n"
    "v_add_u32 v251, v224, v226\n"
    "v_add_u32 v224, s51, v255\n"
    "v_mfma_f32_16x16x32_bf16 v[48:51], a[16:19], a[92:95], v[228:231]\n"
    "s_add_u32 s68, s83, s82\n"
    "ds_read_b64_tr_b16 a[110:111], v239 offset:13968\n"
    "v_mul_u32_u24 v225, 3641, v224\n"
    "v_mfma_f32_16x16x32_bf16 v[48:51], a[0:3], a[88:91], v[48:51]\n"
    "s_waitcnt lgkmcnt(4)\n"
    "buffer_load_dwordx4 v242, s[16:19], s58 offen offset:0 lds\n"
    "v_mfma_f32_16x16x32_bf16 v[64:67], a[0:3], a[96:99], v[232:235]\n"
    "s_mul_i32 s82, s81, 786432\n"
    "ds_read_b64_tr_b16 a[112:113], v239 offset:184\n"
    "v_lshrrev_b32 v225, 16, v225\n"
    "v_mfma_f32_16x16x32_bf16 v[64:67], a[16:19], a[100:103], v[64:67]\n"
    "s_add_u32 s71, s75, s82\n"
    "v_mul_u32_u24 v226, 18, v225\n"
    "v_sub_u32 v224, v224, v226\n"
    "v_mfma_f32_16x16x32_bf16 v[68:71], a[20:23], a[100:103], v[232:235]\n"
    "s_add_u32 s81, s81, 1\n"
    "ds_read_b64_tr_b16 a[114:115], v239 offset:4792\n"
    "v_lshrrev_b32 v226, 1, v224\n"
    "v_mfma_f32_16x16x32_bf16 v[68:71], a[4:7], a[96:99], v[68:71]\n"
    "s_cmp_eq_u32 s81, 3\n"
    "v_cmp_gt_u32 vcc, 2, v224\n"
    "v_add_u32 v226, s4, v226\n"
    "v_mfma_f32_16x16x32_bf16 v[72:75], a[8:11], a[96:99], v[232:235]\n"
    "s_cselect_b32 s81, 0, s81\n"
    "ds_read_b64_tr_b16 a[116:117], v239 offset:9400\n"
    "v_and_b32 v226, 7, v226\n"
    "v_mfma_f32_16x16x32_bf16 v[72:75], a[24:27], a[100:103], v[72:75]\n"
    "s_mul_i32 s82, s81, 221184\n"
    "buffer_load_dwordx4 v243, s[16:19], s58 offen offset:1024 lds\n"
    "v_mfma_f32_16x16x32_bf16 v[76:79], a[28:31], a[100:103], v[232:235]\n"
    "s_add_u32 m0, s63, 10240\n"
    "ds_read_b64_tr_b16 a[118:119], v239 offset:14008\n"
    "v_lshl_add_u32 v226, v226, 5, s52\n"
    "v_mfma_f32_16x16x32_bf16 v[76:79], a[12:15], a[96:99], v[76:79]\n"
    "s_waitcnt lgkmcnt(4)\n"
    "v_mov_b32 v227, s53\n"
    "v_cndmask_b32 v226, v226, v227, vcc\n"
    "v_mfma_f32_16x16x32_bf16 v[92:95], a[12:15], a[104:107], 0\n"
    "s_add_u32 s69, s83, s82\n"
    "ds_read_b64_tr_b16 a[120:121], v239 offset:216\n"
    "v_and_b32 v224, 1, v224\n"
    "v_mfma_f32_16x16x32_bf16 v[92:95], a[28:31], a[108:111], v[92:95]\n"
    "s_mul_i32 s82, s81, 786432\n"
    "buffer_load_dwordx4 v240, s[16:19], s59 offen lds\n"
    "v_mfma_f32_16x16x32_bf16 v[88:91], a[24:27], a[108:111], 0\n"
    "s_waitcnt vmcnt(22)\n"
    "ds_read_b64_tr_b16 a[122:123], v239 offset:4824\n"
    "v_mul_u32_u24 v225, 1152, v225\n"
    "v_mfma_f32_16x16x32_bf16 v[88:91], a[8:11], a[104:107], v[88:91]\n"
    "s_add_u32 m0, s64, 10240\n"
    "v_lshl_add_u32 v224, v224, 4, v225\n"
    "v_add_u32 v252, v224, v226\n"
    "v_mfma_f32_16x16x32_bf16 v[84:87], a[4:7], a[104:107], 0\n"
    "s_barrier\n"
    "ds_read_b64_tr_b16 a[124:125], v239 offset:9432\n"
    "v_add_u32 v224, 512, v255\n"
    "v_mfma_f32_16x16x32_bf16 v[84:87], a[20:23], a[108:111], v[84:87]\n"
    "s_add_u32 s72, s75, s82\n"
    "ds_read_b64_tr_b16 a[126:127], v239 offset:14040\n"
    "v_mul_u32_u24 v225, 3641, v224\n"
    "v_mfma_f32_16x16x32_bf16 v[80:83], a[16:19], a[108:111], 0\n"
    "s_add_u32 s81, s81, 1\n"
    "ds_read_b64_tr_b16 a[128:129], v239 offset:256\n"
    "v_lshrrev_b32 v225, 16, v225\n"
    "v_mfma_f32_16x16x32_bf16 v[80:83], a[0:3], a[104:107], v[80:83]\n"
    "s_waitcnt lgkmcnt(5)\n"
    "buffer_load_dwordx4 v241, s[16:19], s59 offen lds\n"
    "v_mfma_f32_16x16x32_bf16 v[96:99], a[0:3], a[112:115], 0\n"
    "s_add_u32 m0, s63, 15360\n"
    "ds_read_b64_tr_b16 a[130:131], v239 offset:4864\n"
    "v_mul_u32_u24 v226, 18, v225\n"
    "v_mfma_f32_16x16x32_bf16 v[96:99], a[16:19], a[116:119], v[96:99]\n"
    "s_add_u32 s59, s59, 110592\n"
    "ds_read_b64_tr_b16 a[132:133], v239 offset:9472\n"
    "v_sub_u32 v224, v224, v226\n"
    "v_mfma_f32_16x16x32_bf16 v[100:103], a[20:23], a[116:119], 0\n"
    "s_cmp_eq_u32 s81, 3\n"
    "ds_read_b64_tr_b16 a[134:135], v239 offset:14080\n"
    "v_lshrrev_b32 v226, 1, v224\n"
    "v_mfma_f32_16x16x32_bf16 v[100:103], a[4:7], a[112:115], v[100:103]\n"
    "s_cselect_b32 s81, 0, s81\n"
    "buffer_load_dwordx4 v240, s[16:19], s59 offen lds\n"
    "v_mfma_f32_16x16x32_bf16 v[104:107], a[8:11], a[112:115], 0\n"
    "s_add_u32 s56, s56, 1\n"
    "s_cmp_eq_u32 s56, 9\n"
    "s_cselect_b32 s56, 0, s56\n"
    "s_lshl_b32 s60, s56, 7\n"
    "ds_read_b64_tr_b16 a[136:137], v237 offset:0\n"
    "v_cmp_gt_u32 vcc, 2, v224\n"
    "v_mfma_f32_16x16x32_bf16 v[104:107], a[24:27], a[116:119], v[104:107]\n"
    "s_add_u32 m0, s65, 0\n"
    "ds_read_b64_tr_b16 a[138:139], v237 offset:2560\n"
    "v_add_u32 v226, s4, v226\n"
    "v_mfma_f32_16x16x32_bf16 v[108:111], a[28:31], a[116:119], 0\n"
    "s_mul_i32 s82, s81, 221184\n"
    "ds_read_b64_tr_b16 a[140:141], v237 offset:5120\n"
    "v_and_b32 v226, 7, v226\n"
    "v_mfma_f32_16x16x32_bf16 v[108:111], a[12:15], a[112:115], v[108:111]\n"
    "s_waitcnt lgkmcnt(7)\n"
    "v_lshl_add_u32 v226, v226, 5, s54\n"
    "v_mov_b32 v227, s55\n"
    "v_mfma_f32_16x16x32_bf16 v[124:127], a[12:15], a[120:123], 0\n"
    "s_add_u32 s70, s83, s82\n"
    "ds_read_b64_tr_b16 a[142:143], v237 offset:7680\n"
    "v_cndmask_b32 v226, v226, v227, vcc\n"
    "v_mfma_f32_16x16x32_bf16 v[124:127], a[28:31], a[124:127], v[124:127]\n"
    "s_mul_i32 s82, s81, 786432\n"
    "buffer_load_dwordx4 v247, s[8:11], s60 offen offset:0 lds\n"
    "v_mfma_f32_16x16x32_bf16 v[120:123], a[24:27], a[124:127], 0\n"
    "s_mul_i32 s58, s56, 221184\n"
    "ds_read_b64_tr_b16 a[144:145], v237 offset:40\n"
    "v_and_b32 v224, 1, v224\n"
    "v_mfma_f32_16x16x32_bf16 v[120:123], a[8:11], a[120:123], v[120:123]\n"
    "s_mul_i32 s59, s56, 221184\n"
    "ds_read_b64_tr_b16 a[146:147], v237 offset:2600\n"
    "v_mul_u32_u24 v225, 1152, v225\n"
    "v_mfma_f32_16x16x32_bf16 v[116:119], a[4:7], a[120:123], 0\n"
    "s_add_u32 s57, s57, 1\n"
    "ds_read_b64_tr_b16 a[148:149], v237 offset:5160\n"
    "v_lshl_add_u32 v224, v224, 4, v225\n"
    "v_mfma_f32_16x16x32_bf16 v[116:119], a[20:23], a[124:127], v[116:119]\n"
    "s_add_u32 s73, s75, s82\n"
    "buffer_load_dwordx4 v248, s[8:11], s60 offen offset:1024 lds\n"
    "v_mfma_f32_16x16x32_bf16 v[112:115], a[16:19], a[124:127], 0\n"
    "ds_read_b64_tr_b16 a[150:151], v237 offset:7720\n"
    "v_add_u32 v253, v224, v226\n"
    "v_mfma_f32_16x16x32_bf16 v[112:115], a[0:3], a[120:123], v[112:115]\n"
    "s_waitcnt lgkmcnt(8)\n"
    "v_mfma_f32_16x16x32_bf16 v[128:131], a[0:3], a[128:131], 0\n"
    "ds_read_b64_tr_b16 a[152:153], v237 offset:72\n"
    "v_mfma_f32_16x16x32_bf16 v[128:131], a[16:19], a[132:135], v[128:131]\n"
    "v_mfma_f32_16x16x32_bf16 v[132:135], a[20:23], a[132:135], 0\n"
    "ds_read_b64_tr_b16 a[154:155], v237 offset:2632\n"
    "v_mfma_f32_16x16x32_bf16 v[132:135], a[4:7], a[128:131], v[132:135]\n"
    "buffer_load_dwordx4 v249, s[8:11], s60 offen offset:2048 lds\n"
    "v_mfma_f32_16x16x32_bf16 v[136:139], a[8:11], a[128:131], 0\n"
    "ds_read_b64_tr_b16 a[156:157], v237 offset:5192\n"
    "v_mfma_f32_16x16x32_bf16 v[136:139], a[24:27], a[132:135], v[136:139]\n"
    "v_mfma_f32_16x16x32_bf16 v[140:143], a[28:31], a[132:135], 0\n"
    "ds_read_b64_tr_b16 a[158:159], v237 offset:7752\n"
    "v_mfma_f32_16x16x32_bf16 v[140:143], a[12:15], a[128:131], v[140:143]\n"
    "s_waitcnt lgkmcnt(8)\n"
    "buffer_load_dwordx4 v250, s[8:11], s60 offen offset:3072 lds\n"
    "v_mfma_f32_16x16x32_bf16 v[156:159], a[136:139], a[12:15], v[228:231]\n"
    "s_add_u32 m0, s65, 4096\n"
    "ds_read_b64_tr_b16 a[160:161], v237 offset:112\n"
    "v_mfma_f32_16x16x32_bf16 v[156:159], a[140:143], a[28:31], v[156:159]\n"
    "s_add_u32 s60, s60, 36864\n"
    "v_mfma_f32_16x16x32_bf16 v[152:155], a[140:143], a[24:27], v[228:231]\n"
    "ds_read_b64_tr_b16 a[162:163], v237 offset:2672\n"
    "v_mfma_f32_16x16x32_bf16 v[152:155], a[136:139], a[8:11], v[152:155]\n"
    "v_mfma_f32_16x16x32_bf16 v[148:151], a[136:139], a[4:7], v[228:231]\n"
    "ds_read_b64_tr_b16 a[164:165], v237 offset:5232\n"
    "v_mfma_f32_16x16x32_bf16 v[148:151], a[140:143], a[20:23], v[148:151]\n"
    "buffer_load_dwordx4 v247, s[8:11], s60 offen offset:0 lds\n"
    "v_mfma_f32_16x16x32_bf16 v[144:147], a[140:143], a[16:19], v[228:231]\n"
    "ds_read_b64_tr_b16 a[166:167], v237 offset:7792\n"
    "v_mfma_f32_16x16x32_bf16 v[144:147], a[136:139], a[0:3], v[144:147]\n"
    "s_waitcnt lgkmcnt(8)\n"
    "s_mov_b32 s80, 0x3f803f80\n"
    "s_waitcnt vmcnt(21)\n"
    "v_readfirstlane_b32 s66, v213\n"
    "v_dot2_f32_bf16 v201, v192, s80, 0\n"
    "v_dot2_f32_bf16 v202, v192, v192, 0\n"
    "v_dot2_f32_bf16 v203, v192, v204, 0\n"
    "v_dot2_f32_bf16 v201, v193, s80, v201\n"
    "v_dot2_f32_bf16 v202, v193, v193, v202\n"
    "v_dot2_f32_bf16 v203, v193, v205, v203\n"
    "v_dot2_f32_bf16 v201, v194, s80, v201\n"
    "v_dot2_f32_bf16 v202, v194, v194, v202\n"
    "v_dot2_f32_bf16 v203, v194, v206, v203\n"
    "v_dot2_f32_bf16 v201, v195, s80, v201\n"
    "v_dot2_f32_bf16 v202, v195, v195, v202\n"
    "v_dot2_f32_bf16 v203, v195, v207, v203\n"
    "v_dot2_f32_bf16 v201, v196, s80, v201\n"
    "v_dot2_f32_bf16 v202, v196, v196, v202\n"
    "v_dot2_f32_bf16 v203, v196, v208, v203\n"
    "v_dot2_f32_bf16 v201, v197, s80, v201\n"
    "v_dot2_f32_bf16 v202, v197, v197, v202\n"
    "v_dot2_f32_bf16 v203, v197, v209, v203\n"
    "v_dot2_f32_bf16 v201, v198, s80, v201\n"
    "v_dot2_f32_bf16 v202, v198, v198, v202\n"
    "v_dot2_f32_bf16 v203, v198, v210, v203\n"
    "v_dot2_f32_bf16 v201, v199, s80, v201\n"
    "v_dot2_f32_bf16 v202, v199, v199, v202\n"
    "v_dot2_f32_bf16 v203, v199, v211, v203\n"
    "v_dot2_f32_bf16 v201, v200, s80, v201\n"
    "v_dot2_f32_bf16 v202, v200, v200, v202\n"
    "v_dot2_f32_bf16 v203, v200, v212, v203\n"
    "s_mul_i32 s66, s66, 1664525\n"
    "s_add_u32 s66, s66, 1013904223\n"
    "v_add_f32_dpp v201, v201, v201 quad_perm:[1,0,3,2] row_mask:0xf bank_mask:0xf\n"
    "v_add_f32_dpp v202, v202, v202 quad_perm:[1,0,3,2] row_mask:0xf bank_mask:0xf\n"
    "v_add_f32_dpp v203, v203, v203 quad_perm:[1,0,3,2] row_mask:0xf bank_mask:0xf\n"
    "v_add_f32_dpp v201, v201, v201 quad_perm:[2,3,0,1] row_mask:0xf bank_mask:0xf\n"
    "v_add_f32_dpp v202, v202, v202 quad_perm:[2,3,0,1] row_mask:0xf bank_mask:0xf\n"
    "v_add_f32_dpp v203, v203, v203 quad_perm:[2,3,0,1] row_mask:0xf bank_mask:0xf\n"
    "v_add_f32_dpp v201, v201, v201 row_half_mirror row_mask:0xf bank_mask:0xf\n"
    "v_add_f32_dpp v202, v202, v202 row_half_mirror row_mask:0xf bank_mask:0xf\n"
    "v_add_f32_dpp v203, v203, v203 row_half_mirror row_mask:0xf bank_mask:0xf\n"
    "v_add_f32_dpp v201, v201, v201 row_mirror row_mask:0xf bank_mask:0xf\n"
    "v_add_f32_dpp v202, v202, v202 row_mirror row_mask:0xf bank_mask:0xf\n"
    "v_add_f32_dpp v203, v203, v203 row_mirror row_mask:0xf bank_mask:0xf\n"
    "v_mov_b32 v204, v201\n"
    "v_mov_b32 v205, v202\n"
    "v_mov_b32 v206, v203\n"
    "v_permlane16_swap_b32 v201, v204\n"
    "v_permlane16_swap_b32 v202, v205\n"
    "v_permlane16_swap_b32 v203, v206\n"
    "v_add_f32 v201, v201, v204\n"
    "v_add_f32 v202, v202, v205\n"
    "v_add_f32 v194, v203, v206\n"
    "v_mul_f32 v201, 0xbae38e39, v201\n"
    "v_mul_f32 v202, 0x3ae38e39, v202\n"
    "v_fma_f32 v202, -v201, v201, v202\n"
    "v_max_f32 v202, 0, v202\n"
    "v_add_f32 v202, 0x3727c5ac, v202\n"
    "v_rsq_f32 v192, v202\n"
    "v_mov_b32 v195, s66\n"
    "v_mul_f32 v193, v201, v192\n"
    "buffer_store_dwordx4 v[192:195], v214, s[20:23], s86 offen sc1\n"
    "s_mul_i32 s82, s5, 786432\n"
    "s_add_u32 s74, s75, s82\n"
    "s_add_u32 s75, s75, 2359296\n"
    "s_lshl_b32 s77, s77, 10\n"
    "s_add_u32 s77, s77, 65536\n"
    "s_lshl_b32 s82, s5, 15\n"
    "s_add_u32 s76, s77, s82\n"
    "s_lshl_b32 s78, s7, 11\n"
    "s_and_b32 s79, s7, 1\n"
    "s_mul_i32 s79, s79, 9216\n"
    "s_add_u32 s79, s79, 8192\n"
    "s_lshl_b32 s37, s87, 15\n"
    "s_add_u32 s37, s37, 1179648\n"
    "s_lshl_b32 s39, s4, 2\n"
    "s_add_u32 s39, s39, s7\n"
    "s_lshl_b32 s39, s39, 14\n"
    "s_add_u32 s39, s39, 2228224\n"
    "s_lshl_b32 s38, s6, 11\n"
    "s_add_u32 s38, s38, s39\n"
    "v_mfma_f32_16x16x32_bf16 v[160:163], a[144:147], a[0:3], v[228:231]\n"
    "s_waitcnt vmcnt(14)\n"
    "ds_read_b64_tr_b16 a[168:169], v236 offset:0\n"
    "v_mfma_f32_16x16x32_bf16 v[160:163], a[148:151], a[16:19], v[160:163]\n"
    "s_lshr_b32 s81, %2, 3\n"
    "buffer_load_dwordx4 v248, s[8:11], s60 offen offset:1024 lds\n"
    "v_mfma_f32_16x16x32_bf16 v[164:167], a[148:151], a[20:23], v[228:231]\n"
    "s_lshl_b32 s81, s81, 2\n"
    "ds_read_b64_tr_b16 a[170:171], v236 offset:2560\n"
    "v_mfma_f32_16x16x32_bf16 v[164:167], a[144:147], a[4:7], v[164:167]\n"
    "s_add_u32 s81, s81, s7\n"
    "ds_read_b128 a[32:35], v245 offset:8192\n"
    "v_mfma_f32_16x16x32_bf16 v[168:171], a[144:147], a[8:11], v[228:231]\n"
    "s_mul_i32 s82, s81, 7282\n"
    "ds_read_b64_tr_b16 a[172:173], v236 offset:5120\n"
    "v_mfma_f32_16x16x32_bf16 v[168:171], a[148:151], a[24:27], v[168:171]\n"
    "s_lshr_b32 s82, s82, 16\n"
    "ds_read_b128 a[48:51], v246 offset:8192\n"
    "v_mfma_f32_16x16x32_bf16 v[172:175], a[148:151], a[28:31], v[228:231]\n"
    "s_mul_i32 s83, s82, 9\n"
    "ds_read_b64_tr_b16 a[174:175], v236 offset:7680\n"
    "v_mfma_f32_16x16x32_bf16 v[172:175], a[144:147], a[12:15], v[172:175]\n"
    "s_waitcnt lgkmcnt(10)\n"
    "buffer_load_dwordx4 v249, s[8:11], s60 offen offset:2048 lds\n"
    "v_mfma_f32_16x16x32_bf16 v[188:191], a[152:155], a[12:15], v[228:231]\n"
    "s_sub_u32 s81, s81, s83\n"
    "ds_read_b128 a[36:39], v245 offset:10240\n"
    "v_mfma_f32_16x16x32_bf16 v[188:191], a[156:159], a[28:31], v[188:191]\n"
    "s_lshl_b32 s81, s81, 7\n"
    "ds_read_b128 a[52:55], v246 offset:10240\n"
    "v_mfma_f32_16x16x32_bf16 v[184:187], a[156:159], a[24:27], v[228:231]\n"
    "s_mul_i32 s83, s82, 73728\n"
    "ds_read_b128 a[40:43], v245 offset:12288\n"
    "v_mfma_f32_16x16x32_bf16 v[184:187], a[152:155], a[8:11], v[184:187]\n"
    "s_add_u32 s81, s81, s83\n"
    "buffer_load_dwordx4 v250, s[8:11], s60 offen offset:3072 lds\n"
    "v_mfma_f32_16x16x32_bf16 v[180:183], a[152:155], a[4:7], v[228:231]\n"
    "s_add_u32 m0, s61, 0\n"
    "ds_read_b128 a[56:59], v246 offset:12288\n"
    "v_mfma_f32_16x16x32_bf16 v[180:183], a[156:159], a[20:23], v[180:183]\n"
    "s_waitcnt vmcnt(9)\n"
    "ds_read_b128 a[44:47], v245 offset:14336\n"
    "v_mfma_f32_16x16x32_bf16 v[176:179], a[156:159], a[16:19], v[228:231]\n"
    "s_waitcnt lgkmcnt(5)\n"
    "ds_read_b128 a[60:63], v246 offset:14336\n"
    "v_mfma_f32_16x16x32_bf16 v[176:179], a[152:155], a[0:3], v[176:179]\n"
    "v_mfma_f32_16x16x32_bf16 v[192:195], a[160:163], a[0:3], v[228:231]\n"
    "s_barrier\n"
    "ds_read_b64_tr_b16 a[64:65], v239 offset:18432\n"
    "v_mfma_f32_16x16x32_bf16 v[192:195], a[164:167], a[16:19], v[192:195]\n"
    "s_mul_i32 s83, s4, 663552\n"
    "buffer_load_dwordx4 v242, s[16:19], s58 offen offset:0 lds\n"
    "v_mfma_f32_16x16x32_bf16 v[196:199], a[164:167], a[20:23], v[228:231]\n"
    "s_add_u32 s50, s81, s83\n"
    "ds_read_b64_tr_b16 a[66:67], v239 offset:23040\n"
    "v_mfma_f32_16x16x32_bf16 v[196:199], a[160:163], a[4:7], v[196:199]\n"
    "s_add_u32 s81, s81, s36\n"
    "v_mfma_f32_16x16x32_bf16 v[200:203], a[160:163], a[8:11], v[228:231]\n"
    "s_sub_u32 s81, s81, 663552\n"
    "ds_read_b64_tr_b16 a[68:69], v239 offset:27648\n"
    "v_mfma_f32_16x16x32_bf16 v[200:203], a[164:167], a[24:27], v[200:203]\n"
    "v_mfma_f32_16x16x32_bf16 v[204:207], a[164:167], a[28:31], v[228:231]\n"
    "ds_read_b64_tr_b16 a[70:71], v239 offset:32256\n"
    "v_mfma_f32_16x16x32_bf16 v[204:207], a[160:163], a[12:15], v[204:207]\n"
    "buffer_load_dwordx4 v243, s[16:19], s58 offen offset:1024 lds\n"
    "v_mfma_f32_16x16x32_bf16 v[220:223], a[168:171], a[12:15], v[228:231]\n"
    "s_add_u32 m0, s62, 0\n"
    "ds_read_b64_tr_b16 a[72:73], v239 offset:18472\n"
    "v_mfma_f32_16x16x32_bf16 v[220:223], a[172:175], a[28:31], v[220:223]\n"
    "s_cmp_lt_u32 s82, 9\n"
    "v_mfma_f32_16x16x32_bf16 v[216:219], a[172:175], a[24:27], v[228:231]\n"
    "s_cselect_b32 s50, s50, s81\n"
    "ds_read_b64_tr_b16 a[74:75], v239 offset:23080\n"
    "v_mfma_f32_16x16x32_bf16 v[216:219], a[168:171], a[8:11], v[216:219]\n"
    "s_cselect_b64 s[48:49], s[24:25], s[28:29]\n"
    "v_mfma_f32_16x16x32_bf16 v[212:215], a[168:171], a[4:7], v[228:231]\n"
    "s_cselect_b32 s51, s26, s30\n"
    "ds_read_b64_tr_b16 a[76:77], v239 offset:27688\n"
    "v_mfma_f32_16x16x32_bf16 v[212:215], a[172:175], a[20:23], v[212:215]\n"
    "s_cmp_lt_u32 s82, 11\n"
    "s_cselect_b32 s51, s51, 0\n"
    "buffer_load_dwordx4 v244, s[16:19], s58 offen offset:0 lds\n"
    "v_mfma_f32_16x16x32_bf16 v[208:211], a[172:175], a[16:19], v[228:231]\n"
    "s_waitcnt lgkmcnt(3)\n"
    "ds_read_b64_tr_b16 a[78:79], v239 offset:32296\n"
    "v_mfma_f32_16x16x32_bf16 v[208:211], a[168:171], a[0:3], v[208:211]\n"
    "LP1B_%=:\n"
    "v_mfma_f32_16x16x32_bf16 v[0:3], a[32:35], a[64:67], v[0:3]\n"
    "s_add_u32 m0, s61, 9216\n"
    "ds_read_b64_tr_b16 a[80:81], v239 offset:18504\n"
    "v_mfma_f32_16x16x32_bf16 v[0:3], a[48:51], a[68:71], v[0:3]\n"
    "s_add_u32 s58, s58, 110592\n"
    "ds_read_b64_tr_b16 a[82:83], v239 offset:23112\n"
    "v_mfma_f32_16x16x32_bf16 v[4:7], a[52:55], a[68:71], v[4:7]\n"
    "ds_read_b64_tr_b16 a[84:85], v239 offset:27720\n"
    "v_mfma_f32_16x16x32_bf16 v[4:7], a[36:39], a[64:67], v[4:7]\n"
    "buffer_load_dwordx4 v242, s[16:19], s58 offen offset:0 lds\n"
    "v_mfma_f32_16x16x32_bf16 v[8:11], a[40:43], a[64:67], v[8:11]\n"
    "ds_read_b64_tr_b16 a[86:87], v239 offset:32328\n"
    "v_mfma_f32_16x16x32_bf16 v[8:11], a[56:59], a[68:71], v[8:11]\n"
    "v_mfma_f32_16x16x32_bf16 v[12:15], a[60:63], a[68:71], v[12:15]\n"
    "ds_read_b64_tr_b16 a[88:89], v239 offset:18544\n"
    "v_mfma_f32_16x16x32_bf16 v[12:15], a[44:47], a[64:67], v[12:15]\n"
    "s_waitcnt lgkmcnt(5)\n"
    "v_mfma_f32_16x16x32_bf16 v[28:31], a[44:47], a[72:75], v[28:31]\n"
    "s_add_u32 s56, s56, 1\n"
    "ds_read_b64_tr_b16 a[90:91], v239 offset:23152\n"
    "v_mfma_f32_16x16x32_bf16 v[28:31], a[60:63], a[76:79], v[28:31]\n"
    "s_cmp_eq_u32 s56, 9\n"
    "v_mfma_f32_16x16x32_bf16 v[24:27], a[56:59], a[76:79], v[24:27]\n"
    "s_cselect_b32 s56, 0, s56\n"
    "ds_read_b64_tr_b16 a[92:93], v239 offset:27760\n"
    "v_mfma_f32_16x16x32_bf16 v[24:27], a[40:43], a[72:75], v[24:27]\n"
    "buffer_load_dwordx4 v243, s[16:19], s58 offen offset:1024 lds\n"
    "v_mfma_f32_16x16x32_bf16 v[20:23], a[36:39], a[72:75], v[20:23]\n"
    "s_add_u32 m0, s63, 0\n"
    "ds_read_b64_tr_b16 a[94:95], v239 offset:32368\n"
    "v_mfma_f32_16x16x32_bf16 v[20:23], a[52:55], a[76:79], v[20:23]\n"
    "s_cmp_ge_u32 s57, 7\n"
    "ds_read_b64_tr_b16 a[96:97], v238 offset:18432\n"
    "v_mfma_f32_16x16x32_bf16 v[16:19], a[48:51], a[76:79], v[16:19]\n"
    "s_cselect_b32 s56, 7000, s56\n"
    "ds_read_b64_tr_b16 a[98:99], v238 offset:23040\n"
    "v_mfma_f32_16x16x32_bf16 v[16:19], a[32:35], a[72:75], v[16:19]\n"
    "s_waitcnt lgkmcnt(6)\n"
    "v_mfma_f32_16x16x32_bf16 v[32:35], a[32:35], a[80:83], v[32:35]\n"
    "s_lshl_b32 s60, s56, 7\n"
    "ds_read_b64_tr_b16 a[100:101], v238 offset:27648\n"
    "v_mfma_f32_16x16x32_bf16 v[32:35], a[48:51], a[84:87], v[32:35]\n"
    "buffer_load_dwordx4 v240, s[16:19], s59 offen lds\n"
    "v_mfma_f32_16x16x32_bf16 v[36:39], a[52:55], a[84:87], v[36:39]\n"
    "s_add_u32 m0, s64, 0\n"
    "ds_read_b64_tr_b16 a[102:103], v238 offset:32256\n"
    "v_mfma_f32_16x16x32_bf16 v[36:39], a[36:39], a[80:83], v[36:39]\n"
    "s_cmp_eq_u32 s57, 7\n"
    "v_mfma_f32_16x16x32_bf16 v[40:43], a[40:43], a[80:83], v[40:43]\n"
    "s_cselect_b32 s60, s50, s60\n"
    "ds_read_b64_tr_b16 a[104:105], v239 offset:18576\n"
    "v_mfma_f32_16x16x32_bf16 v[40:43], a[56:59], a[84:87], v[40:43]\n"
    "s_cselect_b64 s[8:9], s[48:49], s[8:9]\n"
    "v_mfma_f32_16x16x32_bf16 v[44:47], a[60:63], a[84:87], v[44:47]\n"
    "s_cselect_b32 s10, s51, s10\n"
    "ds_read_b64_tr_b16 a[106:107], v239 offset:23184\n"
    "v_mfma_f32_16x16x32_bf16 v[44:47], a[44:47], a[80:83], v[44:47]\n"
    "s_waitcnt lgkmcnt(6)\n"
    "v_mfma_f32_16x16x32_bf16 v[60:63], a[44:47], a[88:91], v[60:63]\n"
    "ds_read_b64_tr_b16 a[108:109], v239 offset:27792\n"
    "v_mfma_f32_16x16x32_bf16 v[60:63], a[60:63], a[92:95], v[60:63]\n"
    "buffer_load_dwordx4 v241, s[16:19], s59 offen lds\n"
    "v_mfma_f32_16x16x32_bf16 v[56:59], a[56:59], a[92:95], v[56:59]\n"
    "s_add_u32 m0, s63, 5120\n"
    "ds_read_b64_tr_b16 a[110:111], v239 offset:32400\n"
    "v_mfma_f32_16x16x32_bf16 v[56:59], a[40:43], a[88:91], v[56:59]\n"
    "s_add_u32 s59, s59, 110592\n"
    "ds_read_b64_tr_b16 a[112:113], v239 offset:18616\n"
    "v_mfma_f32_16x16x32_bf16 v[52:55], a[36:39], a[88:91], v[52:55]\n"
    "ds_read_b64_tr_b16 a[114:115], v239 offset:23224\n"
    "v_mfma_f32_16x16x32_bf16 v[52:55], a[52:55], a[92:95], v[52:55]\n"
    "v_mfma_f32_16x16x32_bf16 v[48:51], a[48:51], a[92:95], v[48:51]\n"
    "ds_read_b64_tr_b16 a[116:117], v239 offset:27832\n"
    "v_mfma_f32_16x16x32_bf16 v[48:51], a[32:35], a[88:91], v[48:51]\n"
    "s_waitcnt lgkmcnt(7)\n"
    "buffer_load_dwordx4 v240, s[16:19], s59 offen lds\n"
    "v_mfma_f32_16x16x32_bf16 v[64:67], a[32:35], a[96:99], v[64:67]\n"
    "s_add_u32 m0, s65, 8192\n"
    "ds_read_b64_tr_b16 a[118:119], v239 offset:32440\n"
    "v_mfma_f32_16x16x32_bf16 v[64:67], a[48:51], a[100:103], v[64:67]\n"
    "v_mfma_f32_16x16x32_bf16 v[68:71], a[52:55], a[100:103], v[68:71]\n"
    "ds_read_b64_tr_b16 a[120:121], v239 offset:18648\n"
    "v_mfma_f32_16x16x32_bf16 v[68:71], a[36:39], a[96:99], v[68:71]\n"
    "v_mfma_f32_16x16x32_bf16 v[72:75], a[40:43], a[96:99], v[72:75]\n"
    "ds_read_b64_tr_b16 a[122:123], v239 offset:23256\n"
    "v_mfma_f32_16x16x32_bf16 v[72:75], a[56:59], a[100:103], v[72:75]\n"
    "v_mfma_f32_16x16x32_bf16 v[76:79], a[60:63], a[100:103], v[76:79]\n"
    "ds_read_b64_tr_b16 a[124:125], v239 offset:27864\n"
    "v_mfma_f32_16x16x32_bf16 v[76:79], a[44:47], a[96:99], v[76:79]\n"
    "s_waitcnt lgkmcnt(7)\n"
    "buffer_load_dwordx4 v247, s[8:11], s60 offen offset:0 lds\n"
    "v_mfma_f32_16x16x32_bf16 v[92:95], a[44:47], a[104:107], v[92:95]\n"
    "s_mul_i32 s58, s56, 221184\n"
    "ds_read_b64_tr_b16 a[126:127], v239 offset:32472\n"
    "v_mfma_f32_16x16x32_bf16 v[92:95], a[60:63], a[108:111], v[92:95]\n"
    "s_mul_i32 s59, s56, 221184\n"
    "ds_read_b64_tr_b16 a[128:129], v239 offset:18688\n"
    "v_mfma_f32_16x16x32_bf16 v[88:91], a[56:59], a[108:111], v[88:91]\n"
    "s_add_u32 s57, s57, 1\n"
    "ds_read_b64_tr_b16 a[130:131], v239 offset:23296\n"
    "v_mfma_f32_16x16x32_bf16 v[88:91], a[40:43], a[104:107], v[88:91]\n"
    "v_mfma_f32_16x16x32_bf16 v[84:87], a[36:39], a[104:107], v[84:87]\n"
    "ds_read_b64_tr_b16 a[132:133], v239 offset:27904\n"
    "v_mfma_f32_16x16x32_bf16 v[84:87], a[52:55], a[108:111], v[84:87]\n"
    "buffer_load_dwordx4 v248, s[8:11], s60 offen offset:1024 lds\n"
    "v_mfma_f32_16x16x32_bf16 v[80:83], a[48:51], a[108:111], v[80:83]\n"
    "ds_read_b64_tr_b16 a[134:135], v239 offset:32512\n"
    "v_mfma_f32_16x16x32_bf16 v[80:83], a[32:35], a[104:107], v[80:83]\n"
    "s_waitcnt lgkmcnt(8)\n"
    "v_mfma_f32_16x16x32_bf16 v[96:99], a[32:35], a[112:115], v[96:99]\n"
    "ds_read_b64_tr_b16 a[136:137], v237 offset:10240\n"
    "v_mfma_f32_16x16x32_bf16 v[96:99], a[48:51], a[116:119], v[96:99]\n"
    "v_mfma_f32_16x16x32_bf16 v[100:103], a[52:55], a[116:119], v[100:103]\n"
    "ds_read_b64_tr_b16 a[138:139], v237 offset:12800\n"
    "v_mfma_f32_16x16x32_bf16 v[100:103], a[36:39], a[112:115], v[100:103]\n"
    "v_mfma_f32_16x16x32_bf16 v[104:107], a[40:43], a[112:115], v[104:107]\n"
    "ds_read_b64_tr_b16 a[140:141], v237 offset:15360\n"
    "v_mfma_f32_16x16x32_bf16 v[104:107], a[56:59], a[116:119], v[104:107]\n"
    "buffer_load_dwordx4 v249, s[8:11], s60 offen offset:2048 lds\n"
    "v_mfma_f32_16x16x32_bf16 v[108:111], a[60:63], a[116:119], v[108:111]\n"
    "ds_read_b64_tr_b16 a[142:143], v237 offset:17920\n"
    "v_mfma_f32_16x16x32_bf16 v[108:111], a[44:47], a[112:115], v[108:111]\n"
    "s_waitcnt lgkmcnt(8)\n"
    "v_mfma_f32_16x16x32_bf16 v[124:127], a[44:47], a[120:123], v[124:127]\n"
    "ds_read_b64_tr_b16 a[144:145], v237 offset:10280\n"
    "v_mfma_f32_16x16x32_bf16 v[124:127], a[60:63], a[124:127], v[124:127]\n"
    "v_mfma_f32_16x16x32_bf16 v[120:123], a[56:59], a[124:127], v[120:123]\n"
    "ds_read_b64_tr_b16 a[146:147], v237 offset:12840\n"
    "v_mfma_f32_16x16x32_bf16 v[120:123], a[40:43], a[120:123], v[120:123]\n"
    "buffer_load_dwordx4 v250, s[8:11], s60 offen offset:3072 lds\n"
    "v_mfma_f32_16x16x32_bf16 v[116:119], a[36:39], a[120:123], v[116:119]\n"
    "s_add_u32 m0, s65, 12288\n"
    "ds_read_b64_tr_b16 a[148:149], v237 offset:15400\n"
    "v_mfma_f32_16x16x32_bf16 v[116:119], a[52:55], a[124:127], v[116:119]\n"
    "s_add_u32 s60, s60, 36864\n"
    "v_mfma_f32_16x16x32_bf16 v[112:115], a[48:51], a[124:127], v[112:115]\n"
    "ds_read_b64_tr_b16 a[150:151], v237 offset:17960\n"
    "v_mfma_f32_16x16x32_bf16 v[112:115], a[32:35], a[120:123], v[112:115]\n"
    "s_waitcnt lgkmcnt(8)\n"
    "v_mfma_f32_16x16x32_bf16 v[128:131], a[32:35], a[128:131], v[128:131]\n"
    "ds_read_b64_tr_b16 a[152:153], v237 offset:10312\n"
    "v_mfma_f32_16x16x32_bf16 v[128:131], a[48:51], a[132:135], v[128:131]\n"
    "v_mfma_f32_16x16x32_bf16 v[132:135], a[52:55], a[132:135], v[132:135]\n"
    "ds_read_b64_tr_b16 a[154:155], v237 offset:12872\n"
    "v_mfma_f32_16x16x32_bf16 v[132:135], a[36:39], a[128:131], v[132:135]\n"
    "buffer_load_dwordx4 v247, s[8:11], s60 offen offset:0 lds\n"
    "v_mfma_f32_16x16x32_bf16 v[136:139], a[40:43], a[128:131], v[136:139]\n"
    "ds_read_b64_tr_b16 a[156:157], v237 offset:15432\n"
    "v_mfma_f32_16x16x32_bf16 v[136:139], a[56:59], a[132:135], v[136:139]\n"
    "v_mfma_f32_16x16x32_bf16 v[140:143], a[60:63], a[132:135], v[140:143]\n"
    "ds_read_b64_tr_b16 a[158:159], v237 offset:17992\n"
    "v_mfma_f32_16x16x32_bf16 v[140:143], a[44:47], a[128:131], v[140:143]\n"
    "s_waitcnt lgkmcnt(8)\n"
    "v_mfma_f32_16x16x32_bf16 v[156:159], a[136:139], a[44:47], v[156:159]\n"
    "ds_read_b64_tr_b16 a[160:161], v237 offset:10352\n"
    "v_mfma_f32_16x16x32_bf16 v[156:159], a[140:143], a[60:63], v[156:159]\n"
    "buffer_load_dwordx4 v248, s[8:11], s60 offen offset:1024 lds\n"
    "v_mfma_f32_16x16x32_bf16 v[152:155], a[140:143], a[56:59], v[152:155]\n"
    "ds_read_b64_tr_b16 a[162:163], v237 offset:12912\n"
    "v_mfma_f32_16x16x32_bf16 v[152:155], a[136:139], a[40:43], v[152:155]\n"
    "v_mfma_f32_16x16x32_bf16 v[148:151], a[136:139], a[36:39], v[148:151]\n"
    "ds_read_b64_tr_b16 a[164:165], v237 offset:15472\n"
    "v_mfma_f32_16x16x32_bf16 v[148:151], a[140:143], a[52:55], v[148:151]\n"
    "v_mfma_f32_16x16x32_bf16 v[144:147], a[140:143], a[48:51], v[144:147]\n"
    "ds_read_b64_tr_b16 a[166:167], v237 offset:18032\n"
    "v_mfma_f32_16x16x32_bf16 v[144:147], a[136:139], a[32:35], v[144:147]\n"
    "s_waitcnt lgkmcnt(8)\n"
    "v_mfma_f32_16x16x32_bf16 v[160:163], a[144:147], a[32:35], v[160:163]\n"
    "s_waitcnt vmcnt(14)\n"
    "ds_read_b64_tr_b16 a[168:169], v236 offset:10240\n"
    "v_mfma_f32_16x16x32_bf16 v[160:163], a[148:151], a[48:51], v[160:163]\n"
    "buffer_load_dwordx4 v249, s[8:11], s60 offen offset:2048 lds\n"
    "v_mfma_f32_16x16x32_bf16 v[164:167], a[148:151], a[52:55], v[164:167]\n"
    "ds_read_b64_tr_b16 a[170:171], v236 offset:12800\n"
    "v_mfma_f32_16x16x32_bf16 v[164:167], a[144:147], a[36:39], v[164:167]\n"
    "ds_read_b128 a[0:3], v245 offset:0\n"
    "v_mfma_f32_16x16x32_bf16 v[168:171], a[144:147], a[40:43], v[168:171]\n"
    "ds_read_b64_tr_b16 a[172:173], v236 offset:15360\n"
    "v_mfma_f32_16x16x32_bf16 v[168:171], a[148:151], a[56:59], v[168:171]\n"
    "ds_read_b128 a[16:19], v246 offset:0\n"
    "v_mfma_f32_16x16x32_bf16 v[172:175], a[148:151], a[60:63], v[172:175]\n"
    "ds_read_b64_tr_b16 a[174:175], v236 offset:17920\n"
    "v_mfma_f32_16x16x32_bf16 v[172:175], a[144:147], a[44:47], v[172:175]\n"
    "s_waitcnt lgkmcnt(10)\n"
    "buffer_load_dwordx4 v250, s[8:11], s60 offen offset:3072 lds\n"
    "v_mfma_f32_16x16x32_bf16 v[188:191], a[152:155], a[44:47], v[188:191]\n"
    "s_add_u32 m0, s61, 18432\n"
    "ds_read_b128 a[4:7], v245 offset:2048\n"
    "v_mfma_f32_16x16x32_bf16 v[188:191], a[156:159], a[60:63], v[188:191]\n"
    "ds_read_b128 a[20:23], v246 offset:2048\n"
    "v_mfma_f32_16x16x32_bf16 v[184:187], a[156:159], a[56:59], v[184:187]\n"
    "ds_read_b128 a[8:11], v245 offset:4096\n"
    "v_mfma_f32_16x16x32_bf16 v[184:187], a[152:155], a[40:43], v[184:187]\n"
    "ds_read_b128 a[24:27], v246 offset:4096\n"
    "v_mfma_f32_16x16x32_bf16 v[180:183], a[152:155], a[36:39], v[180:183]\n"
    "ds_read_b128 a[12:15], v245 offset:6144\n"
    "v_mfma_f32_16x16x32_bf16 v[180:183], a[156:159], a[52:55], v[180:183]\n"
    "s_waitcnt vmcnt(8)\n"
    "ds_read_b128 a[28:31], v246 offset:6144\n"
    "v_mfma_f32_16x16x32_bf16 v[176:179], a[156:159], a[48:51], v[176:179]\n"
    "s_waitcnt lgkmcnt(6)\n"
    "v_mfma_f32_16x16x32_bf16 v[176:179], a[152:155], a[32:35], v[176:179]\n"
    "v_mfma_f32_16x16x32_bf16 v[192:195], a[160:163], a[32:35], v[192:195]\n"
    "s_barrier\n"
    "ds_read_b64_tr_b16 a[64:65], v239 offset:0\n"
    "v_mfma_f32_16x16x32_bf16 v[192:195], a[164:167], a[48:51], v[192:195]\n"
    "buffer_load_dwordx4 v242, s[16:19], s58 offen offset:0 lds\n"
    "v_mfma_f32_16x16x32_bf16 v[196:199], a[164:167], a[52:55], v[196:199]\n"
    "ds_read_b64_tr_b16 a[66:67], v239 offset:4608\n"
    "v_mfma_f32_16x16x32_bf16 v[196:199], a[160:163], a[36:39], v[196:199]\n"
    "v_mfma_f32_16x16x32_bf16 v[200:203], a[160:163], a[40:43], v[200:203]\n"
    "ds_read_b64_tr_b16 a[68:69], v239 offset:9216\n"
    "v_mfma_f32_16x16x32_bf16 v[200:203], a[164:167], a[56:59], v[200:203]\n"
    "v_mfma_f32_16x16x32_bf16 v[204:207], a[164:167], a[60:63], v[204:207]\n"
    "ds_read_b64_tr_b16 a[70:71], v239 offset:13824\n"
    "v_mfma_f32_16x16x32_bf16 v[204:207], a[160:163], a[44:47], v[204:207]\n"
    "buffer_load_dwordx4 v243, s[16:19], s58 offen offset:1024 lds\n"
    "v_mfma_f32_16x16x32_bf16 v[220:223], a[168:171], a[44:47], v[220:223]\n"
    "s_add_u32 m0, s62, 18432\n"
    "ds_read_b64_tr_b16 a[72:73], v239 offset:40\n"
    "v_mfma_f32_16x16x32_bf16 v[220:223], a[172:175], a[60:63], v[220:223]\n"
    "v_mfma_f32_16x16x32_bf16 v[216:219], a[172:175], a[56:59], v[216:219]\n"
    "ds_read_b64_tr_b16 a[74:75], v239 offset:4648\n"
    "v_mfma_f32_16x16x32_bf16 v[216:219], a[168:171], a[40:43], v[216:219]\n"
    "v_mfma_f32_16x16x32_bf16 v[212:215], a[168:171], a[36:39], v[212:215]\n"
    "ds_read_b64_tr_b16 a[76:77], v239 offset:9256\n"
    "v_mfma_f32_16x16x32_bf16 v[212:215], a[172:175], a[52:55], v[212:215]\n"
    "buffer_load_dwordx4 v244, s[16:19], s58 offen offset:0 lds\n"
    "v_mfma_f32_16x16x32_bf16 v[208:211], a[172:175], a[48:51], v[208:211]\n"
    "s_waitcnt lgkmcnt(3)\n"
    "ds_read_b64_tr_b16 a[78:79], v239 offset:13864\n"
    "v_mfma_f32_16x16x32_bf16 v[208:211], a[168:171], a[32:35], v[208:211]\n"
    "v_mfma_f32_16x16x32_bf16 v[0:3], a[0:3], a[64:67], v[0:3]\n"
    "s_add_u32 m0, s61, 27648\n"
    "ds_read_b64_tr_b16 a[80:81], v239 offset:72\n"
    "v_mfma_f32_16x16x32_bf16 v[0:3], a[16:19], a[68:71], v[0:3]\n"
    "s_add_u32 s58, s58, 110592\n"
    "ds_read_b64_tr_b16 a[82:83], v239 offset:4680\n"
    "v_mfma_f32_16x16x32_bf16 v[4:7], a[20:23], a[68:71], v[4:7]\n"
    "ds_read_b64_tr_b16 a[84:85], v239 offset:9288\n"
    "v_mfma_f32_16x16x32_bf16 v[4:7], a[4:7], a[64:67], v[4:7]\n"
    "buffer_load_dwordx4 v242, s[16:19], s58 offen offset:0 lds\n"
    "v_mfma_f32_16x16x32_bf16 v[8:11], a[8:11], a[64:67], v[8:11]\n"
    "ds_read_b64_tr_b16 a[86:87], v239 offset:13896\n"
    "v_mfma_f32_16x16x32_bf16 v[8:11], a[24:27], a[68:71], v[8:11]\n"
    "v_mfma_f32_16x16x32_bf16 v[12:15], a[28:31], a[68:71], v[12:15]\n"
    "ds_read_b64_tr_b16 a[88:89], v239 offset:112\n"
    "v_mfma_f32_16x16x32_bf16 v[12:15], a[12:15], a[64:67], v[12:15]\n"
    "s_waitcnt lgkmcnt(5)\n"
    "v_mfma_f32_16x16x32_bf16 v[28:31], a[12:15], a[72:75], v[28:31]\n"
    "s_add_u32 s56, s56, 1\n"
    "ds_read_b64_tr_b16 a[90:91], v239 offset:4720\n"
    "v_mfma_f32_16x16x32_bf16 v[28:31], a[28:31], a[76:79], v[28:31]\n"
    "s_cmp_eq_u32 s56, 9\n"
    "v_mfma_f32_16x16x32_bf16 v[24:27], a[24:27], a[76:79], v[24:27]\n"
    "s_cselect_b32 s56, 0, s56\n"
    "ds_read_b64_tr_b16 a[92:93], v239 offset:9328\n"
    "v_mfma_f32_16x16x32_bf16 v[24:27], a[8:11], a[72:75], v[24:27]\n"
    "buffer_load_dwordx4 v243, s[16:19], s58 offen offset:1024 lds\n"
    "v_mfma_f32_16x16x32_bf16 v[20:23], a[4:7], a[72:75], v[20:23]\n"
    "s_add_u32 m0, s63, 10240\n"
    "ds_read_b64_tr_b16 a[94:95], v239 offset:13936\n"
    "v_mfma_f32_16x16x32_bf16 v[20:23], a[20:23], a[76:79], v[20:23]\n"
    "s_cmp_ge_u32 s57, 7\n"
    "ds_read_b64_tr_b16 a[96:97], v238 offset:0\n"
    "v_mfma_f32_16x16x32_bf16 v[16:19], a[16:19], a[76:79], v[16:19]\n"
    "s_cselect_b32 s56, 7000, s56\n"
    "ds_read_b64_tr_b16 a[98:99], v238 offset:4608\n"
    "v_mfma_f32_16x16x32_bf16 v[16:19], a[0:3], a[72:75], v[16:19]\n"
    "s_waitcnt lgkmcnt(6)\n"
    "v_mfma_f32_16x16x32_bf16 v[32:35], a[0:3], a[80:83], v[32:35]\n"
    "s_lshl_b32 s60, s56, 7\n"
    "ds_read_b64_tr_b16 a[100:101], v238 offset:9216\n"
    "v_mfma_f32_16x16x32_bf16 v[32:35], a[16:19], a[84:87], v[32:35]\n"
    "buffer_load_dwordx4 v240, s[16:19], s59 offen lds\n"
    "v_mfma_f32_16x16x32_bf16 v[36:39], a[20:23], a[84:87], v[36:39]\n"
    "s_add_u32 m0, s64, 10240\n"
    "ds_read_b64_tr_b16 a[102:103], v238 offset:13824\n"
    "v_mfma_f32_16x16x32_bf16 v[36:39], a[4:7], a[80:83], v[36:39]\n"
    "s_cmp_eq_u32 s57, 8\n"
    "v_mfma_f32_16x16x32_bf16 v[40:43], a[8:11], a[80:83], v[40:43]\n"
    "s_cselect_b32 s10, 0, s10\n"
    "ds_read_b64_tr_b16 a[104:105], v239 offset:144\n"
    "v_mfma_f32_16x16x32_bf16 v[40:43], a[24:27], a[84:87], v[40:43]\n"
    "v_mfma_f32_16x16x32_bf16 v[44:47], a[28:31], a[84:87], v[44:47]\n"
    "ds_read_b64_tr_b16 a[106:107], v239 offset:4752\n"
    "v_mfma_f32_16x16x32_bf16 v[44:47], a[12:15], a[80:83], v[44:47]\n"
    "s_waitcnt lgkmcnt(6)\n"
    "v_mfma_f32_16x16x32_bf16 v[60:63], a[12:15], a[88:91], v[60:63]\n"
    "ds_read_b64_tr_b16 a[108:109], v239 offset:9360\n"
    "v_mfma_f32_16x16x32_bf16 v[60:63], a[28:31], a[92:95], v[60:63]\n"
    "buffer_load_dwordx4 v241, s[16:19], s59 offen lds\n"
    "v_mfma_f32_16x16x32_bf16 v[56:59], a[24:27], a[92:95], v[56:59]\n"
    "s_add_u32 m0, s63, 15360\n"
    "ds_read_b64_tr_b16 a[110:111], v239 offset:13968\n"
    "v_mfma_f32_16x16x32_bf16 v[56:59], a[8:11], a[88:91], v[56:59]\n"
    "s_add_u32 s59, s59, 110592\n"
    "ds_read_b64_tr_b16 a[112:113], v239 offset:184\n"
    "v_mfma_f32_16x16x32_bf16 v[52:55], a[4:7], a[88:91], v[52:55]\n"
    "ds_read_b64_tr_b16 a[114:115], v239 offset:4792\n"
    "v_mfma_f32_16x16x32_bf16 v[52:55], a[20:23], a[92:95], v[52:55]\n"
    "v_mfma_f32_16x16x32_bf16 v[48:51], a[16:19], a[92:95], v[48:51]\n"
    "ds_read_b64_tr_b16 a[116:117], v239 offset:9400\n"
    "v_mfma_f32_16x16x32_bf16 v[48:51], a[0:3], a[88:91], v[48:51]\n"
    "s_waitcnt lgkmcnt(7)\n"
    "buffer_load_dwordx4 v240, s[16:19], s59 offen lds\n"
    "v_mfma_f32_16x16x32_bf16 v[64:67], a[0:3], a[96:99], v[64:67]\n"
    "s_add_u32 m0, s65, 0\n"
    "ds_read_b64_tr_b16 a[118:119], v239 offset:14008\n"
    "v_mfma_f32_16x16x32_bf16 v[64:67], a[16:19], a[100:103], v[64:67]\n"
    "v_mfma_f32_16x16x32_bf16 v[68:71], a[20:23], a[100:103], v[68:71]\n"
    "ds_read_b64_tr_b16 a[120:121], v239 offset:216\n"
    "v_mfma_f32_16x16x32_bf16 v[68:71], a[4:7], a[96:99], v[68:71]\n"
    "v_mfma_f32_16x16x32_bf16 v[72:75], a[8:11], a[96:99], v[72:75]\n"
    "ds_read_b64_tr_b16 a[122:123], v239 offset:4824\n"
    "v_mfma_f32_16x16x32_bf16 v[72:75], a[24:27], a[100:103], v[72:75]\n"
    "v_mfma_f32_16x16x32_bf16 v[76:79], a[28:31], a[100:103], v[76:79]\n"
    "ds_read_b64_tr_b16 a[124:125], v239 offset:9432\n"
    "v_mfma_f32_16x16x32_bf16 v[76:79], a[12:15], a[96:99], v[76:79]\n"
    "s_waitcnt lgkmcnt(7)\n"
    "buffer_load_dwordx4 v247, s[8:11], s60 offen offset:0 lds\n"
    "v_mfma_f32_16x16x32_bf16 v[92:95], a[12:15], a[104:107], v[92:95]\n"
    "s_mul_i32 s58, s56, 221184\n"
    "ds_read_b64_tr_b16 a[126:127], v239 offset:14040\n"
    "v_mfma_f32_16x16x32_bf16 v[92:95], a[28:31], a[108:111], v[92:95]\n"
    "s_mul_i32 s59, s56, 221184\n"
    "ds_read_b64_tr_b16 a[128:129], v239 offset:256\n"
    "v_mfma_f32_16x16x32_bf16 v[88:91], a[24:27], a[108:111], v[88:91]\n"
    "s_add_u32 s57, s57, 1\n"
    "ds_read_b64_tr_b16 a[130:131], v239 offset:4864\n"
    "v_mfma_f32_16x16x32_bf16 v[88:91], a[8:11], a[104:107], v[88:91]\n"
    "v_mfma_f32_16x16x32_bf16 v[84:87], a[4:7], a[104:107], v[84:87]\n"
    "ds_read_b64_tr_b16 a[132:133], v239 offset:9472\n"
    "v_mfma_f32_16x16x32_bf16 v[84:87], a[20:23], a[108:111], v[84:87]\n"
    "buffer_load_dwordx4 v248, s[8:11], s60 offen offset:1024 lds\n"
    "v_mfma_f32_16x16x32_bf16 v[80:83], a[16:19], a[108:111], v[80:83]\n"
    "ds_read_b64_tr_b16 a[134:135], v239 offset:14080\n"
    "v_mfma_f32_16x16x32_bf16 v[80:83], a[0:3], a[104:107], v[80:83]\n"
    "s_waitcnt lgkmcnt(8)\n"
    "v_mfma_f32_16x16x32_bf16 v[96:99], a[0:3], a[112:115], v[96:99]\n"
    "ds_read_b64_tr_b16 a[136:137], v237 offset:0\n"
    "v_mfma_f32_16x16x32_bf16 v[96:99], a[16:19], a[116:119], v[96:99]\n"
    "v_mfma_f32_16x16x32_bf16 v[100:103], a[20:23], a[116:119], v[100:103]\n"
    "ds_read_b64_tr_b16 a[138:139], v237 offset:2560\n"
    "v_mfma_f32_16x16x32_bf16 v[100:103], a[4:7], a[112:115], v[100:103]\n"
    "v_mfma_f32_16x16x32_bf16 v[104:107], a[8:11], a[112:115], v[104:107]\n"
    "ds_read_b64_tr_b16 a[140:141], v237 offset:5120\n"
    "v_mfma_f32_16x16x32_bf16 v[104:107], a[24:27], a[116:119], v[104:107]\n"
    "buffer_load_dwordx4 v249, s[8:11], s60 offen offset:2048 lds\n"
    "v_mfma_f32_16x16x32_bf16 v[108:111], a[28:31], a[116:119], v[108:111]\n"
    "ds_read_b64_tr_b16 a[142:143], v237 offset:7680\n"
    "v_mfma_f32_16x16x32_bf16 v[108:111], a[12:15], a[112:115], v[108:111]\n"
    "s_waitcnt lgkmcnt(8)\n"
    "v_mfma_f32_16x16x32_bf16 v[124:127], a[12:15], a[120:123], v[124:127]\n"
    "ds_read_b64_tr_b16 a[144:145], v237 offset:40\n"
    "v_mfma_f32_16x16x32_bf16 v[124:127], a[28:31], a[124:127], v[124:127]\n"
    "v_mfma_f32_16x16x32_bf16 v[120:123], a[24:27], a[124:127], v[120:123]\n"
    "ds_read_b64_tr_b16 a[146:147], v237 offset:2600\n"
    "v_mfma_f32_16x16x32_bf16 v[120:123], a[8:11], a[120:123], v[120:123]\n"
    "buffer_load_dwordx4 v250, s[8:11], s60 offen offset:3072 lds\n"
    "v_mfma_f32_16x16x32_bf16 v[116:119], a[4:7], a[120:123], v[116:119]\n"
    "s_add_u32 m0, s65, 4096\n"
    "ds_read_b64_tr_b16 a[148:149], v237 offset:5160\n"
    "v_mfma_f32_16x16x32_bf16 v[116:119], a[20:23], a[124:127], v[116:119]\n"
    "s_add_u32 s60, s60, 36864\n"
    "v_mfma_f32_16x16x32_bf16 v[112:115], a[16:19], a[124:127], v[112:115]\n"
    "ds_read_b64_tr_b16 a[150:151], v237 offset:7720\n"
    "v_mfma_f32_16x16x32_bf16 v[112:115], a[0:3], a[120:123], v[112:115]\n"
    "s_waitcnt lgkmcnt(8)\n"
    "v_mfma_f32_16x16x32_bf16 v[128:131], a[0:3], a[128:131], v[128:131]\n"
    "ds_read_b64_tr_b16 a[152:153], v237 offset:72\n"
    "v_mfma_f32_16x16x32_bf16 v[128:131], a[16:19], a[132:135], v[128:131]\n"
    "v_mfma_f32_16x16x32_bf16 v[132:135], a[20:23], a[132:135], v[132:135]\n"
    "ds_read_b64_tr_b16 a[154:155], v237 offset:2632\n"
    "v_mfma_f32_16x16x32_bf16 v[132:135], a[4:7], a[128:131], v[132:135]\n"
    "buffer_load_dwordx4 v247, s[8:11], s60 offen offset:0 lds\n"
    "v_mfma_f32_16x16x32_bf16 v[136:139], a[8:11], a[128:131], v[136:139]\n"
    "ds_read_b64_tr_b16 a[156:157], v237 offset:5192\n"
    "v_mfma_f32_16x16x32_bf16 v[136:139], a[24:27], a[132:135], v[136:139]\n"
    "v_mfma_f32_16x16x32_bf16 v[140:143], a[28:31], a[132:135], v[140:143]\n"
    "ds_read_b64_tr_b16 a[158:159], v237 offset:7752\n"
    "v_mfma_f32_16x16x32_bf16 v[140:143], a[12:15], a[128:131], v[140:143]\n"
    "s_waitcnt lgkmcnt(8)\n"
    "v_mfma_f32_16x16x32_bf16 v[156:159], a[136:139], a[12:15], v[156:159]\n"
    "ds_read_b64_tr_b16 a[160:161], v237 offset:112\n"
    "v_mfma_f32_16x16x32_bf16 v[156:159], a[140:143], a[28:31], v[156:159]\n"
    "buffer_load_dwordx4 v248, s[8:11], s60 offen offset:1024 lds\n"
    "v_mfma_f32_16x16x32_bf16 v[152:155], a[140:143], a[24:27], v[152:155]\n"
    "ds_read_b64_tr_b16 a[162:163], v237 offset:2672\n"
    "v_mfma_f32_16x16x32_bf16 v[152:155], a[136:139], a[8:11], v[152:155]\n"
    "v_mfma_f32_16x16x32_bf16 v[148:151], a[136:139], a[4:7], v[148:151]\n"
    "ds_read_b64_tr_b16 a[164:165], v237 offset:5232\n"
    "v_mfma_f32_16x16x32_bf16 v[148:151], a[140:143], a[20:23], v[148:151]\n"
    "v_mfma_f32_16x16x32_bf16 v[144:147], a[140:143], a[16:19], v[144:147]\n"
    "ds_read_b64_tr_b16 a[166:167], v237 offset:7792\n"
    "v_mfma_f32_16x16x32_bf16 v[144:147], a[136:139], a[0:3], v[144:147]\n"
    "s_waitcnt lgkmcnt(8)\n"
    "v_mfma_f32_16x16x32_bf16 v[160:163], a[144:147], a[0:3], v[160:163]\n"
    "s_waitcnt vmcnt(14)\n"
    "ds_read_b64_tr_b16 a[168:169], v236 offset:0\n"
    "v_mfma_f32_16x16x32_bf16 v[160:163], a[148:151], a[16:19], v[160:163]\n"
    "buffer_load_dwordx4 v249, s[8:11], s60 offen offset:2048 lds\n"
    "v_mfma_f32_16x16x32_bf16 v[164:167], a[148:151], a[20:23], v[164:167]\n"
    "ds_read_b64_tr_b16 a[170:171], v236 offset:2560\n"
    "v_mfma_f32_16x16x32_bf16 v[164:167], a[144:147], a[4:7], v[164:167]\n"
    "ds_read_b128 a[32:35], v245 offset:8192\n"
    "v_mfma_f32_16x16x32_bf16 v[168:171], a[144:147], a[8:11], v[168:171]\n"
    "ds_read_b64_tr_b16 a[172:173], v236 offset:5120\n"
    "v_mfma_f32_16x16x32_bf16 v[168:171], a[148:151], a[24:27], v[168:171]\n"
    "ds_read_b128 a[48:51], v246 offset:8192\n"
    "v_mfma_f32_16x16x32_bf16 v[172:175], a[148:151], a[28:31], v[172:175]\n"
    "ds_read_b64_tr_b16 a[174:175], v236 offset:7680\n"
    "v_mfma_f32_16x16x32_bf16 v[172:175], a[144:147], a[12:15], v[172:175]\n"
    "s_waitcnt lgkmcnt(10)\n"
    "buffer_load_dwordx4 v250, s[8:11], s60 offen offset:3072 lds\n"
    "v_mfma_f32_16x16x32_bf16 v[188:191], a[152:155], a[12:15], v[188:191]\n"
    "s_add_u32 m0, s61, 0\n"
    "ds_read_b128 a[36:39], v245 offset:10240\n"
    "v_mfma_f32_16x16x32_bf16 v[188:191], a[156:159], a[28:31], v[188:191]\n"
    "ds_read_b128 a[52:55], v246 offset:10240\n"
    "v_mfma_f32_16x16x32_bf16 v[184:187], a[156:159], a[24:27], v[184:187]\n"
    "ds_read_b128 a[40:43], v245 offset:12288\n"
    "v_mfma_f32_16x16x32_bf16 v[184:187], a[152:155], a[8:11], v[184:187]\n"
    "ds_read_b128 a[56:59], v246 offset:12288\n"
    "v_mfma_f32_16x16x32_bf16 v[180:183], a[152:155], a[4:7], v[180:183]\n"
    "ds_read_b128 a[44:47], v245 offset:14336\n"
    "v_mfma_f32_16x16x32_bf16 v[180:183], a[156:159], a[20:23], v[180:183]\n"
    "s_waitcnt vmcnt(8)\n"
    "ds_read_b128 a[60:63], v246 offset:14336\n"
    "v_mfma_f32_16x16x32_bf16 v[176:179], a[156:159], a[16:19], v[176:179]\n"
    "s_waitcnt lgkmcnt(6)\n"
    "v_mfma_f32_16x16x32_bf16 v[176:179], a[152:155], a[0:3], v[176:179]\n"
    "v_mfma_f32_16x16x32_bf16 v[192:195], a[160:163], a[0:3], v[192:195]\n"
    "s_barrier\n"
    "ds_read_b64_tr_b16 a[64:65], v239 offset:18432\n"
    "v_mfma_f32_16x16x32_bf16 v[192:195], a[164:167], a[16:19], v[192:195]\n"
    "buffer_load_dwordx4 v242, s[16:19], s58 offen offset:0 lds\n"
    "v_mfma_f32_16x16x32_bf16 v[196:199], a[164:167], a[20:23], v[196:199]\n"
    "ds_read_b64_tr_b16 a[66:67], v239 offset:23040\n"
    "v_mfma_f32_16x16x32_bf16 v[196:199], a[160:163], a[4:7], v[196:199]\n"
    "v_mfma_f32_16x16x32_bf16 v[200:203], a[160:163], a[8:11], v[200:203]\n"
    "ds_read_b64_tr_b16 a[68:69], v239 offset:27648\n"
    "v_mfma_f32_16x16x32_bf16 v[200:203], a[164:167], a[24:27], v[200:203]\n"
    "v_mfma_f32_16x16x32_bf16 v[204:207], a[164:167], a[28:31], v[204:207]\n"
    "ds_read_b64_tr_b16 a[70:71], v239 offset:32256\n"
    "v_mfma_f32_16x16x32_bf16 v[204:207], a[160:163], a[12:15], v[204:207]\n"
    "buffer_load_dwordx4 v243, s[16:19], s58 offen offset:1024 lds\n"
    "v_mfma_f32_16x16x32_bf16 v[220:223], a[168:171], a[12:15], v[220:223]\n"
    "s_add_u32 m0, s62, 0\n"
    "ds_read_b64_tr_b16 a[72:73], v239 offset:18472\n"
    "v_mfma_f32_16x16x32_bf16 v[220:223], a[172:175], a[28:31], v[220:223]\n"
    "v_mfma_f32_16x16x32_bf16 v[216:219], a[172:175], a[24:27], v[216:219]\n"
    "ds_read_b64_tr_b16 a[74:75], v239 offset:23080\n"
    "v_mfma_f32_16x16x32_bf16 v[216:219], a[168:171], a[8:11], v[216:219]\n"
    "v_mfma_f32_16x16x32_bf16 v[212:215], a[168:171], a[4:7], v[212:215]\n"
    "ds_read_b64_tr_b16 a[76:77], v239 offset:27688\n"
    "v_mfma_f32_16x16x32_bf16 v[212:215], a[172:175], a[20:23], v[212:215]\n"
    "s_cmp_lt_u32 s57, 9\n"
    "buffer_load_dwordx4 v244, s[16:19], s58 offen offset:0 lds\n"
    "v_mfma_f32_16x16x32_bf16 v[208:211], a[172:175], a[16:19], v[208:211]\n"
    "s_waitcnt lgkmcnt(3)\n"
    "ds_read_b64_tr_b16 a[78:79], v239 offset:32296\n"
    "v_mfma_f32_16x16x32_bf16 v[208:211], a[168:171], a[0:3], v[208:211]\n"
    "s_cbranch_scc1 LP1B_%=\n"
    "v_lshlrev_b32 v227, 9, v255\n"
    "buffer_load_dwordx4 v[228:231], v227, s[20:23], s37 offen sc1\n"
    "s_mov_b32 s8, 0x3fb8aa3b\n"
    "s_mov_b32 s9, 0xb5c1a2a0\n"
    "s_mov_b32 s81, 0x358637bd\n"
    "s_mov_b32 s10, 0xff00ff00\n"
    "s_mov_b32 s11, 0xff00ff00\n"
    "v_mov_b32 v224, 0x3f803f80\n"
    "v_cvt_pk_bf16_f32 v80, v80, v81\n"
    "v_cvt_pk_bf16_f32 v81, v82, v83\n"
    "v_cvt_pk_bf16_f32 v82, v84, v85\n"
    "v_cvt_pk_bf16_f32 v83, v86, v87\n"
    "v_cvt_pk_bf16_f32 v88, v88, v89\n"
    "v_cvt_pk_bf16_f32 v89, v90, v91\n"
    "v_cvt_pk_bf16_f32 v90, v92, v93\n"
    "v_cvt_pk_bf16_f32 v91, v94, v95\n"
    "v_cvt_pk_bf16_f32 v96, v96, v97\n"
    "v_cvt_pk_bf16_f32 v97, v98, v99\n"
    "v_cvt_pk_bf16_f32 v98, v100, v101\n"
    "v_cvt_pk_bf16_f32 v99, v102, v103\n"
    "v_cvt_pk_bf16_f32 v104, v104, v105\n"
    "v_cvt_pk_bf16_f32 v105, v106, v107\n"
    "v_cvt_pk_bf16_f32 v106, v108, v109\n"
    "v_cvt_pk_bf16_f32 v107, v110, v111\n"
    "v_cvt_pk_bf16_f32 v112, v112, v113\n"
    "v_cvt_pk_bf16_f32 v113, v114, v115\n"
    "v_cvt_pk_bf16_f32 v114, v116, v117\n"
    "v_cvt_pk_bf16_f32 v115, v118, v119\n"
    "v_cvt_pk_bf16_f32 v120, v120, v121\n"
    "v_cvt_pk_bf16_f32 v121, v122, v123\n"
    "v_cvt_pk_bf16_f32 v122, v124, v125\n"
    "v_cvt_pk_bf16_f32 v123, v126, v127\n"
    "v_cvt_pk_bf16_f32 v128, v128, v129\n"
    "v_cvt_pk_bf16_f32 v129, v130, v131\n"
    "v_cvt_pk_bf16_f32 v130, v132, v133\n"
    "v_cvt_pk_bf16_f32 v131, v134, v135\n"
    "v_cvt_pk_bf16_f32 v136, v136, v137\n"
    "v_cvt_pk_bf16_f32 v137, v138, v139\n"
    "v_cvt_pk_bf16_f32 v138, v140, v141\n"
    "v_cvt_pk_bf16_f32 v139, v142, v143\n"
    "v_cvt_pk_bf16_f32 v84, v64, v65\n"
    "v_cvt_pk_bf16_f32 v85, v66, v67\n"
    "v_cvt_pk_bf16_f32 v86, v68, v69\n"
    "v_cvt_pk_bf16_f32 v87, v70, v71\n"
    "v_cvt_pk_bf16_f32 v92, v72, v73\n"
    "v_cvt_pk_bf16_f32 v93, v74, v75\n"
    "v_cvt_pk_bf16_f32 v94, v76, v77\n"
    "v_cvt_pk_bf16_f32 v95, v78, v79\n"
    "v_cndmask_b32 v84, v224, v84, s[10:11]\n"
    "v_cndmask_b32 v85, v224, v85, s[10:11]\n"
    "v_cndmask_b32 v86, v224, v86, s[10:11]\n"
    "v_cndmask_b32 v87, v224, v87, s[10:11]\n"
    "v_cndmask_b32 v92, v224, v92, s[10:11]\n"
    "v_cndmask_b32 v93, v224, v93, s[10:11]\n"
    "v_cndmask_b32 v94, v224, v94, s[10:11]\n"
    "v_cndmask_b32 v95, v224, v95, s[10:11]\n"
    "v_pk_fma_f32 v[236:237], v[64:65], s[8:9], s[8:9] op_sel:[0,0,1] op_sel_hi:[1,0,1]\n"
    "v_pk_fma_f32 v[238:239], v[66:67], s[8:9], s[8:9] op_sel:[0,0,1] op_sel_hi:[1,0,1]\n"
    "v_pk_fma_f32 v[240:241], v[68:69], s[8:9], s[8:9] op_sel:[0,0,1] op_sel_hi:[1,0,1]\n"
    "v_pk_fma_f32 v[242:243], v[70:71], s[8:9], s[8:9] op_sel:[0,0,1] op_sel_hi:[1,0,1]\n"
    "v_max_f32 v64, s81, v64\n"
    "v_max_f32 v66, s81, v66\n"
    "v_max_f32 v68, s81, v68\n"
    "v_max_f32 v70, s81, v70\n"
    "v_max_f32 v65, s81, v65\n"
    "v_max_f32 v67, s81, v67\n"
    "v_max_f32 v69, s81, v69\n"
    "v_max_f32 v71, s81, v71\n"
    "v_exp_f32_e64 v236, v236 clamp\n"
    "v_exp_f32_e64 v238, v238 clamp\n"
    "v_exp_f32_e64 v240, v240 clamp\n"
    "v_exp_f32_e64 v242, v242 clamp\n"
    "v_exp_f32_e64 v237, v237 clamp\n"
    "v_exp_f32_e64 v239, v239 clamp\n"
    "v_exp_f32_e64 v241, v241 clamp\n"
    "v_exp_f32_e64 v243, v243 clamp\n"
    "v_pk_add_f32 v[64:65], v[64:65], v[236:237]\n"
    "v_pk_add_f32 v[66:67], v[66:67], v[238:239]\n"
    "v_pk_add_f32 v[68:69], v[68:69], v[240:241]\n"
    "v_pk_add_f32 v[70:71], v[70:71], v[242:243]\n"
    "v_cvt_pk_bf16_f32 v64, v64, v65\n"
    "v_cvt_pk_bf16_f32 v65, v66, v67\n"
    "v_cvt_pk_bf16_f32 v66, v68, v69\n"
    "v_cvt_pk_bf16_f32 v67, v70, v71\n"
    "v_pk_fma_f32 v[236:237], v[72:73], s[8:9], s[8:9] op_sel:[0,0,1] op_sel_hi:[1,0,1]\n"
    "v_pk_fma_f32 v[238:239], v[74:75], s[8:9], s[8:9] op_sel:[0,0,1] op_sel_hi:[1,0,1]\n"
    "v_pk_fma_f32 v[240:241], v[76:77], s[8:9], s[8:9] op_sel:[0,0,1] op_sel_hi:[1,0,1]\n"
    "v_pk_fma_f32 v[242:243], v[78:79], s[8:9], s[8:9] op_sel:[0,0,1] op_sel_hi:[1,0,1]\n"
    "v_max_f32 v72, s81, v72\n"
    "v_max_f32 v74, s81, v74\n"
    "v_max_f32 v76, s81, v76\n"
    "v_max_f32 v78, s81, v78\n"
    "v_max_f32 v73, s81, v73\n"
    "v_max_f32 v75, s81, v75\n"
    "v_max_f32 v77, s81, v77\n"
    "v_max_f32 v79, s81, v79\n"
    "v_exp_f32_e64 v236, v236 clamp\n"
    "v_exp_f32_e64 v238, v238 clamp\n"
    "v_exp_f32_e64 v240, v240 clamp\n"
    "v_exp_f32_e64 v242, v242 clamp\n"
    "v_exp_f32_e64 v237, v237 clamp\n"
    "v_exp_f32_e64 v239, v239 clamp\n"
    "v_exp_f32_e64 v241, v241 clamp\n"
    "v_exp_f32_e64 v243, v243 clamp\n"
    "v_pk_add_f32 v[72:73], v[72:73], v[236:237]\n"
    "v_pk_add_f32 v[74:75], v[74:75], v[238:239]\n"
    "v_pk_add_f32 v[76:77], v[76:77], v[240:241]\n"
    "v_pk_add_f32 v[78:79], v[78:79], v[242:243]\n"
    "v_cvt_pk_bf16_f32 v72, v72, v73\n"
    "v_cvt_pk_bf16_f32 v73, v74, v75\n"
    "v_cvt_pk_bf16_f32 v74, v76, v77\n"
    "v_cvt_pk_bf16_f32 v75, v78, v79\n"
    "v_pk_fma_f32 v[236:237], v[0:1], s[8:9], s[8:9] op_sel:[0,0,1] op_sel_hi:[1,0,1]\n"
    "v_pk_fma_f32 v[238:239], v[2:3], s[8:9], s[8:9] op_sel:[0,0,1] op_sel_hi:[1,0,1]\n"
    "v_pk_fma_f32 v[240:241], v[4:5], s[8:9], s[8:9] op_sel:[0,0,1] op_sel_hi:[1,0,1]\n"
    "v_pk_fma_f32 v[242:243], v[6:7], s[8:9], s[8:9] op_sel:[0,0,1] op_sel_hi:[1,0,1]\n"
    "v_max_f32 v0, s81, v0\n"
    "v_max_f32 v2, s81, v2\n"
    "v_max_f32 v4, s81, v4\n"
    "v_max_f32 v6, s81, v6\n"
    "v_max_f32 v1, s81, v1\n"
    "v_max_f32 v3, s81, v3\n"
    "v_max_f32 v5, s81, v5\n"
    "v_max_f32 v7, s81, v7\n"
    "v_exp_f32_e64 v236, v236 clamp\n"
    "v_exp_f32_e64 v238, v238 clamp\n"
    "v_exp_f32_e64 v240, v240 clamp\n"
    "v_exp_f32_e64 v242, v242 clamp\n"
    "v_exp_f32_e64 v237, v237 clamp\n"
    "v_exp_f32_e64 v239, v239 clamp\n"
    "v_exp_f32_e64 v241, v241 clamp\n"
    "v_exp_f32_e64 v243, v243 clamp\n"
    "v_pk_add_f32 v[0:1], v[0:1], v[236:237]\n"
    "v_pk_add_f32 v[2:3], v[2:3], v[238:239]\n"
    "v_pk_add_f32 v[4:5], v[4:5], v[240:241]\n"
    "v_pk_add_f32 v[6:7], v[6:7], v[242:243]\n"
    "v_cvt_pk_bf16_f32 v0, v0, v1\n"
    "v_cvt_pk_bf16_f32 v1, v2, v3\n"
    "v_cvt_pk_bf16_f32 v2, v4, v5\n"
    "v_cvt_pk_bf16_f32 v3, v6, v7\n"
    "v_pk_fma_f32 v[236:237], v[8:9], s[8:9], s[8:9] op_sel:[0,0,1] op_sel_hi:[1,0,1]\n"
    "v_pk_fma_f32 v[238:239], v[10:11], s[8:9], s[8:9] op_sel:[0,0,1] op_sel_hi:[1,0,1]\n"
    "v_pk_fma_f32 v[240:241], v[12:13], s[8:9], s[8:9] op_sel:[0,0,1] op_sel_hi:[1,0,1]\n"
    "v_pk_fma_f32 v[242:243], v[14:15], s[8:9], s[8:9] op_sel:[0,0,1] op_sel_hi:[1,0,1]\n"
    "v_max_f32 v8, s81, v8\n"
    "v_max_f32 v10, s81, v10\n"
    "v_max_f32 v12, s81, v12\n"
    "v_max_f32 v14, s81, v14\n"
    "v_max_f32 v9, s81, v9\n"
    "v_max_f32 v11, s81, v11\n"
    "v_max_f32 v13, s81, v13\n"
    "v_max_f32 v15, s81, v15\n"
    "v_exp_f32_e64 v236, v236 clamp\n"
    "v_exp_f32_e64 v238, v238 clamp\n"
    "v_exp_f32_e64 v240, v240 clamp\n"
    "v_exp_f32_e64 v242, v242 clamp\n"
    "v_exp_f32_e64 v237, v237 clamp\n"
    "v_exp_f32_e64 v239, v239 clamp\n"
    "v_exp_f32_e64 v241, v241 clamp\n"
    "v_exp_f32_e64 v243, v243 clamp\n"
    "v_pk_add_f32 v[8:9], v[8:9], v[236:237]\n"
    "v_pk_add_f32 v[10:11], v[10:11], v[238:239]\n"
    "v_pk_add_f32 v[12:13], v[12:13], v[240:241]\n"
    "v_pk_add_f32 v[14:15], v[14:15], v[242:243]\n"
    "v_cvt_pk_bf16_f32 v8, v8, v9\n"
    "v_cvt_pk_bf16_f32 v9, v10, v11\n"
    "v_cvt_pk_bf16_f32 v10, v12, v13\n"
    "v_cvt_pk_bf16_f32 v11, v14, v15\n"
    "v_pk_fma_f32 v[236:237], v[16:17], s[8:9], s[8:9] op_sel:[0,0,1] op_sel_hi:[1,0,1]\n"
    "v_pk_fma_f32 v[238:239], v[18:19], s[8:9], s[8:9] op_sel:[0,0,1] op_sel_hi:[1,0,1]\n"
    "v_pk_fma_f32 v[240:241], v[20:21], s[8:9], s[8:9] op_sel:[0,0,1] op_sel_hi:[1,0,1]\n"
    "v_pk_fma_f32 v[242:243], v[22:23], s[8:9], s[8:9] op_sel:[0,0,1] op_sel_hi:[1,0,1]\n"
    "v_max_f32 v16, s81, v16\n"
    "v_max_f32 v18, s81, v18\n"
    "v_max_f32 v20, s81, v20\n"
    "v_max_f32 v22, s81, v22\n"
    "v_max_f32 v17, s81, v17\n"
    "v_max_f32 v19, s81, v19\n"
    "v_max_f32 v21, s81, v21\n"
    "v_max_f32 v23, s81, v23\n"
    "v_exp_f32_e64 v236, v236 clamp\n"
    "v_exp_f32_e64 v238, v238 clamp\n"
    "v_exp_f32_e64 v240, v240 clamp\n"
    "v_exp_f32_e64 v242, v242 clamp\n"
    "v_exp_f32_e64 v237, v237 clamp\n"
    "v_exp_f32_e64 v239, v239 clamp\n"
    "v_exp_f32_e64 v241, v241 clamp\n"
    "v_exp_f32_e64 v243, v243 clamp\n"
    "v_pk_add_f32 v[16:17], v[16:17], v[236:237]\n"
    "v_pk_add_f32 v[18:19], v[18:19], v[238:239]\n"
    "v_pk_add_f32 v[20:21], v[20:21], v[240:241]\n"
    "v_pk_add_f32 v[22:23], v[22:23], v[242:243]\n"
    "v_cvt_pk_bf16_f32 v16, v16, v17\n"
    "v_cvt_pk_bf16_f32 v17, v18, v19\n"
    "v_cvt_pk_bf16_f32 v18, v20, v21\n"
    "v_cvt_pk_bf16_f32 v19, v22, v23\n"
    "v_pk_fma_f32 v[236:237], v[24:25], s[8:9], s[8:9] op_sel:[0,0,1] op_sel_hi:[1,0,1]\n"
    "v_pk_fma_f32 v[238:239], v[26:27], s[8:9], s[8:9] op_sel:[0,0,1] op_sel_hi:[1,0,1]\n"
    "v_pk_fma_f32 v[240:241], v[28:29], s[8:9], s[8:9] op_sel:[0,0,1] op_sel_hi:[1,0,1]\n"
    "v_pk_fma_f32 v[242:243], v[30:31], s[8:9], s[8:9] op_sel:[0,0,1] op_sel_hi:[1,0,1]\n"
    "v_max_f32 v24, s81, v24\n"
    "v_max_f32 v26, s81, v26\n"
    "v_max_f32 v28, s81, v28\n"
    "v_max_f32 v30, s81, v30\n"
    "v_max_f32 v25, s81, v25\n"
    "v_max_f32 v27, s81, v27\n"
    "v_max_f32 v29, s81, v29\n"
    "v_max_f32 v31, s81, v31\n"
    "v_exp_f32_e64 v236, v236 clamp\n"
    "v_exp_f32_e64 v238, v238 clamp\n"
    "v_exp_f32_e64 v240, v240 clamp\n"
    "v_exp_f32_e64 v242, v242 clamp\n"
    "v_exp_f32_e64 v237, v237 clamp\n"
    "v_exp_f32_e64 v239, v239 clamp\n"
    "v_exp_f32_e64 v241, v241 clamp\n"
    "v_exp_f32_e64 v243, v243 clamp\n"
    "v_pk_add_f32 v[24:25], v[24:25], v[236:237]\n"
    "v_pk_add_f32 v[26:27], v[26:27], v[238:239]\n"
    "v_pk_add_f32 v[28:29], v[28:29], v[240:241]\n"
    "v_pk_add_f32 v[30:31], v[30:31], v[242:243]\n"
    "v_cvt_pk_bf16_f32 v24, v24, v25\n"
    "v_cvt_pk_bf16_f32 v25, v26, v27\n"
    "v_cvt_pk_bf16_f32 v26, v28, v29\n"
    "v_cvt_pk_bf16_f32 v27, v30, v31\n"
    "v_pk_fma_f32 v[236:237], v[32:33], s[8:9], s[8:9] op_sel:[0,0,1] op_sel_hi:[1,0,1]\n"
    "v_pk_fma_f32 v[238:239], v[34:35], s[8:9], s[8:9] op_sel:[0,0,1] op_sel_hi:[1,0,1]\n"
    "v_pk_fma_f32 v[240:241], v[36:37], s[8:9], s[8:9] op_sel:[0,0,1] op_sel_hi:[1,0,1]\n"
    "v_pk_fma_f32 v[242:243], v[38:39], s[8:9], s[8:9] op_sel:[0,0,1] op_sel_hi:[1,0,1]\n"
    "v_max_f32 v32, s81, v32\n"
    "v_max_f32 v34, s81, v34\n"
    "v_max_f32 v36, s81, v36\n"
    "v_max_f32 v38, s81, v38\n"
    "v_max_f32 v33, s81, v33\n"
    "v_max_f32 v35, s81, v35\n"
    "v_max_f32 v37, s81, v37\n"
    "v_max_f32 v39, s81, v39\n"
    "v_exp_f32_e64 v236, v236 clamp\n"
    "v_exp_f32_e64 v238, v238 clamp\n"
    "v_exp_f32_e64 v240, v240 clamp\n"
    "v_exp_f32_e64 v242, v242 clamp\n"
    "v_exp_f32_e64 v237, v237 clamp\n"
    "v_exp_f32_e64 v239, v239 clamp\n"
    "v_exp_f32_e64 v241, v241 clamp\n"
    "v_exp_f32_e64 v243, v243 clamp\n"
    "v_pk_add_f32 v[32:33], v[32:33], v[236:237]\n"
    "v_pk_add_f32 v[34:35], v[34:35], v[238:239]\n"
    "v_pk_add_f32 v[36:37], v[36:37], v[240:241]\n"
    "v_pk_add_f32 v[38:39], v[38:39], v[242:243]\n"
    "v_cvt_pk_bf16_f32 v32, v32, v33\n"
    "v_cvt_pk_bf16_f32 v33, v34, v35\n"
    "v_cvt_pk_bf16_f32 v34, v36, v37\n"
    "v_cvt_pk_bf16_f32 v35, v38, v39\n"
    "v_pk_fma_f32 v[236:237], v[40:41], s[8:9], s[8:9] op_sel:[0,0,1] op_sel_hi:[1,0,1]\n"
    "v_pk_fma_f32 v[238:239], v[42:43], s[8:9], s[8:9] op_sel:[0,0,1] op_sel_hi:[1,0,1]\n"
    "v_pk_fma_f32 v[240:241], v[44:45], s[8:9], s[8:9] op_sel:[0,0,1] op_sel_hi:[1,0,1]\n"
    "v_pk_fma_f32 v[242:243], v[46:47], s[8:9], s[8:9] op_sel:[0,0,1] op_sel_hi:[1,0,1]\n"
    "v_max_f32 v40, s81, v40\n"
    "v_max_f32 v42, s81, v42\n"
    "v_max_f32 v44, s81, v44\n"
    "v_max_f32 v46, s81, v46\n"
    "v_max_f32 v41, s81, v41\n"
    "v_max_f32 v43, s81, v43\n"
    "v_max_f32 v45, s81, v45\n"
    "v_max_f32 v47, s81, v47\n"
    "v_exp_f32_e64 v236, v236 clamp\n"
    "v_exp_f32_e64 v238, v238 clamp\n"
    "v_exp_f32_e64 v240, v240 clamp\n"
    "v_exp_f32_e64 v242, v242 clamp\n"
    "v_exp_f32_e64 v237, v237 clamp\n"
    "v_exp_f32_e64 v239, v239 clamp\n"
    "v_exp_f32_e64 v241, v241 clamp\n"
    "v_exp_f32_e64 v243, v243 clamp\n"
    "v_pk_add_f32 v[40:41], v[40:41], v[236:237]\n"
    "v_pk_add_f32 v[42:43], v[42:43], v[238:239]\n"
    "v_pk_add_f32 v[44:45], v[44:45], v[240:241]\n"
    "v_pk_add_f32 v[46:47], v[46:47], v[242:243]\n"
    "v_cvt_pk_bf16_f32 v40, v40, v41\n"
    "v_cvt_pk_bf16_f32 v41, v42, v43\n"
    "v_cvt_pk_bf16_f32 v42, v44, v45\n"
    "v_cvt_pk_bf16_f32 v43, v46, v47\n"
    "v_pk_fma_f32 v[236:237], v[48:49], s[8:9], s[8:9] op_sel:[0,0,1] op_sel_hi:[1,0,1]\n"
    "v_pk_fma_f32 v[238:239], v[50:51], s[8:9], s[8:9] op_sel:[0,0,1] op_sel_hi:[1,0,1]\n"
    "v_pk_fma_f32 v[240:241], v[52:53], s[8:9], s[8:9] op_sel:[0,0,1] op_sel_hi:[1,0,1]\n"
    "v_pk_fma_f32 v[242:243], v[54:55], s[8:9], s[8:9] op_sel:[0,0,1] op_sel_hi:[1,0,1]\n"
    "v_max_f32 v48, s81, v48\n"
    "v_max_f32 v50, s81, v50\n"
    "v_max_f32 v52, s81, v52\n"
    "v_max_f32 v54, s81, v54\n"
    "v_max_f32 v49, s81, v49\n"
    "v_max_f32 v51, s81, v51\n"
    "v_max_f32 v53, s81, v53\n"
    "v_max_f32 v55, s81, v55\n"
    "v_exp_f32_e64 v236, v236 clamp\n"
    "v_exp_f32_e64 v238, v238 clamp\n"
    "v_exp_f32_e64 v240, v240 clamp\n"
    "v_exp_f32_e64 v242, v242 clamp\n"
    "v_exp_f32_e64 v237, v237 clamp\n"
    "v_exp_f32_e64 v239, v239 clamp\n"
    "v_exp_f32_e64 v241, v241 clamp\n"
    "v_exp_f32_e64 v243, v243 clamp\n"
    "v_pk_add_f32 v[48:49], v[48:49], v[236:237]\n"
    "v_pk_add_f32 v[50:51], v[50:51], v[238:239]\n"
    "v_pk_add_f32 v[52:53], v[52:53], v[240:241]\n"
    "v_pk_add_f32 v[54:55], v[54:55], v[242:243]\n"
    "v_cvt_pk_bf16_f32 v48, v48, v49\n"
    "v_cvt_pk_bf16_f32 v49, v50, v51\n"
    "v_cvt_pk_bf16_f32 v50, v52, v53\n"
    "v_cvt_pk_bf16_f32 v51, v54, v55\n"
    "v_pk_fma_f32 v[236:237], v[56:57], s[8:9], s[8:9] op_sel:[0,0,1] op_sel_hi:[1,0,1]\n"
    "v_pk_fma_f32 v[238:239], v[58:59], s[8:9], s[8:9] op_sel:[0,0,1] op_sel_hi:[1,0,1]\n"
    "v_pk_fma_f32 v[240:241], v[60:61], s[8:9], s[8:9] op_sel:[0,0,1] op_sel_hi:[1,0,1]\n"
    "v_pk_fma_f32 v[242:243], v[62:63], s[8:9], s[8:9] op_sel:[0,0,1] op_sel_hi:[1,0,1]\n"
    "v_max_f32 v56, s81, v56\n"
    "v_max_f32 v58, s81, v58\n"
    "v_max_f32 v60, s81, v60\n"
    "v_max_f32 v62, s81, v62\n"
    "v_max_f32 v57, s81, v57\n"
    "v_max_f32 v59, s81, v59\n"
    "v_max_f32 v61, s81, v61\n"
    "v_max_f32 v63, s81, v63\n"
    "v_exp_f32_e64 v236, v236 clamp\n"
    "v_exp_f32_e64 v238, v238 clamp\n"
    "v_exp_f32_e64 v240, v240 clamp\n"
    "v_exp_f32_e64 v242, v242 clamp\n"
    "v_exp_f32_e64 v237, v237 clamp\n"
    "v_exp_f32_e64 v239, v239 clamp\n"
    "v_exp_f32_e64 v241, v241 clamp\n"
    "v_exp_f32_e64 v243, v243 clamp\n"
    "v_pk_add_f32 v[56:57], v[56:57], v[236:237]\n"
    "v_pk_add_f32 v[58:59], v[58:59], v[238:239]\n"
    "v_pk_add_f32 v[60:61], v[60:61], v[240:241]\n"
    "v_pk_add_f32 v[62:63], v[62:63], v[242:243]\n"
    "v_cvt_pk_bf16_f32 v56, v56, v57\n"
    "v_cvt_pk_bf16_f32 v57, v58, v59\n"
    "v_cvt_pk_bf16_f32 v58, v60, v61\n"
    "v_cvt_pk_bf16_f32 v59, v62, v63\n"
    "v_mfma_f32_16x16x32_bf16 v[100:103], v[64:67], v[80:83], 0\n"
    "s_mov_b64 s[10:11], 0xffff\n"
    "v_lshrrev_b32 v224, 2, v255\n"
    "v_and_b32 v225, 3, v255\n"
    "v_mfma_f32_16x16x32_bf16 v[100:103], v[72:75], v[88:91], v[100:103]\n"
    "s_mov_b32 s12, 0x3030303\n"
    "v_mul_u32_u24 v226, 288, v224\n"
    "v_lshl_add_u32 v244, v225, 3, v226\n"
    "v_mfma_f32_16x16x32_bf16 v[108:111], v[64:67], v[96:99], 0\n"
    "s_mov_b32 s13, 0x3030303\n"
    "v_add_u32 v245, 55296, v244\n"
    "v_add_u32 v246, 110592, v244\n"
    "v_mfma_f32_16x16x32_bf16 v[108:111], v[72:75], v[104:107], v[108:111]\n"
    "s_mov_b32 s14, 0xc0c0c0c\n"
    "v_min_u32 v247, 2, v225\n"
    "v_lshlrev_b32 v247, 15, v247\n"
    "v_mfma_f32_16x16x32_bf16 v[116:119], v[64:67], v[112:115], 0\n"
    "s_mov_b32 s15, 0xc0c0c0c\n"
    "v_mov_b32 v250, s66\n"
    "v_lshlrev_b32 v249, 2, v255\n"
    "v_mfma_f32_16x16x32_bf16 v[116:119], v[72:75], v[120:123], v[116:119]\n"
    "s_mov_b32 s16, 0x30303030\n"
    "v_and_b32 v235, 0xf0, v254\n"
    "v_mfma_f32_16x16x32_bf16 v[124:127], v[64:67], v[128:131], 0\n"
    "s_mov_b32 s17, 0x30303030\n"
    "v_mfma_f32_16x16x32_bf16 v[124:127], v[72:75], v[136:139], v[124:127]\n"
    "s_mov_b32 s18, 0xc0c0c0c0\n"
    "v_mfma_f32_16x16x32_bf16 v[12:15], v[0:3], v[84:87], 0\n"
    "s_mov_b32 s19, 0xc0c0c0c0\n"
    "v_mfma_f32_16x16x32_bf16 v[12:15], v[8:11], v[92:95], v[12:15]\n"
    "s_lshl_b32 s82, s7, 13\n"
    "v_mfma_f32_16x16x32_bf16 v[28:31], v[16:19], v[84:87], 0\n"
    "s_add_u32 s82, s82, 122880\n"
    "v_mfma_f32_16x16x32_bf16 v[28:31], v[24:27], v[92:95], v[28:31]\n"
    "s_lshl_b32 s83, s7, 10\n"
    "v_cndmask_b32 v100, v116, v100, s[10:11]\n"
    "v_cndmask_b32 v101, v117, v101, s[10:11]\n"
    "v_mfma_f32_16x16x32_bf16 v[44:47], v[32:35], v[84:87], 0\n"
    "s_add_u32 s84, s83, 122880\n"
    "v_cndmask_b32 v102, v126, v110, s[10:11]\n"
    "v_cndmask_b32 v103, v127, v111, s[10:11]\n"
    "v_mfma_f32_16x16x32_bf16 v[44:47], v[40:43], v[92:95], v[44:47]\n"
    "v_add_u32 v232, s82, v254\n"
    "v_add_u32 v233, s84, v254\n"
    "v_mfma_f32_16x16x32_bf16 v[60:63], v[48:51], v[84:87], 0\n"
    "v_add_u32 v234, s83, v254\n"
    "v_add_u32 v235, s83, v235\n"
    "v_mfma_f32_16x16x32_bf16 v[60:63], v[56:59], v[92:95], v[60:63]\n"
    "ds_write_b128 v232, v[100:103] offset:5120\n"
    "v_mfma_f32_16x16x32_bf16 v[132:135], v[64:67], v[84:87], 0\n"
    "v_mfma_f32_16x16x32_bf16 v[132:135], v[72:75], v[92:95], v[132:135]\n"
    "v_mfma_f32_16x16x32_bf16 v[4:7], v[0:3], v[80:83], 0\n"
    "v_mfma_f32_16x16x32_bf16 v[4:7], v[8:11], v[88:91], v[4:7]\n"
    "v_cndmask_b32 v12, v12, v28, s[14:15]\n"
    "v_cndmask_b32 v13, v13, v29, s[14:15]\n"
    "v_mfma_f32_16x16x32_bf16 v[20:23], v[16:19], v[96:99], 0\n"
    "v_cndmask_b32 v14, v14, v30, s[14:15]\n"
    "v_cndmask_b32 v15, v15, v31, s[14:15]\n"
    "v_mfma_f32_16x16x32_bf16 v[20:23], v[24:27], v[104:107], v[20:23]\n"
    "v_cndmask_b32 v12, v12, v44, s[16:17]\n"
    "v_cndmask_b32 v13, v13, v45, s[16:17]\n"
    "v_mfma_f32_16x16x32_bf16 v[36:39], v[32:35], v[112:115], 0\n"
    "v_cndmask_b32 v14, v14, v46, s[16:17]\n"
    "v_cndmask_b32 v15, v15, v47, s[16:17]\n"
    "v_mfma_f32_16x16x32_bf16 v[36:39], v[40:43], v[120:123], v[36:39]\n"
    "v_cndmask_b32 v12, v12, v60, s[18:19]\n"
    "v_cndmask_b32 v13, v13, v61, s[18:19]\n"
    "v_mfma_f32_16x16x32_bf16 v[52:55], v[48:51], v[128:131], 0\n"
    "v_cndmask_b32 v14, v14, v62, s[18:19]\n"
    "v_cndmask_b32 v15, v15, v63, s[18:19]\n"
    "v_mfma_f32_16x16x32_bf16 v[52:55], v[56:59], v[136:139], v[52:55]\n"
    "ds_write_b128 v232, v[132:135] offset:6144\n"
    "ds_write_b128 v232, v[12:15] offset:4096\n"
    "ds_write_b128 v232, v[4:7] offset:0\n"
    "ds_write_b128 v232, v[20:23] offset:1024\n"
    "ds_write_b128 v232, v[36:39] offset:2048\n"
    "s_nop 2\n"
    "ds_write_b128 v232, v[52:55] offset:3072\n"
    "v_pk_fma_f32 v[236:237], v[208:209], s[8:9], s[8:9] op_sel:[0,0,1] op_sel_hi:[1,0,1]\n"
    "v_pk_fma_f32 v[238:239], v[210:211], s[8:9], s[8:9] op_sel:[0,0,1] op_sel_hi:[1,0,1]\n"
    "v_pk_fma_f32 v[240:241], v[212:213], s[8:9], s[8:9] op_sel:[0,0,1] op_sel_hi:[1,0,1]\n"
    "v_pk_fma_f32 v[242:243], v[214:215], s[8:9], s[8:9] op_sel:[0,0,1] op_sel_hi:[1,0,1]\n"
    "v_max_f32 v208, s81, v208\n"
    "v_max_f32 v210, s81, v210\n"
    "v_max_f32 v212, s81, v212\n"
    "v_max_f32 v214, s81, v214\n"
    "v_max_f32 v209, s81, v209\n"
    "v_max_f32 v211, s81, v211\n"
    "v_max_f32 v213, s81, v213\n"
    "v_max_f32 v215, s81, v215\n"
    "v_exp_f32_e64 v236, v236 clamp\n"
    "v_exp_f32_e64 v238, v238 clamp\n"
    "v_exp_f32_e64 v240, v240 clamp\n"
    "v_exp_f32_e64 v242, v242 clamp\n"
    "v_exp_f32_e64 v237, v237 clamp\n"
    "v_exp_f32_e64 v239, v239 clamp\n"
    "v_exp_f32_e64 v241, v241 clamp\n"
    "v_exp_f32_e64 v243, v243 clamp\n"
    "v_pk_add_f32 v[208:209], v[208:209], v[236:237]\n"
    "v_pk_add_f32 v[210:211], v[210:211], v[238:239]\n"
    "v_pk_add_f32 v[212:213], v[212:213], v[240:241]\n"
    "v_pk_add_f32 v[214:215], v[214:215], v[242:243]\n"
    "v_cvt_pk_bf16_f32 v208, v208, v209\n"
    "v_cvt_pk_bf16_f32 v209, v210, v211\n"
    "v_cvt_pk_bf16_f32 v210, v212, v213\n"
    "v_cvt_pk_bf16_f32 v211, v214, v215\n"
    "s_waitcnt vmcnt(1)\n"
    "s_waitcnt lgkmcnt(0)\n"
    "s_barrier\n"
    "ds_read_b128 v[0:3], v233 offset:0\n"
    "ds_read_b128 v[4:7], v233 offset:8192\n"
    "ds_read_b128 v[8:11], v233 offset:16384\n"
    "ds_read_b128 v[12:15], v233 offset:24576\n"
    "ds_read_b128 v[16:19], v233 offset:4096\n"
    "ds_read_b128 v[20:23], v233 offset:12288\n"
    "ds_read_b128 v[24:27], v233 offset:20480\n"
    "ds_read_b128 v[28:31], v233 offset:28672\n"
    "v_pk_fma_f32 v[236:237], v[216:217], s[8:9], s[8:9] op_sel:[0,0,1] op_sel_hi:[1,0,1]\n"
    "v_pk_fma_f32 v[238:239], v[218:219], s[8:9], s[8:9] op_sel:[0,0,1] op_sel_hi:[1,0,1]\n"
    "v_pk_fma_f32 v[240:241], v[220:221], s[8:9], s[8:9] op_sel:[0,0,1] op_sel_hi:[1,0,1]\n"
    "v_pk_fma_f32 v[242:243], v[222:223], s[8:9], s[8:9] op_sel:[0,0,1] op_sel_hi:[1,0,1]\n"
    "v_max_f32 v216, s81, v216\n"
    "v_max_f32 v218, s81, v218\n"
    "v_max_f32 v220, s81, v220\n"
    "v_max_f32 v222, s81, v222\n"
    "v_max_f32 v217, s81, v217\n"
    "v_max_f32 v219, s81, v219\n"
    "v_max_f32 v221, s81, v221\n"
    "v_max_f32 v223, s81, v223\n"
    "v_exp_f32_e64 v236, v236 clamp\n"
    "v_exp_f32_e64 v238, v238 clamp\n"
    "v_exp_f32_e64 v240, v240 clamp\n"
    "v_exp_f32_e64 v242, v242 clamp\n"
    "v_exp_f32_e64 v237, v237 clamp\n"
    "v_exp_f32_e64 v239, v239 clamp\n"
    "v_exp_f32_e64 v241, v241 clamp\n"
    "v_exp_f32_e64 v243, v243 clamp\n"
    "v_pk_add_f32 v[216:217], v[216:217], v[236:237]\n"
    "v_pk_add_f32 v[218:219], v[218:219], v[238:239]\n"
    "v_pk_add_f32 v[220:221], v[220:221], v[240:241]\n"
    "v_pk_add_f32 v[222:223], v[222:223], v[242:243]\n"
    "v_cvt_pk_bf16_f32 v212, v216, v217\n"
    "v_cvt_pk_bf16_f32 v213, v218, v219\n"
    "v_cvt_pk_bf16_f32 v214, v220, v221\n"
    "v_cvt_pk_bf16_f32 v215, v222, v223\n"
    "s_mov_b32 s82, 0x07060501\n"
    "s_mov_b32 s83, 0x07060502\n"
    "s_mov_b32 s84, 0x07060503\n"
    "s_waitcnt lgkmcnt(6)\n"
    "v_pk_add_f32 v[0:1], v[0:1], v[4:5]\n"
    "v_pk_add_f32 v[2:3], v[2:3], v[6:7]\n"
    "s_waitcnt lgkmcnt(4)\n"
    "v_pk_add_f32 v[8:9], v[8:9], v[12:13]\n"
    "v_pk_add_f32 v[10:11], v[10:11], v[14:15]\n"
    "v_pk_add_f32 v[0:1], v[0:1], v[8:9]\n"
    "v_pk_add_f32 v[2:3], v[2:3], v[10:11]\n"
    "s_waitcnt lgkmcnt(2)\n"
    "v_pk_add_f32 v[16:17], v[16:17], v[20:21]\n"
    "v_pk_add_f32 v[18:19], v[18:19], v[22:23]\n"
    "s_waitcnt lgkmcnt(0)\n"
    "v_pk_add_f32 v[24:25], v[24:25], v[28:29]\n"
    "v_pk_add_f32 v[26:27], v[26:27], v[30:31]\n"
    "v_pk_add_f32 v[16:17], v[16:17], v[24:25]\n"
    "v_pk_add_f32 v[18:19], v[18:19], v[26:27]\n"
    "v_perm_b32 v0, v0, v3, s82\n"
    "v_perm_b32 v1, v1, v3, s83\n"
    "v_perm_b32 v2, v2, v3, s84\n"
    "v_mov_b32 v3, s66\n"
    "buffer_store_dwordx4 v[0:3], v254, s[20:23], s38 offen offset:0 sc1\n"
    "v_perm_b32 v16, v16, v19, s82\n"
    "v_perm_b32 v17, v17, v19, s83\n"
    "v_perm_b32 v18, v18, v19, s84\n"
    "v_mov_b32 v19, s66\n"
    "buffer_store_dwordx4 v[16:19], v254, s[20:23], s38 offen offset:1024 sc1\n"
    "buffer_load_dword v248, v247, s[20:23], s77 offen offset:0 sc1\n"
    "s_add_u32 m0, s78, 9216\n"
    "s_add_u32 s56, s68, 147456\n"
    "buffer_load_dwordx4 v251, s[24:27], s56 offen offset:0 lds\n"
    "s_mov_b32 s82, 0xffff0000\n"
    "s_mov_b32 s83, 0\n"
    "v_cndmask_b32 v66, 0, v208, s[10:11]\n"
    "v_mov_b32 v67, 0\n"
    "v_cndmask_b32 v70, 0, v210, s[10:11]\n"
    "v_mov_b32 v71, 0\n"
    "v_cndmask_b32 v74, 0, v212, s[10:11]\n"
    "v_mov_b32 v75, 0\n"
    "v_cndmask_b32 v78, 0, v214, s[10:11]\n"
    "v_mov_b32 v79, 0\n"
    "buffer_load_dwordx4 v252, s[24:27], s56 offen offset:1024 lds\n"
    "v_cndmask_b32 v83, 0, v209, s[10:11]\n"
    "v_mov_b32 v82, 0\n"
    "v_cndmask_b32 v87, 0, v211, s[10:11]\n"
    "v_mov_b32 v86, 0\n"
    "v_cndmask_b32 v91, 0, v213, s[10:11]\n"
    "v_mov_b32 v90, 0\n"
    "v_cndmask_b32 v95, 0, v215, s[10:11]\n"
    "v_mov_b32 v94, 0\n"
    "v_cndmask_b32 v98, 0, v208, s[82:83]\n"
    "v_mov_b32 v99, 0\n"
    "v_cndmask_b32 v102, 0, v210, s[82:83]\n"
    "s_add_u32 m0, s79, 9216\n"
    "s_nop 0\n"
    "buffer_load_dwordx4 v253, s[24:27], s56 offen offset:0 lds\n"
    "v_mov_b32 v103, 0\n"
    "v_cndmask_b32 v106, 0, v212, s[82:83]\n"
    "v_mov_b32 v107, 0\n"
    "v_cndmask_b32 v110, 0, v214, s[82:83]\n"
    "v_mov_b32 v111, 0\n"
    "v_cndmask_b32 v115, 0, v209, s[82:83]\n"
    "v_mov_b32 v114, 0\n"
    "v_cndmask_b32 v119, 0, v211, s[82:83]\n"
    "v_mov_b32 v118, 0\n"
    "v_cndmask_b32 v123, 0, v213, s[82:83]\n"
    "v_mov_b32 v122, 0\n"
    "s_add_u32 m0, s78, 18432\n"
    "s_add_u32 s56, s56, 36864\n"
    "buffer_load_dwordx4 v251, s[24:27], s56 offen offset:0 lds\n"
    "v_cndmask_b32 v127, 0, v215, s[82:83]\n"
    "v_mov_b32 v126, 0\n"
    "s_waitcnt vmcnt(7)\n"
    "v_cmp_ne_u32 vcc, s66, v231\n"
    "s_nop 1\n"
    "s_cbranch_vccnz RT6_%=\n"
    "RB6_%=:\n"
    "v_mul_f32 v230, 0x3fb8aa3b, v230\n"
    "v_exp_f32 v231, -v230\n"
    "v_exp_f32 v230, v230\n"
    "v_add_f32 v231, 1.0, v231\n"
    "v_add_f32 v230, 1.0, v230\n"
    "v_rcp_f32 v231, v231\n"
    "buffer_load_dwordx4 v252, s[24:27], s56 offen offset:1024 lds\n"
    "v_min_f32 v230, 0x7f000000, v230\n"
    "ds_write_b128 v234, v[228:231] offset:0\n"
    "ds_read_b128 v[208:211], v235 offset:0\n"
    "ds_read_b128 v[212:215], v235 offset:256\n"
    "ds_read_b128 v[216:219], v235 offset:512\n"
    "ds_read_b128 v[220:223], v235 offset:768\n"
    "v_pk_fma_f32 v[236:237], v[144:145], s[8:9], s[8:9] op_sel:[0,0,1] op_sel_hi:[1,0,1]\n"
    "v_pk_fma_f32 v[238:239], v[146:147], s[8:9], s[8:9] op_sel:[0,0,1] op_sel_hi:[1,0,1]\n"
    "v_pk_fma_f32 v[240:241], v[148:149], s[8:9], s[8:9] op_sel:[0,0,1] op_sel_hi:[1,0,1]\n"
    "v_pk_fma_f32 v[242:243], v[150:151], s[8:9], s[8:9] op_sel:[0,0,1] op_sel_hi:[1,0,1]\n"
    "v_max_f32 v144, s81, v144\n"
    "s_add_u32 m0, s78, 27648\n"
    "s_add_u32 s56, s69, 147456\n"
    "buffer_load_dwordx4 v251, s[24:27], s56 offen offset:0 lds\n"
    "v_max_f32 v146, s81, v146\n"
    "v_max_f32 v148, s81, v148\n"
    "v_max_f32 v150, s81, v150\n"
    "v_max_f32 v145, s81, v145\n"
    "v_max_f32 v147, s81, v147\n"
    "v_max_f32 v149, s81, v149\n"
    "v_max_f32 v151, s81, v151\n"
    "v_exp_f32_e64 v236, v236 clamp\n"
    "v_exp_f32_e64 v238, v238 clamp\n"
    "v_exp_f32_e64 v240, v240 clamp\n"
    "v_exp_f32_e64 v242, v242 clamp\n"
    "buffer_load_dwordx4 v252, s[24:27], s56 offen offset:1024 lds\n"
    "v_exp_f32_e64 v237, v237 clamp\n"
    "v_exp_f32_e64 v239, v239 clamp\n"
    "v_exp_f32_e64 v241, v241 clamp\n"
    "v_exp_f32_e64 v243, v243 clamp\n"
    "v_pk_add_f32 v[144:145], v[144:145], v[236:237]\n"
    "v_pk_add_f32 v[146:147], v[146:147], v[238:239]\n"
    "v_pk_add_f32 v[148:149], v[148:149], v[240:241]\n"
    "v_pk_add_f32 v[150:151], v[150:151], v[242:243]\n"
    "v_cvt_pk_bf16_f32 v64, v144, v145\n"
    "v_cvt_pk_bf16_f32 v65, v146, v147\n"
    "v_cvt_pk_bf16_f32 v68, v148, v149\n"
    "s_add_u32 m0, s79, 27648\n"
    "s_nop 0\n"
    "buffer_load_dwordx4 v253, s[24:27], s56 offen offset:0 lds\n"
    "v_cvt_pk_bf16_f32 v69, v150, v151\n"
    "v_pk_fma_f32 v[236:237], v[152:153], s[8:9], s[8:9] op_sel:[0,0,1] op_sel_hi:[1,0,1]\n"
    "v_pk_fma_f32 v[238:239], v[154:155], s[8:9], s[8:9] op_sel:[0,0,1] op_sel_hi:[1,0,1]\n"
    "v_pk_fma_f32 v[240:241], v[156:157], s[8:9], s[8:9] op_sel:[0,0,1] op_sel_hi:[1,0,1]\n"
    "v_pk_fma_f32 v[242:243], v[158:159], s[8:9], s[8:9] op_sel:[0,0,1] op_sel_hi:[1,0,1]\n"
    "v_max_f32 v152, s81, v152\n"
    "v_max_f32 v154, s81, v154\n"
    "v_max_f32 v156, s81, v156\n"
    "v_max_f32 v158, s81, v158\n"
    "v_max_f32 v153, s81, v153\n"
    "v_max_f32 v155, s81, v155\n"
    "s_add_u32 m0, s78, 36864\n"
    "s_add_u32 s56, s56, 36864\n"
    "buffer_load_dwordx4 v251, s[24:27], s56 offen offset:0 lds\n"
    "v_max_f32 v157, s81, v157\n"
    "v_max_f32 v159, s81, v159\n"
    "v_exp_f32_e64 v236, v236 clamp\n"
    "v_exp_f32_e64 v238, v238 clamp\n"
    "v_exp_f32_e64 v240, v240 clamp\n"
    "v_exp_f32_e64 v242, v242 clamp\n"
    "v_exp_f32_e64 v237, v237 clamp\n"
    "v_exp_f32_e64 v239, v239 clamp\n"
    "v_exp_f32_e64 v241, v241 clamp\n"
    "v_exp_f32_e64 v243, v243 clamp\n"
    "v_pk_add_f32 v[152:153], v[152:153], v[236:237]\n"
    "buffer_load_dwordx4 v252, s[24:27], s56 offen offset:1024 lds\n"
    "v_pk_add_f32 v[154:155], v[154:155], v[238:239]\n"
    "v_pk_add_f32 v[156:157], v[156:157], v[240:241]\n"
    "v_pk_add_f32 v[158:159], v[158:159], v[242:243]\n"
    "v_cvt_pk_bf16_f32 v72, v152, v153\n"
    "v_cvt_pk_bf16_f32 v73, v154, v155\n"
    "v_cvt_pk_bf16_f32 v76, v156, v157\n"
    "v_cvt_pk_bf16_f32 v77, v158, v159\n"
    "v_pk_fma_f32 v[236:237], v[160:161], s[8:9], s[8:9] op_sel:[0,0,1] op_sel_hi:[1,0,1]\n"
    "v_pk_fma_f32 v[238:239], v[162:163], s[8:9], s[8:9] op_sel:[0,0,1] op_sel_hi:[1,0,1]\n"
    "v_pk_fma_f32 v[240:241], v[164:165], s[8:9], s[8:9] op_sel:[0,0,1] op_sel_hi:[1,0,1]\n"
    "v_pk_fma_f32 v[242:243], v[166:167], s[8:9], s[8:9] op_sel:[0,0,1] op_sel_hi:[1,0,1]\n"
    "s_add_u32 m0, s78, 46080\n"
    "s_add_u32 s56, s70, 147456\n"
    "buffer_load_dwordx4 v251, s[24:27], s56 offen offset:0 lds\n"
    "v_max_f32 v160, s81, v160\n"
    "v_max_f32 v162, s81, v162\n"
    "v_max_f32 v164, s81, v164\n"
    "v_max_f32 v166, s81, v166\n"
    "v_max_f32 v161, s81, v161\n"
    "v_max_f32 v163, s81, v163\n"
    "v_max_f32 v165, s81, v165\n"
    "v_max_f32 v167, s81, v167\n"
    "v_exp_f32_e64 v236, v236 clamp\n"
    "v_exp_f32_e64 v238, v238 clamp\n"
    "v_exp_f32_e64 v240, v240 clamp\n"
    "buffer_load_dwordx4 v252, s[24:27], s56 offen offset:1024 lds\n"
    "v_exp_f32_e64 v242, v242 clamp\n"
    "v_exp_f32_e64 v237, v237 clamp\n"
    "v_exp_f32_e64 v239, v239 clamp\n"
    "v_exp_f32_e64 v241, v241 clamp\n"
    "v_exp_f32_e64 v243, v243 clamp\n"
    "v_pk_add_f32 v[160:161], v[160:161], v[236:237]\n"
    "v_pk_add_f32 v[162:163], v[162:163], v[238:239]\n"
    "v_pk_add_f32 v[164:165], v[164:165], v[240:241]\n"
    "v_pk_add_f32 v[166:167], v[166:167], v[242:243]\n"
    "v_cvt_pk_bf16_f32 v80, v160, v161\n"
    "v_cvt_pk_bf16_f32 v81, v162, v163\n"
    "s_add_u32 m0, s79, 46080\n"
    "s_nop 0\n"
    "buffer_load_dwordx4 v253, s[24:27], s56 offen offset:0 lds\n"
    "v_cvt_pk_bf16_f32 v84, v164, v165\n"
    "v_cvt_pk_bf16_f32 v85, v166, v167\n"
    "v_pk_fma_f32 v[236:237], v[168:169], s[8:9], s[8:9] op_sel:[0,0,1] op_sel_hi:[1,0,1]\n"
    "v_pk_fma_f32 v[238:239], v[170:171], s[8:9], s[8:9] op_sel:[0,0,1] op_sel_hi:[1,0,1]\n"
    "v_pk_fma_f32 v[240:241], v[172:173], s[8:9], s[8:9] op_sel:[0,0,1] op_sel_hi:[1,0,1]\n"
    "v_pk_fma_f32 v[242:243], v[174:175], s[8:9], s[8:9] op_sel:[0,0,1] op_sel_hi:[1,0,1]\n"
    "v_max_f32 v168, s81, v168\n"
    "v_max_f32 v170, s81, v170\n"
    "v_max_f32 v172, s81, v172\n"
    "v_max_f32 v174, s81, v174\n"
    "s_add_u32 m0, s78, 55296\n"
    "s_add_u32 s56, s56, 36864\n"
    "buffer_load_dwordx4 v251, s[24:27], s56 offen offset:0 lds\n"
    "v_max_f32 v169, s81, v169\n"
    "v_max_f32 v171, s81, v171\n"
    "v_max_f32 v173, s81, v173\n"
    "v_max_f32 v175, s81, v175\n"
    "v_exp_f32_e64 v236, v236 clamp\n"
    "v_exp_f32_e64 v238, v238 clamp\n"
    "v_exp_f32_e64 v240, v240 clamp\n"
    "v_exp_f32_e64 v242, v242 clamp\n"
    "v_exp_f32_e64 v237, v237 clamp\n"
    "v_exp_f32_e64 v239, v239 clamp\n"
    "v_exp_f32_e64 v241, v241 clamp\n"
    "buffer_load_dwordx4 v252, s[24:27], s56 offen offset:1024 lds\n"
    "v_exp_f32_e64 v243, v243 clamp\n"
    "v_pk_add_f32 v[168:169], v[168:169], v[236:237]\n"
    "v_pk_add_f32 v[170:171], v[170:171], v[238:239]\n"
    "v_pk_add_f32 v[172:173], v[172:173], v[240:241]\n"
    "v_pk_add_f32 v[174:175], v[174:175], v[242:243]\n"
    "v_cvt_pk_bf16_f32 v88, v168, v169\n"
    "v_cvt_pk_bf16_f32 v89, v170, v171\n"
    "v_cvt_pk_bf16_f32 v92, v172, v173\n"
    "v_cvt_pk_bf16_f32 v93, v174, v175\n"
    "v_pk_fma_f32 v[236:237], v[176:177], s[8:9], s[8:9] op_sel:[0,0,1] op_sel_hi:[1,0,1]\n"
    "v_pk_fma_f32 v[238:239], v[178:179], s[8:9], s[8:9] op_sel:[0,0,1] op_sel_hi:[1,0,1]\n"
    "s_add_u32 m0, s78, 64512\n"
    "s_add_u32 s56, s68, 73728\n"
    "buffer_load_dwordx4 v251, s[24:27], s56 offen offset:0 lds\n"
    "v_pk_fma_f32 v[240:241], v[180:181], s[8:9], s[8:9] op_sel:[0,0,1] op_sel_hi:[1,0,1]\n"
    "v_pk_fma_f32 v[242:243], v[182:183], s[8:9], s[8:9] op_sel:[0,0,1] op_sel_hi:[1,0,1]\n"
    "v_max_f32 v176, s81, v176\n"
    "v_max_f32 v178, s81, v178\n"
    "v_max_f32 v180, s81, v180\n"
    "v_max_f32 v182, s81, v182\n"
    "v_max_f32 v177, s81, v177\n"
    "v_max_f32 v179, s81, v179\n"
    "v_max_f32 v181, s81, v181\n"
    "v_max_f32 v183, s81, v183\n"
    "v_exp_f32_e64 v236, v236 clamp\n"
    "buffer_load_dwordx4 v252, s[24:27], s56 offen offset:1024 lds\n"
    "v_exp_f32_e64 v238, v238 clamp\n"
    "v_exp_f32_e64 v240, v240 clamp\n"
    "v_exp_f32_e64 v242, v242 clamp\n"
    "v_exp_f32_e64 v237, v237 clamp\n"
    "v_exp_f32_e64 v239, v239 clamp\n"
    "v_exp_f32_e64 v241, v241 clamp\n"
    "v_exp_f32_e64 v243, v243 clamp\n"
    "v_pk_add_f32 v[176:177], v[176:177], v[236:237]\n"
    "v_pk_add_f32 v[178:179], v[178:179], v[238:239]\n"
    "v_pk_add_f32 v[180:181], v[180:181], v[240:241]\n"
    "v_pk_add_f32 v[182:183], v[182:183], v[242:243]\n"
    "s_add_u32 m0, s79, 64512\n"
    "s_nop 0\n"
    "buffer_load_dwordx4 v253, s[24:27], s56 offen offset:0 lds\n"
    "v_cvt_pk_bf16_f32 v96, v176, v177\n"
    "v_cvt_pk_bf16_f32 v97, v178, v179\n"
    "v_cvt_pk_bf16_f32 v100, v180, v181\n"
    "v_cvt_pk_bf16_f32 v101, v182, v183\n"
    "v_pk_fma_f32 v[236:237], v[184:185], s[8:9], s[8:9] op_sel:[0,0,1] op_sel_hi:[1,0,1]\n"
    "v_pk_fma_f32 v[238:239], v[186:187], s[8:9], s[8:9] op_sel:[0,0,1] op_sel_hi:[1,0,1]\n"
    "v_pk_fma_f32 v[240:241], v[188:189], s[8:9], s[8:9] op_sel:[0,0,1] op_sel_hi:[1,0,1]\n"
    "v_pk_fma_f32 v[242:243], v[190:191], s[8:9], s[8:9] op_sel:[0,0,1] op_sel_hi:[1,0,1]\n"
    "v_max_f32 v184, s81, v184\n"
    "v_max_f32 v186, s81, v186\n"
    "v_max_f32 v188, s81, v188\n"
    "s_add_u32 m0, s78, 73728\n"
    "s_add_u32 s56, s56, 36864\n"
    "buffer_load_dwordx4 v251, s[24:27], s56 offen offset:0 lds\n"
    "v_max_f32 v190, s81, v190\n"
    "v_max_f32 v185, s81, v185\n"
    "v_max_f32 v187, s81, v187\n"
    "v_max_f32 v189, s81, v189\n"
    "v_max_f32 v191, s81, v191\n"
    "v_exp_f32_e64 v236, v236 clamp\n"
    "v_exp_f32_e64 v238, v238 clamp\n"
    "v_exp_f32_e64 v240, v240 clamp\n"
    "v_exp_f32_e64 v242, v242 clamp\n"
    "v_exp_f32_e64 v237, v237 clamp\n"
    "v_exp_f32_e64 v239, v239 clamp\n"
    "buffer_load_dwordx4 v252, s[24:27], s56 offen offset:1024 lds\n"
    "v_exp_f32_e64 v241, v241 clamp\n"
    "v_exp_f32_e64 v243, v243 clamp\n"
    "v_pk_add_f32 v[184:185], v[184:185], v[236:237]\n"
    "v_pk_add_f32 v[186:187], v[186:187], v[238:239]\n"
    "v_pk_add_f32 v[188:189], v[188:189], v[240:241]\n"
    "v_pk_add_f32 v[190:191], v[190:191], v[242:243]\n"
    "v_cvt_pk_bf16_f32 v104, v184, v185\n"
    "v_cvt_pk_bf16_f32 v105, v186, v187\n"
    "v_cvt_pk_bf16_f32 v108, v188, v189\n"
    "v_cvt_pk_bf16_f32 v109, v190, v191\n"
    "v_pk_fma_f32 v[236:237], v[192:193], s[8:9], s[8:9] op_sel:[0,0,1] op_sel_hi:[1,0,1]\n"
    "s_add_u32 m0, s78, 82944\n"
    "s_add_u32 s56, s69, 73728\n"
    "buffer_load_dwordx4 v251, s[24:27], s56 offen offset:0 lds\n"
    "v_pk_fma_f32 v[238:239], v[194:195], s[8:9], s[8:9] op_sel:[0,0,1] op_sel_hi:[1,0,1]\n"
    "v_pk_fma_f32 v[240:241], v[196:197], s[8:9], s[8:9] op_sel:[0,0,1] op_sel_hi:[1,0,1]\n"
    "v_pk_fma_f32 v[242:243], v[198:199], s[8:9], s[8:9] op_sel:[0,0,1] op_sel_hi:[1,0,1]\n"
    "v_max_f32 v192, s81, v192\n"
    "v_max_f32 v194, s81, v194\n"
    "v_max_f32 v196, s81, v196\n"
    "v_max_f32 v198, s81, v198\n"
    "v_max_f32 v193, s81, v193\n"
    "v_max_f32 v195, s81, v195\n"
    "v_max_f32 v197, s81, v197\n"
    "v_max_f32 v199, s81, v199\n"
    "buffer_load_dwordx4 v252, s[24:27], s56 offen offset:1024 lds\n"
    "v_exp_f32_e64 v236, v236 clamp\n"
    "v_exp_f32_e64 v238, v238 clamp\n"
    "v_exp_f32_e64 v240, v240 clamp\n"
    "v_exp_f32_e64 v242, v242 clamp\n"
    "v_exp_f32_e64 v237, v237 clamp\n"
    "v_exp_f32_e64 v239, v239 clamp\n"
    "v_exp_f32_e64 v241, v241 clamp\n"
    "v_exp_f32_e64 v243, v243 clamp\n"
    "v_pk_add_f32 v[192:193], v[192:193], v[236:237]\n"
    "v_pk_add_f32 v[194:195], v[194:195], v[238:239]\n"
    "v_pk_add_f32 v[196:197], v[196:197], v[240:241]\n"
    "s_add_u32 m0, s79, 82944\n"
    "s_nop 0\n"
    "buffer_load_dwordx4 v253, s[24:27], s56 offen offset:0 lds\n"
    "v_pk_add_f32 v[198:199], v[198:199], v[242:243]\n"
    "v_cvt_pk_bf16_f32 v112, v192, v193\n"
    "v_cvt_pk_bf16_f32 v113, v194, v195\n"
    "v_cvt_pk_bf16_f32 v116, v196, v197\n"
    "v_cvt_pk_bf16_f32 v117, v198, v199\n"
    "v_pk_fma_f32 v[236:237], v[200:201], s[8:9], s[8:9] op_sel:[0,0,1] op_sel_hi:[1,0,1]\n"
    "v_pk_fma_f32 v[238:239], v[202:203], s[8:9], s[8:9] op_sel:[0,0,1] op_sel_hi:[1,0,1]\n"
    "v_pk_fma_f32 v[240:241], v[204:205], s[8:9], s[8:9] op_sel:[0,0,1] op_sel_hi:[1,0,1]\n"
    "v_pk_fma_f32 v[242:243], v[206:207], s[8:9], s[8:9] op_sel:[0,0,1] op_sel_hi:[1,0,1]\n"
    "v_max_f32 v200, s81, v200\n"
    "v_max_f32 v202, s81, v202\n"
    "s_add_u32 m0, s78, 92160\n"
    "s_add_u32 s56, s56, 36864\n"
    "buffer_load_dwordx4 v251, s[24:27], s56 offen offset:0 lds\n"
    "v_max_f32 v204, s81, v204\n"
    "v_max_f32 v206, s81, v206\n"
    "v_max_f32 v201, s81, v201\n"
    "v_max_f32 v203, s81, v203\n"
    "v_max_f32 v205, s81, v205\n"
    "v_max_f32 v207, s81, v207\n"
    "v_exp_f32_e64 v236, v236 clamp\n"
    "v_exp_f32_e64 v238, v238 clamp\n"
    "v_exp_f32_e64 v240, v240 clamp\n"
    "v_exp_f32_e64 v242, v242 clamp\n"
    "v_exp_f32_e64 v237, v237 clamp\n"
    "buffer_load_dwordx4 v252, s[24:27], s56 offen offset:1024 lds\n"
    "v_exp_f32_e64 v239, v239 clamp\n"
    "v_exp_f32_e64 v241, v241 clamp\n"
    "v_exp_f32_e64 v243, v243 clamp\n"
    "v_pk_add_f32 v[200:201], v[200:201], v[236:237]\n"
    "v_pk_add_f32 v[202:203], v[202:203], v[238:239]\n"
    "v_pk_add_f32 v[204:205], v[204:205], v[240:241]\n"
    "v_pk_add_f32 v[206:207], v[206:207], v[242:243]\n"
    "v_cvt_pk_bf16_f32 v120, v200, v201\n"
    "v_cvt_pk_bf16_f32 v121, v202, v203\n"
    "v_cvt_pk_bf16_f32 v124, v204, v205\n"
    "v_cvt_pk_bf16_f32 v125, v206, v207\n"
    "s_add_u32 s80, s39, 0\n"
    "buffer_load_dwordx4 v[0:3], v254, s[20:23], s80 offen offset:0 sc1\n"
    "buffer_load_dwordx4 v[32:35], v254, s[20:23], s80 offen offset:1024 sc1\n"
    "buffer_load_dwordx4 v[4:7], v254, s[20:23], s80 offen offset:2048 sc1\n"
    "buffer_load_dwordx4 v[36:39], v254, s[20:23], s80 offen offset:3072 sc1\n"
    "s_add_u32 s80, s39, 4096\n"
    "buffer_load_dwordx4 v[8:11], v254, s[20:23], s80 offen offset:0 sc1\n"
    "buffer_load_dwordx4 v[40:43], v254, s[20:23], s80 offen offset:1024 sc1\n"
    "buffer_load_dwordx4 v[12:15], v254, s[20:23], s80 offen offset:2048 sc1\n"
    "buffer_load_dwordx4 v[44:47], v254, s[20:23], s80 offen offset:3072 sc1\n"
    "s_add_u32 s80, s39, 8192\n"
    "buffer_load_dwordx4 v[16:19], v254, s[20:23], s80 offen offset:0 sc1\n"
    "buffer_load_dwordx4 v[48:51], v254, s[20:23], s80 offen offset:1024 sc1\n"
    "buffer_load_dwordx4 v[20:23], v254, s[20:23], s80 offen offset:2048 sc1\n"
    "buffer_load_dwordx4 v[52:55], v254, s[20:23], s80 offen offset:3072 sc1\n"
    "s_add_u32 s80, s39, 12288\n"
    "buffer_load_dwordx4 v[24:27], v254, s[20:23], s80 offen offset:0 sc1\n"
    "buffer_load_dwordx4 v[56:59], v254, s[20:23], s80 offen offset:1024 sc1\n"
    "buffer_load_dwordx4 v[28:31], v254, s[20:23], s80 offen offset:2048 sc1\n"
    "buffer_load_dwordx4 v[60:63], v254, s[20:23], s80 offen offset:3072 sc1\n"
    "s_waitcnt vmcnt(41)\n"
    "v_cmp_ne_u32 vcc, s66, v248\n"
    "s_nop 1\n"
    "s_cbranch_vccnz RT7_%=\n"
    "RB7_%=:\n"
    "s_lshl_b32 s82, s7, 9\n"
    "s_add_u32 s82, s82, 156672\n"
    "v_lshlrev_b32 v224, 3, v255\n"
    "v_add_u32 v228, s82, v224\n"
    "v_add_u32 v230, 156672, v224\n"
    "s_mov_b32 s82, 159232\n"
    "s_cmp_eq_u32 s7, 2\n"
    "s_cselect_b32 s82, 159240, s82\n"
    "s_cmp_eq_u32 s7, 3\n"
    "s_cselect_b32 s82, 160256, s82\n"
    "s_cmp_eq_u32 s7, 1\n"
    "s_cselect_b32 s82, 158720, s82\n"
    "s_cselect_b32 s83, 3, 4\n"
    "v_lshlrev_b32 v229, s83, v255\n"
    "v_add_u32 v229, s82, v229\n"
    "v_and_b32 v231, 0x300, v254\n"
    "v_add_u32 v231, 159232, v231\n"
    "v_add_u32 v224, 159232, v254\n"
    "v_mov_b32 v225, 161280\n"
    "v_cndmask_b32 v232, v225, v224, s[12:13]\n"
    "v_cndmask_b32 v233, v225, v224, s[14:15]\n"
    "v_cndmask_b32 v234, v225, v224, s[16:17]\n"
    "v_cndmask_b32 v235, v225, v224, s[18:19]\n"
    "v_mov_b32 v236, 0\n"
    "v_mov_b32 v237, 0\n"
    "v_mov_b32 v238, 0\n"
    "v_mov_b32 v239, 0\n"
    "ds_write_b128 v225, v[236:239]\n"
    "s_mov_b32 s82, 0x0c04000c\n"
    "s_mov_b32 s83, 0x04020100\n"
    "s_add_u32 s57, s71, 0\n"
    "buffer_load_dwordx4 v[144:147], v254, s[20:23], s57 offen offset:0 sc1\n"
    "buffer_load_dwordx4 v[148:151], v254, s[20:23], s57 offen offset:1024 sc1\n"
    "buffer_load_dwordx4 v[152:155], v254, s[20:23], s57 offen offset:2048 sc1\n"
    "buffer_load_dwordx4 v[156:159], v254, s[20:23], s57 offen offset:3072 sc1\n"
    "s_add_u32 s57, s71, 4096\n"
    "buffer_load_dwordx4 v[160:163], v254, s[20:23], s57 offen offset:0 sc1\n"
    "buffer_load_dwordx4 v[164:167], v254, s[20:23], s57 offen offset:1024 sc1\n"
    "buffer_load_dwordx4 v[168:171], v254, s[20:23], s57 offen offset:2048 sc1\n"
    "buffer_load_dwordx4 v[172:175], v254, s[20:23], s57 offen offset:3072 sc1\n"
    "s_add_u32 s57, s72, 0\n"
    "buffer_load_dwordx4 v[176:179], v254, s[20:23], s57 offen offset:0 sc1\n"
    "buffer_load_dwordx4 v[180:183], v254, s[20:23], s57 offen offset:1024 sc1\n"
    "buffer_load_dwordx4 v[184:187], v254, s[20:23], s57 offen offset:2048 sc1\n"
    "buffer_load_dwordx4 v[188:191], v254, s[20:23], s57 offen offset:3072 sc1\n"
    "s_add_u32 s57, s72, 4096\n"
    "buffer_load_dwordx4 v[192:195], v254, s[20:23], s57 offen offset:0 sc1\n"
    "buffer_load_dwordx4 v[196:199], v254, s[20:23], s57 offen offset:1024 sc1\n"
    "buffer_load_dwordx4 v[200:203], v254, s[20:23], s57 offen offset:2048 sc1\n"
    "buffer_load_dwordx4 v[204:207], v254, s[20:23], s57 offen offset:3072 sc1\n"
    "s_waitcnt vmcnt(16)\n"
    "v_xor_b32 v224, s66, v3\n"
    "v_bitop3_b32 v224, v224, v7, s66 bitop3:0xf6\n"
    "v_bitop3_b32 v224, v224, v11, s66 bitop3:0xf6\n"
    "v_bitop3_b32 v224, v224, v15, s66 bitop3:0xf6\n"
    "v_bitop3_b32 v224, v224, v19, s66 bitop3:0xf6\n"
    "v_bitop3_b32 v224, v224, v23, s66 bitop3:0xf6\n"
    "v_bitop3_b32 v224, v224, v27, s66 bitop3:0xf6\n"
    "v_bitop3_b32 v224, v224, v31, s66 bitop3:0xf6\n"
    "v_bitop3_b32 v224, v224, v35, s66 bitop3:0xf6\n"
    "v_bitop3_b32 v224, v224, v39, s66 bitop3:0xf6\n"
    "v_bitop3_b32 v224, v224, v43, s66 bitop3:0xf6\n"
    "v_bitop3_b32 v224, v224, v47, s66 bitop3:0xf6\n"
    "v_bitop3_b32 v224, v224, v51, s66 bitop3:0xf6\n"
    "v_bitop3_b32 v224, v224, v55, s66 bitop3:0xf6\n"
    "v_bitop3_b32 v224, v224, v59, s66 bitop3:0xf6\n"
    "v_bitop3_b32 v224, v224, v63, s66 bitop3:0xf6\n"
    "v_cmp_ne_u32 vcc, 0, v224\n"
    "s_nop 1\n"
    "s_cbranch_vccnz RT8_%=\n"
    "RB8_%=:\n"
    "v_perm_b32 v3, v1, v0, s82\n"
    "v_perm_b32 v3, v2, v3, s83\n"
    "v_perm_b32 v7, v5, v4, s82\n"
    "v_perm_b32 v7, v6, v7, s83\n"
    "v_perm_b32 v11, v9, v8, s82\n"
    "v_perm_b32 v11, v10, v11, s83\n"
    "v_perm_b32 v15, v13, v12, s82\n"
    "v_perm_b32 v15, v14, v15, s83\n"
    "v_perm_b32 v19, v17, v16, s82\n"
    "v_perm_b32 v19, v18, v19, s83\n"
    "v_perm_b32 v23, v21, v20, s82\n"
    "v_perm_b32 v23, v22, v23, s83\n"
    "v_perm_b32 v27, v25, v24, s82\n"
    "v_perm_b32 v27, v26, v27, s83\n"
    "v_perm_b32 v31, v29, v28, s82\n"
    "v_perm_b32 v31, v30, v31, s83\n"
    "v_perm_b32 v35, v33, v32, s82\n"
    "v_perm_b32 v35, v34, v35, s83\n"
    "v_perm_b32 v39, v37, v36, s82\n"
    "v_perm_b32 v39, v38, v39, s83\n"
    "v_perm_b32 v43, v41, v40, s82\n"
    "v_perm_b32 v43, v42, v43, s83\n"
    "v_perm_b32 v47, v45, v44, s82\n"
    "v_perm_b32 v47, v46, v47, s83\n"
    "v_perm_b32 v51, v49, v48, s82\n"
    "v_perm_b32 v51, v50, v51, s83\n"
    "v_perm_b32 v55, v53, v52, s82\n"
    "v_perm_b32 v55, v54, v55, s83\n"
    "v_perm_b32 v59, v57, v56, s82\n"
    "v_perm_b32 v59, v58, v59, s83\n"
    "v_perm_b32 v63, v61, v60, s82\n"
    "v_perm_b32 v63, v62, v63, s83\n"
    "v_pk_add_f32 v[0:1], v[0:1], v[16:17]\n"
    "v_pk_add_f32 v[2:3], v[2:3], v[18:19]\n"
    "v_pk_add_f32 v[4:5], v[4:5], v[20:21]\n"
    "v_pk_add_f32 v[6:7], v[6:7], v[22:23]\n"
    "v_pk_add_f32 v[8:9], v[8:9], v[24:25]\n"
    "v_pk_add_f32 v[10:11], v[10:11], v[26:27]\n"
    "v_pk_add_f32 v[12:13], v[12:13], v[28:29]\n"
    "v_pk_add_f32 v[14:15], v[14:15], v[30:31]\n"
    "v_pk_add_f32 v[32:33], v[32:33], v[48:49]\n"
    "v_pk_add_f32 v[34:35], v[34:35], v[50:51]\n"
    "v_pk_add_f32 v[36:37], v[36:37], v[52:53]\n"
    "v_pk_add_f32 v[38:39], v[38:39], v[54:55]\n"
    "v_pk_add_f32 v[40:41], v[40:41], v[56:57]\n"
    "v_pk_add_f32 v[42:43], v[42:43], v[58:59]\n"
    "v_pk_add_f32 v[44:45], v[44:45], v[60:61]\n"
    "v_pk_add_f32 v[46:47], v[46:47], v[62:63]\n"
    "v_pk_add_f32 v[0:1], v[0:1], v[8:9]\n"
    "v_pk_add_f32 v[2:3], v[2:3], v[10:11]\n"
    "v_pk_add_f32 v[4:5], v[4:5], v[12:13]\n"
    "v_pk_add_f32 v[6:7], v[6:7], v[14:15]\n"
    "v_pk_add_f32 v[32:33], v[32:33], v[40:41]\n"
    "v_pk_add_f32 v[34:35], v[34:35], v[42:43]\n"
    "v_pk_add_f32 v[36:37], v[36:37], v[44:45]\n"
    "v_pk_add_f32 v[38:39], v[38:39], v[46:47]\n"
    "v_pk_add_f32 v[0:1], v[0:1], v[4:5]\n"
    "v_pk_add_f32 v[2:3], v[2:3], v[6:7]\n"
    "v_pk_add_f32 v[32:33], v[32:33], v[36:37]\n"
    "v_pk_add_f32 v[34:35], v[34:35], v[38:39]\n"
    "v_cvt_pk_bf16_f32 v0, v0, v1\n"
    "v_cvt_pk_bf16_f32 v1, v2, v3\n"
    "v_cvt_pk_bf16_f32 v32, v32, v33\n"
    "v_cvt_pk_bf16_f32 v33, v34, v35\n"
    "ds_write_b64 v228, v[0:1]\n"
    "ds_write_b64 v229, v[32:33]\n"
    "s_waitcnt lgkmcnt(0)\n"
    "s_barrier\n"
    "ds_read_b128 a[208:211], v231 offset:0\n"
    "ds_read_b128 a[212:215], v231 offset:32\n"
    "ds_read_b128 a[216:219], v231 offset:64\n"
    "ds_read_b128 a[220:223], v231 offset:96\n"
    "ds_read_b64 a[176:177], v230 offset:0\n"
    "ds_read_b64 a[178:179], v230 offset:2048\n"
    "ds_read_b64 a[180:181], v230 offset:512\n"
    "ds_read_b64 a[182:183], v230 offset:2048\n"
    "ds_read_b64 a[184:185], v230 offset:1024\n"
    "ds_read_b64 a[186:187], v230 offset:2048\n"
    "ds_read_b64 a[188:189], v230 offset:1536\n"
    "ds_read_b64 a[190:191], v230 offset:2048\n"
    "ds_read_b128 a[192:195], v232\n"
    "ds_read_b128 a[196:199], v233\n"
    "ds_read_b128 a[200:203], v234\n"
    "ds_read_b128 a[204:207], v235\n"
    "s_mov_b32 s10, 0\n"
    "s_mov_b32 s11, 0xffff\n"
    "s_mov_b32 s12, 0\n"
    "s_mov_b32 s13, 0xffff0000\n"
    "s_add_u32 s84, s74, 4096\n"
    "s_add_u32 s85, s74, 8192\n"
    "s_waitcnt lgkmcnt(15)\n"
    "v_mfma_f32_16x16x32_bf16 v[16:19], a[208:211], v[64:67], 0\n"
    "s_waitcnt lgkmcnt(14)\n"
    "v_mfma_f32_16x16x32_bf16 v[20:23], a[212:215], v[80:83], 0\n"
    "s_waitcnt lgkmcnt(13)\n"
    "v_mfma_f32_16x16x32_bf16 v[24:27], a[216:219], v[96:99], 0\n"
    "s_waitcnt lgkmcnt(12)\n"
    "v_mfma_f32_16x16x32_bf16 v[28:31], a[220:223], v[112:115], 0\n"
    "s_waitcnt lgkmcnt(10)\n"
    "v_mfma_f32_16x16x32_bf16 v[0:3], a[176:179], v[64:67], 0\n"
    "s_waitcnt lgkmcnt(8)\n"
    "v_mfma_f32_16x16x32_bf16 v[4:7], a[180:183], v[80:83], 0\n"
    "s_waitcnt lgkmcnt(6)\n"
    "v_mfma_f32_16x16x32_bf16 v[8:11], a[184:187], v[96:99], 0\n"
    "s_waitcnt lgkmcnt(4)\n"
    "v_mfma_f32_16x16x32_bf16 v[12:15], a[188:191], v[112:115], 0\n"
    "s_waitcnt lgkmcnt(3)\n"
    "v_mfma_f32_16x16x32_bf16 v[128:131], a[192:195], v[64:67], 0\n"
    "s_waitcnt lgkmcnt(2)\n"
    "v_mfma_f32_16x16x32_bf16 v[128:131], a[196:199], v[80:83], v[128:131]\n"
    "s_waitcnt lgkmcnt(1)\n"
    "v_mfma_f32_16x16x32_bf16 v[128:131], a[200:203], v[96:99], v[128:131]\n"
    "s_waitcnt lgkmcnt(0)\n"
    "v_mfma_f32_16x16x32_bf16 v[128:131], a[204:207], v[112:115], v[128:131]\n"
    "s_add_u32 m0, s78, 101376\n"
    "s_add_u32 s56, s70, 73728\n"
    "buffer_load_dwordx4 v251, s[24:27], s56 offen offset:0 lds\n"
    "v_mfma_f32_16x16x32_bf16 v[48:51], a[208:211], v[68:71], 0\n"
    "v_add_f32 v16, s81, v16\n"
    "v_add_f32 v20, s81, v20\n"
    "v_add_f32 v24, s81, v24\n"
    "v_mfma_f32_16x16x32_bf16 v[52:55], a[212:215], v[84:87], 0\n"
    "v_add_f32 v28, s81, v28\n"
    "v_rcp_f32 v16, v16\n"
    "v_rcp_f32 v20, v20\n"
    "v_rcp_f32 v24, v24\n"
    "v_mfma_f32_16x16x32_bf16 v[56:59], a[216:219], v[100:103], 0\n"
    "v_rcp_f32 v28, v28\n"
    "v_mul_f32 v16, v211, v16\n"
    "v_mul_f32 v20, v211, v20\n"
    "v_mul_f32 v24, v211, v24\n"
    "v_mfma_f32_16x16x32_bf16 v[60:63], a[220:223], v[116:119], 0\n"
    "v_mul_f32 v28, v211, v28\n"
    "buffer_load_dwordx4 v252, s[24:27], s56 offen offset:1024 lds\n"
    "v_cndmask_b32 v17, 0, v16, s[10:11]\n"
    "v_cndmask_b32 v18, 0, v20, s[10:11]\n"
    "v_mfma_f32_16x16x32_bf16 v[32:35], a[176:179], v[68:71], 0\n"
    "v_cndmask_b32 v17, v17, v24, s[12:13]\n"
    "v_cndmask_b32 v18, v18, v28, s[12:13]\n"
    "v_mul_f32 v0, v16, v0\n"
    "v_mul_f32 v1, v16, v1\n"
    "v_mfma_f32_16x16x32_bf16 v[36:39], a[180:183], v[84:87], 0\n"
    "v_mul_f32 v2, v16, v2\n"
    "v_mul_f32 v3, v16, v3\n"
    "v_mul_f32 v4, v20, v4\n"
    "v_mul_f32 v5, v20, v5\n"
    "v_mfma_f32_16x16x32_bf16 v[40:43], a[184:187], v[100:103], 0\n"
    "v_mul_f32 v6, v20, v6\n"
    "v_mul_f32 v7, v20, v7\n"
    "v_mul_f32 v8, v24, v8\n"
    "s_add_u32 m0, s79, 101376\n"
    "s_nop 0\n"
    "buffer_load_dwordx4 v253, s[24:27], s56 offen offset:0 lds\n"
    "v_mfma_f32_16x16x32_bf16 v[44:47], a[188:191], v[116:119], 0\n"
    "v_mul_f32 v9, v24, v9\n"
    "v_mul_f32 v10, v24, v10\n"
    "v_mul_f32 v11, v24, v11\n"
    "v_mul_f32 v12, v28, v12\n"
    "v_mfma_f32_16x16x32_bf16 v[132:135], a[192:195], v[68:71], 0\n"
    "v_mul_f32 v13, v28, v13\n"
    "v_mul_f32 v14, v28, v14\n"
    "v_mul_f32 v15, v28, v15\n"
    "v_mul_f32 v128, v17, v128\n"
    "v_mfma_f32_16x16x32_bf16 v[132:135], a[196:199], v[84:87], v[132:135]\n"
    "v_mul_f32 v129, v17, v129\n"
    "v_mul_f32 v130, v18, v130\n"
    "v_mul_f32 v131, v18, v131\n"
    "v_mfma_f32_16x16x32_bf16 v[132:135], a[200:203], v[100:103], v[132:135]\n"
    "v_cvt_pk_bf16_f32 v0, v0, v1\n"
    "s_add_u32 m0, s78, 110592\n"
    "s_add_u32 s56, s56, 36864\n"
    "buffer_load_dwordx4 v251, s[24:27], s56 offen offset:0 lds\n"
    "v_cvt_pk_bf16_f32 v1, v2, v3\n"
    "v_cvt_pk_bf16_f32 v2, v4, v5\n"
    "v_cvt_pk_bf16_f32 v3, v6, v7\n"
    "v_mfma_f32_16x16x32_bf16 v[132:135], a[204:207], v[116:119], v[132:135]\n"
    "buffer_store_dwordx4 v[0:3], v254, s[20:23], s74 offen offset:0 sc1\n"
    "v_cvt_pk_bf16_f32 v8, v8, v9\n"
    "v_cvt_pk_bf16_f32 v9, v10, v11\n"
    "v_cvt_pk_bf16_f32 v10, v12, v13\n"
    "v_cvt_pk_bf16_f32 v11, v14, v15\n"
    "buffer_store_dwordx4 v[8:11], v254, s[20:23], s84 offen offset:0 sc1\n"
    "v_cvt_pk_bf16_f32 v128, v128, v129\n"
    "v_cvt_pk_bf16_f32 v129, v130, v131\n"
    "v_mov_b32 v130, 0\n"
    "v_mov_b32 v131, 0\n"
    "buffer_store_dwordx4 v[128:131], v254, s[20:23], s85 offen offset:0 sc1\n"
    "v_mfma_f32_16x16x32_bf16 v[16:19], a[208:211], v[72:75], 0\n"
    "buffer_load_dwordx4 v252, s[24:27], s56 offen offset:1024 lds\n"
    "v_add_f32 v48, s81, v48\n"
    "v_add_f32 v52, s81, v52\n"
    "v_add_f32 v56, s81, v56\n"
    "v_mfma_f32_16x16x32_bf16 v[20:23], a[212:215], v[88:91], 0\n"
    "v_add_f32 v60, s81, v60\n"
    "v_rcp_f32 v48, v48\n"
    "v_rcp_f32 v52, v52\n"
    "v_rcp_f32 v56, v56\n"
    "v_mfma_f32_16x16x32_bf16 v[24:27], a[216:219], v[104:107], 0\n"
    "v_rcp_f32 v60, v60\n"
    "v_mul_f32 v48, v215, v48\n"
    "v_mul_f32 v52, v215, v52\n"
    "v_mul_f32 v56, v215, v56\n"
    "v_mfma_f32_16x16x32_bf16 v[28:31], a[220:223], v[120:123], 0\n"
    "v_mul_f32 v60, v215, v60\n"
    "v_cndmask_b32 v49, 0, v48, s[10:11]\n"
    "s_add_u32 m0, s78, 119808\n"
    "s_add_u32 s56, s68, 0\n"
    "buffer_load_dwordx4 v251, s[24:27], s56 offen offset:0 lds\n"
    "v_cndmask_b32 v50, 0, v52, s[10:11]\n"
    "v_mfma_f32_16x16x32_bf16 v[0:3], a[176:179], v[72:75], 0\n"
    "v_cndmask_b32 v49, v49, v56, s[12:13]\n"
    "v_cndmask_b32 v50, v50, v60, s[12:13]\n"
    "v_mul_f32 v32, v48, v32\n"
    "v_mul_f32 v33, v48, v33\n"
    "v_mfma_f32_16x16x32_bf16 v[4:7], a[180:183], v[88:91], 0\n"
    "v_mul_f32 v34, v48, v34\n"
    "v_mul_f32 v35, v48, v35\n"
    "v_mul_f32 v36, v52, v36\n"
    "v_mul_f32 v37, v52, v37\n"
    "v_mfma_f32_16x16x32_bf16 v[8:11], a[184:187], v[104:107], 0\n"
    "v_mul_f32 v38, v52, v38\n"
    "v_mul_f32 v39, v52, v39\n"
    "v_mul_f32 v40, v56, v40\n"
    "v_mfma_f32_16x16x32_bf16 v[12:15], a[188:191], v[120:123], 0\n"
    "buffer_load_dwordx4 v252, s[24:27], s56 offen offset:1024 lds\n"
    "v_mul_f32 v41, v56, v41\n"
    "v_mul_f32 v42, v56, v42\n"
    "v_mul_f32 v43, v56, v43\n"
    "v_mul_f32 v44, v60, v44\n"
    "v_mfma_f32_16x16x32_bf16 v[128:131], a[192:195], v[72:75], 0\n"
    "v_mul_f32 v45, v60, v45\n"
    "v_mul_f32 v46, v60, v46\n"
    "v_mul_f32 v47, v60, v47\n"
    "v_mul_f32 v132, v49, v132\n"
    "v_mfma_f32_16x16x32_bf16 v[128:131], a[196:199], v[88:91], v[128:131]\n"
    "v_mul_f32 v133, v49, v133\n"
    "v_mul_f32 v134, v50, v134\n"
    "v_mul_f32 v135, v50, v135\n"
    "v_mfma_f32_16x16x32_bf16 v[128:131], a[200:203], v[104:107], v[128:131]\n"
    "v_cvt_pk_bf16_f32 v32, v32, v33\n"
    "v_cvt_pk_bf16_f32 v33, v34, v35\n"
    "s_add_u32 m0, s79, 119808\n"
    "s_nop 0\n"
    "buffer_load_dwordx4 v253, s[24:27], s56 offen offset:0 lds\n"
    "v_cvt_pk_bf16_f32 v34, v36, v37\n"
    "v_cvt_pk_bf16_f32 v35, v38, v39\n"
    "v_mfma_f32_16x16x32_bf16 v[128:131], a[204:207], v[120:123], v[128:131]\n"
    "buffer_store_dwordx4 v[32:35], v254, s[20:23], s74 offen offset:1024 sc1\n"
    "v_cvt_pk_bf16_f32 v40, v40, v41\n"
    "v_cvt_pk_bf16_f32 v41, v42, v43\n"
    "v_cvt_pk_bf16_f32 v42, v44, v45\n"
    "v_cvt_pk_bf16_f32 v43, v46, v47\n"
    "buffer_store_dwordx4 v[40:43], v254, s[20:23], s84 offen offset:1024 sc1\n"
    "v_cvt_pk_bf16_f32 v132, v132, v133\n"
    "v_cvt_pk_bf16_f32 v133, v134, v135\n"
    "v_mov_b32 v134, 0\n"
    "v_mov_b32 v135, 0\n"
    "buffer_store_dwordx4 v[132:135], v254, s[20:23], s85 offen offset:1024 sc1\n"
    "v_mfma_f32_16x16x32_bf16 v[48:51], a[208:211], v[76:79], 0\n"
    "v_add_f32 v16, s81, v16\n"
    "s_add_u32 m0, s78, 129024\n"
    "s_add_u32 s56, s56, 36864\n"
    "buffer_load_dwordx4 v251, s[24:27], s56 offen offset:0 lds\n"
    "v_add_f32 v20, s81, v20\n"
    "v_add_f32 v24, s81, v24\n"
    "v_mfma_f32_16x16x32_bf16 v[52:55], a[212:215], v[92:95], 0\n"
    "v_add_f32 v28, s81, v28\n"
    "v_rcp_f32 v16, v16\n"
    "v_rcp_f32 v20, v20\n"
    "v_rcp_f32 v24, v24\n"
    "v_mfma_f32_16x16x32_bf16 v[56:59], a[216:219], v[108:111], 0\n"
    "v_rcp_f32 v28, v28\n"
    "v_mul_f32 v16, v219, v16\n"
    "v_mul_f32 v20, v219, v20\n"
    "v_mul_f32 v24, v219, v24\n"
    "v_mfma_f32_16x16x32_bf16 v[60:63], a[220:223], v[124:127], 0\n"
    "v_mul_f32 v28, v219, v28\n"
    "v_cndmask_b32 v17, 0, v16, s[10:11]\n"
    "v_cndmask_b32 v18, 0, v20, s[10:11]\n"
    "buffer_load_dwordx4 v252, s[24:27], s56 offen offset:1024 lds\n"
    "v_mfma_f32_16x16x32_bf16 v[32:35], a[176:179], v[76:79], 0\n"
    "v_cndmask_b32 v17, v17, v24, s[12:13]\n"
    "v_cndmask_b32 v18, v18, v28, s[12:13]\n"
    "v_mul_f32 v0, v16, v0\n"
    "v_mul_f32 v1, v16, v1\n"
    "v_mfma_f32_16x16x32_bf16 v[36:39], a[180:183], v[92:95], 0\n"
    "v_mul_f32 v2, v16, v2\n"
    "v_mul_f32 v3, v16, v3\n"
    "v_mul_f32 v4, v20, v4\n"
    "v_mul_f32 v5, v20, v5\n"
    "v_mfma_f32_16x16x32_bf16 v[40:43], a[184:187], v[108:111], 0\n"
    "v_mul_f32 v6, v20, v6\n"
    "v_mul_f32 v7, v20, v7\n"
    "v_mul_f32 v8, v24, v8\n"
    "v_mfma_f32_16x16x32_bf16 v[44:47], a[188:191], v[124:127], 0\n"
    "v_mul_f32 v9, v24, v9\n"
    "s_add_u32 m0, s78, 138240\n"
    "s_add_u32 s56, s69, 0\n"
    "buffer_load_dwordx4 v251, s[24:27], s56 offen offset:0 lds\n"
    "v_mul_f32 v10, v24, v10\n"
    "v_mul_f32 v11, v24, v11\n"
    "v_mul_f32 v12, v28, v12\n"
    "v_mfma_f32_16x16x32_bf16 v[132:135], a[192:195], v[76:79], 0\n"
    "v_mul_f32 v13, v28, v13\n"
    "v_mul_f32 v14, v28, v14\n"
    "v_mul_f32 v15, v28, v15\n"
    "v_mul_f32 v128, v17, v128\n"
    "v_mfma_f32_16x16x32_bf16 v[132:135], a[196:199], v[92:95], v[132:135]\n"
    "v_mul_f32 v129, v17, v129\n"
    "v_mul_f32 v130, v18, v130\n"
    "v_mul_f32 v131, v18, v131\n"
    "v_mfma_f32_16x16x32_bf16 v[132:135], a[200:203], v[108:111], v[132:135]\n"
    "v_cvt_pk_bf16_f32 v0, v0, v1\n"
    "v_cvt_pk_bf16_f32 v1, v2, v3\n"
    "v_cvt_pk_bf16_f32 v2, v4, v5\n"
    "buffer_load_dwordx4 v252, s[24:27], s56 offen offset:1024 lds\n"
    "v_cvt_pk_bf16_f32 v3, v6, v7\n"
    "v_mfma_f32_16x16x32_bf16 v[132:135], a[204:207], v[124:127], v[132:135]\n"
    "buffer_store_dwordx4 v[0:3], v254, s[20:23], s74 offen offset:2048 sc1\n"
    "v_cvt_pk_bf16_f32 v8, v8, v9\n"
    "v_cvt_pk_bf16_f32 v9, v10, v11\n"
    "v_cvt_pk_bf16_f32 v10, v12, v13\n"
    "v_cvt_pk_bf16_f32 v11, v14, v15\n"
    "buffer_store_dwordx4 v[8:11], v254, s[20:23], s84 offen offset:2048 sc1\n"
    "v_cvt_pk_bf16_f32 v128, v128, v129\n"
    "v_cvt_pk_bf16_f32 v129, v130, v131\n"
    "v_mov_b32 v130, 0\n"
    "v_mov_b32 v131, 0\n"
    "buffer_store_dwordx4 v[128:131], v254, s[20:23], s85 offen offset:2048 sc1\n"
    "v_add_f32 v48, s81, v48\n"
    "v_add_f32 v52, s81, v52\n"
    "v_add_f32 v56, s81, v56\n"
    "s_add_u32 m0, s79, 138240\n"
    "s_nop 0\n"
    "buffer_load_dwordx4 v253, s[24:27], s56 offen offset:0 lds\n"
    "v_add_f32 v60, s81, v60\n"
    "v_rcp_f32 v48, v48\n"
    "v_rcp_f32 v52, v52\n"
    "v_rcp_f32 v56, v56\n"
    "v_rcp_f32 v60, v60\n"
    "v_mul_f32 v48, v223, v48\n"
    "v_mul_f32 v52, v223, v52\n"
    "v_mul_f32 v56, v223, v56\n"
    "v_mul_f32 v60, v223, v60\n"
    "v_cndmask_b32 v49, 0, v48, s[10:11]\n"
    "v_cndmask_b32 v50, 0, v52, s[10:11]\n"
    "v_cndmask_b32 v49, v49, v56, s[12:13]\n"
    "v_cndmask_b32 v50, v50, v60, s[12:13]\n"
    "v_mul_f32 v32, v48, v32\n"
    "v_mul_f32 v33, v48, v33\n"
    "v_mul_f32 v34, v48, v34\n"
    "s_add_u32 m0, s78, 147456\n"
    "s_add_u32 s56, s56, 36864\n"
    "buffer_load_dwordx4 v251, s[24:27], s56 offen offset:0 lds\n"
    "v_mul_f32 v35, v48, v35\n"
    "v_mul_f32 v36, v52, v36\n"
    "v_mul_f32 v37, v52, v37\n"
    "v_mul_f32 v38, v52, v38\n"
    "v_mul_f32 v39, v52, v39\n"
    "v_mul_f32 v40, v56, v40\n"
    "v_mul_f32 v41, v56, v41\n"
    "v_mul_f32 v42, v56, v42\n"
    "v_mul_f32 v43, v56, v43\n"
    "v_mul_f32 v44, v60, v44\n"
    "v_mul_f32 v45, v60, v45\n"
    "v_mul_f32 v46, v60, v46\n"
    "v_mul_f32 v47, v60, v47\n"
    "v_mul_f32 v132, v49, v132\n"
    "v_mul_f32 v133, v49, v133\n"
    "v_mul_f32 v134, v50, v134\n"
    "buffer_load_dwordx4 v252, s[24:27], s56 offen offset:1024 lds\n"
    "v_mul_f32 v135, v50, v135\n"
    "v_cvt_pk_bf16_f32 v32, v32, v33\n"
    "v_cvt_pk_bf16_f32 v33, v34, v35\n"
    "v_cvt_pk_bf16_f32 v34, v36, v37\n"
    "v_cvt_pk_bf16_f32 v35, v38, v39\n"
    "buffer_store_dwordx4 v[32:35], v254, s[20:23], s74 offen offset:3072 sc1\n"
    "v_cvt_pk_bf16_f32 v40, v40, v41\n"
    "v_cvt_pk_bf16_f32 v41, v42, v43\n"
    "v_cvt_pk_bf16_f32 v42, v44, v45\n"
    "v_cvt_pk_bf16_f32 v43, v46, v47\n"
    "buffer_store_dwordx4 v[40:43], v254, s[20:23], s84 offen offset:3072 sc1\n"
    "v_cvt_pk_bf16_f32 v132, v132, v133\n"
    "v_cvt_pk_bf16_f32 v133, v134, v135\n"
    "v_mov_b32 v134, 0\n"
    "v_mov_b32 v135, 0\n"
    "buffer_store_dwordx4 v[132:135], v254, s[20:23], s85 offen offset:3072 sc1\n"
    "ds_read_b64_tr_b16 a[224:225], v244 offset:9216\n"
    "ds_read_b64_tr_b16 a[226:227], v244 offset:13824\n"
    "ds_read_b64_tr_b16 a[228:229], v244 offset:18432\n"
    "ds_read_b64_tr_b16 a[230:231], v244 offset:23040\n"
    "ds_read_b64_tr_b16 a[232:233], v244 offset:9248\n"
    "ds_read_b64_tr_b16 a[234:235], v244 offset:13856\n"
    "ds_read_b64_tr_b16 a[236:237], v244 offset:18464\n"
    "ds_read_b64_tr_b16 a[238:239], v244 offset:23072\n"
    "v_lshrrev_b32 v236, 4, v255\n"
    "v_lshlrev_b32 v236, 3, v236\n"
    "s_waitcnt vmcnt(42) lgkmcnt(6)\n"
    "v_mfma_f32_16x16x32_bf16 v[0:3], a[224:227], v[144:147], 0\n"
    "ds_read_b64_tr_b16 a[240:241], v244 offset:9280\n"
    "s_waitcnt vmcnt(38) lgkmcnt(5)\n"
    "v_mfma_f32_16x16x32_bf16 v[0:3], a[228:231], v[160:163], v[0:3]\n"
    "buffer_load_dword v248, v247, s[20:23], s77 offen offset:256 sc1\n"
    "s_waitcnt vmcnt(38)\n"
    "v_mfma_f32_16x16x32_bf16 v[4:7], a[228:231], v[164:167], 0\n"
    "s_add_u32 s57, s73, 0\n"
    "ds_read_b64_tr_b16 a[242:243], v244 offset:13888\n"
    "v_mfma_f32_16x16x32_bf16 v[4:7], a[224:227], v[148:151], v[4:7]\n"
    "s_and_b32 s84, %2, 0xf8\n"
    "v_mfma_f32_16x16x32_bf16 v[8:11], a[224:227], v[152:155], 0\n"
    "s_mul_i32 s84, s84, 81920\n"
    "ds_read_b64_tr_b16 a[244:245], v244 offset:18496\n"
    "s_waitcnt vmcnt(37)\n"
    "v_mfma_f32_16x16x32_bf16 v[8:11], a[228:231], v[168:171], v[8:11]\n"
    "s_add_u32 s84, s84, 29360128\n"
    "buffer_load_dwordx4 a[0:3], v254, s[20:23], s57 offen offset:0 sc1\n"
    "s_waitcnt vmcnt(37)\n"
    "v_mfma_f32_16x16x32_bf16 v[12:15], a[228:231], v[172:175], 0\n"
    "s_mul_i32 s85, s7, 17920\n"
    "ds_read_b64_tr_b16 a[246:247], v244 offset:23104\n"
    "v_mfma_f32_16x16x32_bf16 v[12:15], a[224:227], v[156:159], v[12:15]\n"
    "s_waitcnt lgkmcnt(4)\n"
    "v_mfma_f32_16x16x32_bf16 v[28:31], a[232:235], v[156:159], 0\n"
    "s_add_u32 s58, s84, s85\n"
    "ds_read_b64_tr_b16 a[248:249], v244 offset:9312\n"
    "v_mfma_f32_16x16x32_bf16 v[28:31], a[236:239], v[172:175], v[28:31]\n"
    "s_mul_i32 s85, s4, 81920\n"
    "buffer_load_dwordx4 a[4:7], v254, s[20:23], s57 offen offset:1024 sc1\n"
    "v_mfma_f32_16x16x32_bf16 v[24:27], a[236:239], v[168:171], 0\n"
    "s_add_u32 s59, s58, s85\n"
    "ds_read_b64_tr_b16 a[250:251], v244 offset:13920\n"
    "v_mfma_f32_16x16x32_bf16 v[24:27], a[232:235], v[152:155], v[24:27]\n"
    "s_add_u32 s84, s84, 71680\n"
    "v_mfma_f32_16x16x32_bf16 v[20:23], a[232:235], v[148:151], 0\n"
    "s_add_u32 s61, s84, s85\n"
    "ds_read_b64_tr_b16 a[252:253], v244 offset:18528\n"
    "v_mfma_f32_16x16x32_bf16 v[20:23], a[236:239], v[164:167], v[20:23]\n"
    "s_mul_i32 s85, s7, 163840\n"
    "buffer_load_dwordx4 a[8:11], v254, s[20:23], s57 offen offset:2048 sc1\n"
    "v_mfma_f32_16x16x32_bf16 v[16:19], a[236:239], v[160:163], 0\n"
    "s_add_u32 s84, s84, s85\n"
    "ds_read_b64_tr_b16 a[254:255], v244 offset:23136\n"
    "v_mfma_f32_16x16x32_bf16 v[16:19], a[232:235], v[144:147], v[16:19]\n"
    "s_waitcnt lgkmcnt(4)\n"
    "v_mfma_f32_16x16x32_bf16 v[32:35], a[240:243], v[144:147], 0\n"
    "s_mul_i32 s85, s4, 1280\n"
    "ds_read_b64_tr_b16 a[224:225], v244 offset:9344\n"
    "v_mfma_f32_16x16x32_bf16 v[32:35], a[244:247], v[160:163], v[32:35]\n"
    "s_add_u32 s60, s84, s85\n"
    "buffer_load_dwordx4 a[12:15], v254, s[20:23], s57 offen offset:3072 sc1\n"
    "v_mfma_f32_16x16x32_bf16 v[36:39], a[244:247], v[164:167], 0\n"
    "s_add_u32 s57, s73, 4096\n"
    "ds_read_b64_tr_b16 a[226:227], v244 offset:13952\n"
    "v_mfma_f32_16x16x32_bf16 v[36:39], a[240:243], v[148:151], v[36:39]\n"
    "s_mul_i32 s84, s87, 73728\n"
    "v_mfma_f32_16x16x32_bf16 v[40:43], a[240:243], v[152:155], 0\n"
    "s_lshl_b32 s85, s5, 3\n"
    "ds_read_b64_tr_b16 a[228:229], v244 offset:18560\n"
    "v_mfma_f32_16x16x32_bf16 v[40:43], a[244:247], v[168:171], v[40:43]\n"
    "s_add_u32 s85, s85, s4\n"
    "buffer_load_dwordx4 a[16:19], v254, s[20:23], s57 offen offset:0 sc1\n"
    "v_mfma_f32_16x16x32_bf16 v[44:47], a[244:247], v[172:175], 0\n"
    "s_lshl_b32 s85, s85, 5\n"
    "ds_read_b64_tr_b16 a[230:231], v244 offset:23168\n"
    "v_mfma_f32_16x16x32_bf16 v[44:47], a[240:243], v[156:159], v[44:47]\n"
    "s_waitcnt lgkmcnt(4)\n"
    "v_mfma_f32_16x16x32_bf16 v[60:63], a[248:251], v[156:159], 0\n"
    "s_add_u32 s62, s84, s85\n"
    "ds_read_b64_tr_b16 a[232:233], v244 offset:9376\n"
    "v_mfma_f32_16x16x32_bf16 v[60:63], a[252:255], v[172:175], v[60:63]\n"
    "s_and_b32 s85, s4, 1\n"
    "buffer_load_dwordx4 a[20:23], v254, s[20:23], s57 offen offset:1024 sc1\n"
    "v_mfma_f32_16x16x32_bf16 v[56:59], a[252:255], v[168:171], 0\n"
    "s_mul_i32 s85, s85, 36864\n"
    "ds_read_b64_tr_b16 a[234:235], v244 offset:13984\n"
    "v_mfma_f32_16x16x32_bf16 v[56:59], a[248:251], v[152:155], v[56:59]\n"
    "s_add_u32 s84, s84, s85\n"
    "v_mfma_f32_16x16x32_bf16 v[52:55], a[248:251], v[148:151], 0\n"
    "s_lshl_b32 s85, s5, 5\n"
    "ds_read_b64_tr_b16 a[236:237], v244 offset:18592\n"
    "v_mfma_f32_16x16x32_bf16 v[52:55], a[252:255], v[164:167], v[52:55]\n"
    "s_add_u32 s63, s84, s85\n"
    "buffer_load_dwordx4 a[24:27], v254, s[20:23], s57 offen offset:2048 sc1\n"
    "v_mfma_f32_16x16x32_bf16 v[48:51], a[252:255], v[160:163], 0\n"
    "s_mov_b64 s[48:49], s[20:21]\n"
    "ds_read_b64_tr_b16 a[238:239], v244 offset:23200\n"
    "v_mfma_f32_16x16x32_bf16 v[48:51], a[248:251], v[144:147], v[48:51]\n"
    "s_waitcnt lgkmcnt(4)\n"
    "v_mfma_f32_16x16x32_bf16 v[64:67], a[224:227], v[144:147], 0\n"
    "s_mov_b32 s51, s23\n"
    "ds_read_b64_tr_b16 a[240:241], v244 offset:9408\n"
    "v_mfma_f32_16x16x32_bf16 v[64:67], a[228:231], v[160:163], v[64:67]\n"
    "s_mov_b64 s[52:53], s[32:33]\n"
    "buffer_load_dwordx4 a[28:31], v254, s[20:23], s57 offen offset:3072 sc1\n"
    "v_mfma_f32_16x16x32_bf16 v[68:71], a[228:231], v[164:167], 0\n"
    "s_lshl_b32 s82, s5, 8\n"
    "ds_read_b64_tr_b16 a[242:243], v244 offset:14016\n"
    "v_mfma_f32_16x16x32_bf16 v[68:71], a[224:227], v[148:151], v[68:71]\n"
    "s_lshl_b32 s83, s4, 5\n"
    "v_mfma_f32_16x16x32_bf16 v[72:75], a[224:227], v[152:155], 0\n"
    "s_add_u32 s82, s82, s83\n"
    "ds_read_b64_tr_b16 a[244:245], v244 offset:18624\n"
    "v_mfma_f32_16x16x32_bf16 v[72:75], a[228:231], v[168:171], v[72:75]\n"
    "s_mov_b32 s55, s35\n"
    "buffer_load_dwordx2 v[224:225], v236, s[44:47], s82 offen\n"
    "v_mfma_f32_16x16x32_bf16 v[76:79], a[228:231], v[172:175], 0\n"
    "s_lshl_b32 s82, s5, 5\n"
    "ds_read_b64_tr_b16 a[246:247], v244 offset:23232\n"
    "v_mfma_f32_16x16x32_bf16 v[76:79], a[224:227], v[156:159], v[76:79]\n"
    "s_waitcnt lgkmcnt(4)\n"
    "v_mfma_f32_16x16x32_bf16 v[92:95], a[232:235], v[156:159], 0\n"
    "s_lshl_b32 s83, s4, 12\n"
    "ds_read_b64_tr_b16 a[248:249], v244 offset:9440\n"
    "v_mfma_f32_16x16x32_bf16 v[92:95], a[236:239], v[172:175], v[92:95]\n"
    "s_add_u32 s82, s82, s83\n"
    "buffer_load_dwordx2 v[226:227], v236, s[44:47], s82 offen offset:1024\n"
    "v_mfma_f32_16x16x32_bf16 v[88:91], a[236:239], v[168:171], 0\n"
    "s_waitcnt vmcnt(10)\n"
    "ds_read_b64_tr_b16 a[250:251], v244 offset:14048\n"
    "v_cmp_ne_u32 vcc, s66, v248\n"
    "v_mfma_f32_16x16x32_bf16 v[88:91], a[232:235], v[152:155], v[88:91]\n"
    "s_nop 0\n"
    "s_cbranch_vccnz RT9_%=\n"
    "RB9_%=:\n"
    "v_mfma_f32_16x16x32_bf16 v[84:87], a[232:235], v[148:151], 0\n"
    "s_add_u32 s57, s71, 8192\n"
    "ds_read_b64_tr_b16 a[252:253], v244 offset:18656\n"
    "v_mfma_f32_16x16x32_bf16 v[84:87], a[236:239], v[164:167], v[84:87]\n"
    "s_lshr_b32 s84, s4, 1\n"
    "buffer_load_dwordx4 a[32:35], v254, s[20:23], s57 offen offset:0 sc1\n"
    "v_mfma_f32_16x16x32_bf16 v[80:83], a[236:239], v[160:163], 0\n"
    "ds_read_b64_tr_b16 a[254:255], v244 offset:23264\n"
    "v_mfma_f32_16x16x32_bf16 v[80:83], a[232:235], v[144:147], v[80:83]\n"
    "s_waitcnt lgkmcnt(4)\n"
    "v_mfma_f32_16x16x32_bf16 v[96:99], a[240:243], v[144:147], 0\n"
    "s_cmp_eq_u32 s84, s7\n"
    "ds_read_b64_tr_b16 a[224:225], v244 offset:9472\n"
    "v_mfma_f32_16x16x32_bf16 v[96:99], a[244:247], v[160:163], v[96:99]\n"
    "s_cselect_b32 s50, s22, 0\n"
    "buffer_load_dwordx4 a[36:39], v254, s[20:23], s57 offen offset:1024 sc1\n"
    "v_mfma_f32_16x16x32_bf16 v[100:103], a[244:247], v[164:167], 0\n"
    "s_cselect_b32 s54, s34, 0\n"
    "ds_read_b64_tr_b16 a[226:227], v244 offset:14080\n"
    "v_mfma_f32_16x16x32_bf16 v[100:103], a[240:243], v[148:151], v[100:103]\n"
    "s_cselect_b64 s[64:65], -1, 0\n"
    "v_mfma_f32_16x16x32_bf16 v[104:107], a[240:243], v[152:155], 0\n"
    "ds_read_b64_tr_b16 a[228:229], v244 offset:18688\n"
    "v_mfma_f32_16x16x32_bf16 v[104:107], a[244:247], v[168:171], v[104:107]\n"
    "buffer_load_dwordx4 a[40:43], v254, s[20:23], s57 offen offset:2048 sc1\n"
    "v_mfma_f32_16x16x32_bf16 v[108:111], a[244:247], v[172:175], 0\n"
    "ds_read_b64_tr_b16 a[230:231], v244 offset:23296\n"
    "v_mfma_f32_16x16x32_bf16 v[108:111], a[240:243], v[156:159], v[108:111]\n"
    "s_waitcnt lgkmcnt(4)\n"
    "v_mfma_f32_16x16x32_bf16 v[124:127], a[248:251], v[156:159], 0\n"
    "ds_read_b64_tr_b16 a[232:233], v244 offset:27648\n"
    "v_mfma_f32_16x16x32_bf16 v[124:127], a[252:255], v[172:175], v[124:127]\n"
    "buffer_load_dwordx4 a[44:47], v254, s[20:23], s57 offen offset:3072 sc1\n"
    "v_mfma_f32_16x16x32_bf16 v[120:123], a[252:255], v[168:171], 0\n"
    "s_add_u32 s57, s71, 12288\n"
    "ds_read_b64_tr_b16 a[234:235], v244 offset:32256\n"
    "v_mfma_f32_16x16x32_bf16 v[120:123], a[248:251], v[152:155], v[120:123]\n"
    "v_mfma_f32_16x16x32_bf16 v[116:119], a[248:251], v[148:151], 0\n"
    "ds_read_b64_tr_b16 a[236:237], v244 offset:36864\n"
    "v_mfma_f32_16x16x32_bf16 v[116:119], a[252:255], v[164:167], v[116:119]\n"
    "buffer_load_dwordx4 a[48:51], v254, s[20:23], s57 offen offset:0 sc1\n"
    "v_mfma_f32_16x16x32_bf16 v[112:115], a[252:255], v[160:163], 0\n"
    "ds_read_b64_tr_b16 a[238:239], v244 offset:41472\n"
    "v_mfma_f32_16x16x32_bf16 v[112:115], a[248:251], v[144:147], v[112:115]\n"
    "s_waitcnt lgkmcnt(4)\n"
    "v_mfma_f32_16x16x32_bf16 v[128:131], a[224:227], v[144:147], 0\n"
    "ds_read_b64_tr_b16 a[240:241], v244 offset:27680\n"
    "v_mfma_f32_16x16x32_bf16 v[128:131], a[228:231], v[160:163], v[128:131]\n"
    "buffer_load_dwordx4 a[52:55], v254, s[20:23], s57 offen offset:1024 sc1\n"
    "v_mfma_f32_16x16x32_bf16 v[132:135], a[228:231], v[164:167], 0\n"
    "ds_read_b64_tr_b16 a[242:243], v244 offset:32288\n"
    "v_mfma_f32_16x16x32_bf16 v[132:135], a[224:227], v[148:151], v[132:135]\n"
    "v_mfma_f32_16x16x32_bf16 v[136:139], a[224:227], v[152:155], 0\n"
    "ds_read_b64_tr_b16 a[244:245], v244 offset:36896\n"
    "v_mfma_f32_16x16x32_bf16 v[136:139], a[228:231], v[168:171], v[136:139]\n"
    "buffer_load_dwordx4 a[56:59], v254, s[20:23], s57 offen offset:2048 sc1\n"
    "v_mfma_f32_16x16x32_bf16 v[140:143], a[228:231], v[172:175], 0\n"
    "ds_read_b64_tr_b16 a[246:247], v244 offset:41504\n"
    "v_mfma_f32_16x16x32_bf16 v[140:143], a[224:227], v[156:159], v[140:143]\n"
    "s_waitcnt lgkmcnt(4)\n"
    "v_mfma_f32_16x16x32_bf16 v[12:15], a[232:235], v[188:191], v[12:15]\n"
    "ds_read_b64_tr_b16 a[248:249], v244 offset:27712\n"
    "v_mfma_f32_16x16x32_bf16 v[12:15], a[236:239], v[204:207], v[12:15]\n"
    "buffer_load_dwordx4 a[60:63], v254, s[20:23], s57 offen offset:3072 sc1\n"
    "v_mfma_f32_16x16x32_bf16 v[8:11], a[236:239], v[200:203], v[8:11]\n"
    "ds_read_b64_tr_b16 a[250:251], v244 offset:32320\n"
    "v_mfma_f32_16x16x32_bf16 v[8:11], a[232:235], v[184:187], v[8:11]\n"
    "v_mfma_f32_16x16x32_bf16 v[4:7], a[232:235], v[180:183], v[4:7]\n"
    "ds_read_b64_tr_b16 a[252:253], v244 offset:36928\n"
    "v_mfma_f32_16x16x32_bf16 v[4:7], a[236:239], v[196:199], v[4:7]\n"
    "buffer_store_dword v250, v249, s[20:23], s76 offen offset:0 sc1\n"
    "v_mfma_f32_16x16x32_bf16 v[0:3], a[236:239], v[192:195], v[0:3]\n"
    "s_add_u32 s57, s72, 8192\n"
    "ds_read_b64_tr_b16 a[254:255], v244 offset:41536\n"
    "v_mfma_f32_16x16x32_bf16 v[0:3], a[232:235], v[176:179], v[0:3]\n"
    "s_waitcnt lgkmcnt(4)\n"
    "v_mfma_f32_16x16x32_bf16 v[16:19], a[240:243], v[176:179], v[16:19]\n"
    "ds_read_b64_tr_b16 a[224:225], v244 offset:27744\n"
    "v_mfma_f32_16x16x32_bf16 v[16:19], a[244:247], v[192:195], v[16:19]\n"
    "buffer_load_dwordx4 a[64:67], v254, s[20:23], s57 offen offset:0 sc1\n"
    "v_mfma_f32_16x16x32_bf16 v[20:23], a[244:247], v[196:199], v[20:23]\n"
    "ds_read_b64_tr_b16 a[226:227], v244 offset:32352\n"
    "v_mfma_f32_16x16x32_bf16 v[20:23], a[240:243], v[180:183], v[20:23]\n"
    "v_mfma_f32_16x16x32_bf16 v[24:27], a[240:243], v[184:187], v[24:27]\n"
    "ds_read_b64_tr_b16 a[228:229], v244 offset:36960\n"
    "v_mfma_f32_16x16x32_bf16 v[24:27], a[244:247], v[200:203], v[24:27]\n"
    "buffer_load_dwordx4 a[68:71], v254, s[20:23], s57 offen offset:1024 sc1\n"
    "v_mfma_f32_16x16x32_bf16 v[28:31], a[244:247], v[204:207], v[28:31]\n"
    "ds_read_b64_tr_b16 a[230:231], v244 offset:41568\n"
    "v_mfma_f32_16x16x32_bf16 v[28:31], a[240:243], v[188:191], v[28:31]\n"
    "s_waitcnt lgkmcnt(4)\n"
    "v_mfma_f32_16x16x32_bf16 v[44:47], a[248:251], v[188:191], v[44:47]\n"
    "ds_read_b64_tr_b16 a[232:233], v244 offset:27776\n"
    "v_mfma_f32_16x16x32_bf16 v[44:47], a[252:255], v[204:207], v[44:47]\n"
    "buffer_load_dwordx4 a[72:75], v254, s[20:23], s57 offen offset:2048 sc1\n"
    "v_mfma_f32_16x16x32_bf16 v[40:43], a[252:255], v[200:203], v[40:43]\n"
    "ds_read_b64_tr_b16 a[234:235], v244 offset:32384\n"
    "v_mfma_f32_16x16x32_bf16 v[40:43], a[248:251], v[184:187], v[40:43]\n"
    "v_mfma_f32_16x16x32_bf16 v[36:39], a[248:251], v[180:183], v[36:39]\n"
    "ds_read_b64_tr_b16 a[236:237], v244 offset:36992\n"
    "v_mfma_f32_16x16x32_bf16 v[36:39], a[252:255], v[196:199], v[36:39]\n"
    "buffer_load_dwordx4 a[76:79], v254, s[20:23], s57 offen offset:3072 sc1\n"
    "v_mfma_f32_16x16x32_bf16 v[32:35], a[252:255], v[192:195], v[32:35]\n"
    "s_add_u32 s57, s72, 12288\n"
    "ds_read_b64_tr_b16 a[238:239], v244 offset:41600\n"
    "v_mfma_f32_16x16x32_bf16 v[32:35], a[248:251], v[176:179], v[32:35]\n"
    "s_waitcnt lgkmcnt(4)\n"
    "v_mfma_f32_16x16x32_bf16 v[48:51], a[224:227], v[176:179], v[48:51]\n"
    "ds_read_b64_tr_b16 a[240:241], v244 offset:27808\n"
    "v_mfma_f32_16x16x32_bf16 v[48:51], a[228:231], v[192:195], v[48:51]\n"
    "buffer_load_dwordx4 a[80:83], v254, s[20:23], s57 offen offset:0 sc1\n"
    "v_mfma_f32_16x16x32_bf16 v[52:55], a[228:231], v[196:199], v[52:55]\n"
    "ds_read_b64_tr_b16 a[242:243], v244 offset:32416\n"
    "v_mfma_f32_16x16x32_bf16 v[52:55], a[224:227], v[180:183], v[52:55]\n"
    "v_mfma_f32_16x16x32_bf16 v[56:59], a[224:227], v[184:187], v[56:59]\n"
    "ds_read_b64_tr_b16 a[244:245], v244 offset:37024\n"
    "v_mfma_f32_16x16x32_bf16 v[56:59], a[228:231], v[200:203], v[56:59]\n"
    "buffer_load_dwordx4 a[84:87], v254, s[20:23], s57 offen offset:1024 sc1\n"
    "v_mfma_f32_16x16x32_bf16 v[60:63], a[228:231], v[204:207], v[60:63]\n"
    "ds_read_b64_tr_b16 a[246:247], v244 offset:41632\n"
    "v_mfma_f32_16x16x32_bf16 v[60:63], a[224:227], v[188:191], v[60:63]\n"
    "s_waitcnt lgkmcnt(4)\n"
    "v_mfma_f32_16x16x32_bf16 v[76:79], a[232:235], v[188:191], v[76:79]\n"
    "ds_read_b64_tr_b16 a[248:249], v244 offset:27840\n"
    "v_mfma_f32_16x16x32_bf16 v[76:79], a[236:239], v[204:207], v[76:79]\n"
    "buffer_load_dwordx4 a[88:91], v254, s[20:23], s57 offen offset:2048 sc1\n"
    "v_mfma_f32_16x16x32_bf16 v[72:75], a[236:239], v[200:203], v[72:75]\n"
    "ds_read_b64_tr_b16 a[250:251], v244 offset:32448\n"
    "v_mfma_f32_16x16x32_bf16 v[72:75], a[232:235], v[184:187], v[72:75]\n"
    "v_mfma_f32_16x16x32_bf16 v[68:71], a[232:235], v[180:183], v[68:71]\n"
    "ds_read_b64_tr_b16 a[252:253], v244 offset:37056\n"
    "v_mfma_f32_16x16x32_bf16 v[68:71], a[236:239], v[196:199], v[68:71]\n"
    "buffer_load_dwordx4 a[92:95], v254, s[20:23], s57 offen offset:3072 sc1\n"
    "v_mfma_f32_16x16x32_bf16 v[64:67], a[236:239], v[192:195], v[64:67]\n"
    "ds_read_b64_tr_b16 a[254:255], v244 offset:41664\n"
    "v_mfma_f32_16x16x32_bf16 v[64:67], a[232:235], v[176:179], v[64:67]\n"
    "s_waitcnt lgkmcnt(4)\n"
    "v_mfma_f32_16x16x32_bf16 v[80:83], a[240:243], v[176:179], v[80:83]\n"
    "ds_read_b64_tr_b16 a[224:225], v244 offset:27872\n"
    "v_mfma_f32_16x16x32_bf16 v[80:83], a[244:247], v[192:195], v[80:83]\n"
    "buffer_load_dword v248, v247, s[20:23], s77 offen offset:512 sc1\n"
    "v_mfma_f32_16x16x32_bf16 v[84:87], a[244:247], v[196:199], v[84:87]\n"
    "s_add_u32 s57, s73, 8192\n"
    "ds_read_b64_tr_b16 a[226:227], v244 offset:32480\n"
    "v_mfma_f32_16x16x32_bf16 v[84:87], a[240:243], v[180:183], v[84:87]\n"
    "v_mfma_f32_16x16x32_bf16 v[88:91], a[240:243], v[184:187], v[88:91]\n"
    "ds_read_b64_tr_b16 a[228:229], v244 offset:37088\n"
    "v_mfma_f32_16x16x32_bf16 v[88:91], a[244:247], v[200:203], v[88:91]\n"
    "buffer_load_dwordx4 a[96:99], v254, s[20:23], s57 offen offset:0 sc1\n"
    "v_mfma_f32_16x16x32_bf16 v[92:95], a[244:247], v[204:207], v[92:95]\n"
    "ds_read_b64_tr_b16 a[230:231], v244 offset:41696\n"
    "v_mfma_f32_16x16x32_bf16 v[92:95], a[240:243], v[188:191], v[92:95]\n"
    "s_waitcnt lgkmcnt(4)\n"
    "v_mfma_f32_16x16x32_bf16 v[108:111], a[248:251], v[188:191], v[108:111]\n"
    "ds_read_b64_tr_b16 a[232:233], v244 offset:27904\n"
    "v_mfma_f32_16x16x32_bf16 v[108:111], a[252:255], v[204:207], v[108:111]\n"
    "buffer_load_dwordx4 a[100:103], v254, s[20:23], s57 offen offset:1024 sc1\n"
    "v_mfma_f32_16x16x32_bf16 v[104:107], a[252:255], v[200:203], v[104:107]\n"
    "ds_read_b64_tr_b16 a[234:235], v244 offset:32512\n"
    "v_mfma_f32_16x16x32_bf16 v[104:107], a[248:251], v[184:187], v[104:107]\n"
    "v_mfma_f32_16x16x32_bf16 v[100:103], a[248:251], v[180:183], v[100:103]\n"
    "ds_read_b64_tr_b16 a[236:237], v244 offset:37120\n"
    "v_mfma_f32_16x16x32_bf16 v[100:103], a[252:255], v[196:199], v[100:103]\n"
    "buffer_load_dwordx4 a[104:107], v254, s[20:23], s57 offen offset:2048 sc1\n"
    "v_mfma_f32_16x16x32_bf16 v[96:99], a[252:255], v[192:195], v[96:99]\n"
    "ds_read_b64_tr_b16 a[238:239], v244 offset:41728\n"
    "v_mfma_f32_16x16x32_bf16 v[96:99], a[248:251], v[176:179], v[96:99]\n"
    "s_waitcnt lgkmcnt(4)\n"
    "v_mfma_f32_16x16x32_bf16 v[112:115], a[224:227], v[176:179], v[112:115]\n"
    "ds_read_b64_tr_b16 a[240:241], v244 offset:46080\n"
    "v_mfma_f32_16x16x32_bf16 v[112:115], a[228:231], v[192:195], v[112:115]\n"
    "buffer_load_dwordx4 a[108:111], v254, s[20:23], s57 offen offset:3072 sc1\n"
    "v_mfma_f32_16x16x32_bf16 v[116:119], a[228:231], v[196:199], v[116:119]\n"
    "s_add_u32 s57, s73, 12288\n"
    "ds_read_b64_tr_b16 a[242:243], v244 offset:50688\n"
    "v_mfma_f32_16x16x32_bf16 v[116:119], a[224:227], v[180:183], v[116:119]\n"
    "v_mfma_f32_16x16x32_bf16 v[120:123], a[224:227], v[184:187], v[120:123]\n"
    "ds_read_b64_tr_b16 a[244:245], v245 offset:0\n"
    "v_mfma_f32_16x16x32_bf16 v[120:123], a[228:231], v[200:203], v[120:123]\n"
    "buffer_load_dwordx4 a[112:115], v254, s[20:23], s57 offen offset:0 sc1\n"
    "v_mfma_f32_16x16x32_bf16 v[124:127], a[228:231], v[204:207], v[124:127]\n"
    "ds_read_b64_tr_b16 a[246:247], v245 offset:4608\n"
    "v_mfma_f32_16x16x32_bf16 v[124:127], a[224:227], v[188:191], v[124:127]\n"
    "s_waitcnt lgkmcnt(4)\n"
    "v_mfma_f32_16x16x32_bf16 v[140:143], a[232:235], v[188:191], v[140:143]\n"
    "ds_read_b64_tr_b16 a[248:249], v244 offset:46112\n"
    "v_mfma_f32_16x16x32_bf16 v[140:143], a[236:239], v[204:207], v[140:143]\n"
    "buffer_load_dwordx4 a[116:119], v254, s[20:23], s57 offen offset:1024 sc1\n"
    "v_mfma_f32_16x16x32_bf16 v[136:139], a[236:239], v[200:203], v[136:139]\n"
    "ds_read_b64_tr_b16 a[250:251], v244 offset:50720\n"
    "v_mfma_f32_16x16x32_bf16 v[136:139], a[232:235], v[184:187], v[136:139]\n"
    "v_mfma_f32_16x16x32_bf16 v[132:135], a[232:235], v[180:183], v[132:135]\n"
    "ds_read_b64_tr_b16 a[252:253], v245 offset:32\n"
    "v_mfma_f32_16x16x32_bf16 v[132:135], a[236:239], v[196:199], v[132:135]\n"
    "buffer_load_dwordx4 a[120:123], v254, s[20:23], s57 offen offset:2048 sc1\n"
    "v_mfma_f32_16x16x32_bf16 v[128:131], a[236:239], v[192:195], v[128:131]\n"
    "s_waitcnt vmcnt(27)\n"
    "ds_read_b64_tr_b16 a[254:255], v245 offset:4640\n"
    "v_mfma_f32_16x16x32_bf16 v[128:131], a[232:235], v[176:179], v[128:131]\n"
    "s_waitcnt lgkmcnt(4)\n"
    "v_mfma_f32_16x16x32_bf16 v[0:3], a[240:243], a[0:3], v[0:3]\n"
    "s_barrier\n"
    "ds_read_b64_tr_b16 a[224:225], v244 offset:46144\n"
    "v_mfma_f32_16x16x32_bf16 v[0:3], a[244:247], a[16:19], v[0:3]\n"
    "buffer_load_dwordx4 a[124:127], v254, s[20:23], s57 offen offset:3072 sc1\n"
    "v_mfma_f32_16x16x32_bf16 v[4:7], a[244:247], a[20:23], v[4:7]\n"
    "s_add_u32 m0, s78, 0\n"
    "ds_read_b64_tr_b16 a[226:227], v244 offset:50752\n"
    "v_mfma_f32_16x16x32_bf16 v[4:7], a[240:243], a[4:7], v[4:7]\n"
    "s_mov_b32 s56, s36\n"
    "v_mfma_f32_16x16x32_bf16 v[8:11], a[240:243], a[8:11], v[8:11]\n"
    "ds_read_b64_tr_b16 a[228:229], v245 offset:64\n"
    "v_mfma_f32_16x16x32_bf16 v[8:11], a[244:247], a[24:27], v[8:11]\n"
    "buffer_load_dwordx4 v251, s[28:31], s56 offen offset:0 lds\n"
    "v_mfma_f32_16x16x32_bf16 v[12:15], a[244:247], a[28:31], v[12:15]\n"
    "ds_read_b64_tr_b16 a[230:231], v245 offset:4672\n"
    "v_mfma_f32_16x16x32_bf16 v[12:15], a[240:243], a[12:15], v[12:15]\n"
    "s_waitcnt lgkmcnt(4)\n"
    "v_mfma_f32_16x16x32_bf16 v[28:31], a[248:251], a[12:15], v[28:31]\n"
    "ds_read_b64_tr_b16 a[232:233], v244 offset:46176\n"
    "v_mfma_f32_16x16x32_bf16 v[28:31], a[252:255], a[28:31], v[28:31]\n"
    "buffer_load_dwordx4 v252, s[28:31], s56 offen offset:1024 lds\n"
    "v_mfma_f32_16x16x32_bf16 v[24:27], a[252:255], a[24:27], v[24:27]\n"
    "s_add_u32 m0, s79, 0\n"
    "ds_read_b64_tr_b16 a[234:235], v244 offset:50784\n"
    "v_mfma_f32_16x16x32_bf16 v[24:27], a[248:251], a[8:11], v[24:27]\n"
    "v_mfma_f32_16x16x32_bf16 v[20:23], a[248:251], a[4:7], v[20:23]\n"
    "ds_read_b64_tr_b16 a[236:237], v245 offset:96\n"
    "v_mfma_f32_16x16x32_bf16 v[20:23], a[252:255], a[20:23], v[20:23]\n"
    "buffer_load_dwordx4 v253, s[28:31], s56 offen offset:0 lds\n"
    "v_mfma_f32_16x16x32_bf16 v[16:19], a[252:255], a[16:19], v[16:19]\n"
    "s_add_u32 m0, s78, 9216\n"
    "ds_read_b64_tr_b16 a[238:239], v245 offset:4704\n"
    "v_mfma_f32_16x16x32_bf16 v[16:19], a[248:251], a[0:3], v[16:19]\n"
    "s_waitcnt lgkmcnt(4)\n"
    "v_mfma_f32_16x16x32_bf16 v[32:35], a[224:227], a[0:3], v[32:35]\n"
    "s_add_u32 s56, s56, 36864\n"
    "ds_read_b64_tr_b16 a[240:241], v244 offset:46208\n"
    "v_mfma_f32_16x16x32_bf16 v[32:35], a[228:231], a[16:19], v[32:35]\n"
    "buffer_load_dwordx4 v251, s[28:31], s56 offen offset:0 lds\n"
    "v_mfma_f32_16x16x32_bf16 v[36:39], a[228:231], a[20:23], v[36:39]\n"
    "ds_read_b64_tr_b16 a[242:243], v244 offset:50816\n"
    "v_mfma_f32_16x16x32_bf16 v[36:39], a[224:227], a[4:7], v[36:39]\n"
    "v_mfma_f32_16x16x32_bf16 v[40:43], a[224:227], a[8:11], v[40:43]\n"
    "ds_read_b64_tr_b16 a[244:245], v245 offset:128\n"
    "v_mfma_f32_16x16x32_bf16 v[40:43], a[228:231], a[24:27], v[40:43]\n"
    "buffer_load_dwordx4 v252, s[28:31], s56 offen offset:1024 lds\n"
    "v_mfma_f32_16x16x32_bf16 v[44:47], a[228:231], a[28:31], v[44:47]\n"
    "s_add_u32 m0, s78, 18432\n"
    "ds_read_b64_tr_b16 a[246:247], v245 offset:4736\n"
    "v_mfma_f32_16x16x32_bf16 v[44:47], a[224:227], a[12:15], v[44:47]\n"
    "s_waitcnt lgkmcnt(4)\n"
    "v_mfma_f32_16x16x32_bf16 v[60:63], a[232:235], a[12:15], v[60:63]\n"
    "s_add_u32 s56, s56, 36864\n"
    "ds_read_b64_tr_b16 a[248:249], v244 offset:46240\n"
    "v_mfma_f32_16x16x32_bf16 v[60:63], a[236:239], a[28:31], v[60:63]\n"
    "buffer_load_dwordx4 v251, s[28:31], s56 offen offset:0 lds\n"
    "v_mfma_f32_16x16x32_bf16 v[56:59], a[236:239], a[24:27], v[56:59]\n"
    "ds_read_b64_tr_b16 a[250:251], v244 offset:50848\n"
    "v_mfma_f32_16x16x32_bf16 v[56:59], a[232:235], a[8:11], v[56:59]\n"
    "v_mfma_f32_16x16x32_bf16 v[52:55], a[232:235], a[4:7], v[52:55]\n"
    "ds_read_b64_tr_b16 a[252:253], v245 offset:160\n"
    "v_mfma_f32_16x16x32_bf16 v[52:55], a[236:239], a[20:23], v[52:55]\n"
    "buffer_load_dwordx4 v252, s[28:31], s56 offen offset:1024 lds\n"
    "v_mfma_f32_16x16x32_bf16 v[48:51], a[236:239], a[16:19], v[48:51]\n"
    "s_add_u32 m0, s78, 27648\n"
    "ds_read_b64_tr_b16 a[254:255], v245 offset:4768\n"
    "v_mfma_f32_16x16x32_bf16 v[48:51], a[232:235], a[0:3], v[48:51]\n"
    "s_waitcnt lgkmcnt(4)\n"
    "v_mfma_f32_16x16x32_bf16 v[64:67], a[240:243], a[0:3], v[64:67]\n"
    "s_add_u32 s56, s70, 0\n"
    "ds_read_b64_tr_b16 a[224:225], v244 offset:46272\n"
    "v_mfma_f32_16x16x32_bf16 v[64:67], a[244:247], a[16:19], v[64:67]\n"
    "buffer_load_dwordx4 v251, s[24:27], s56 offen offset:0 lds\n"
    "v_mfma_f32_16x16x32_bf16 v[68:71], a[244:247], a[20:23], v[68:71]\n"
    "ds_read_b64_tr_b16 a[226:227], v244 offset:50880\n"
    "v_mfma_f32_16x16x32_bf16 v[68:71], a[240:243], a[4:7], v[68:71]\n"
    "v_mfma_f32_16x16x32_bf16 v[72:75], a[240:243], a[8:11], v[72:75]\n"
    "ds_read_b64_tr_b16 a[228:229], v245 offset:192\n"
    "v_mfma_f32_16x16x32_bf16 v[72:75], a[244:247], a[24:27], v[72:75]\n"
    "buffer_load_dwordx4 v252, s[24:27], s56 offen offset:1024 lds\n"
    "v_mfma_f32_16x16x32_bf16 v[76:79], a[244:247], a[28:31], v[76:79]\n"
    "s_add_u32 m0, s79, 27648\n"
    "ds_read_b64_tr_b16 a[230:231], v245 offset:4800\n"
    "v_mfma_f32_16x16x32_bf16 v[76:79], a[240:243], a[12:15], v[76:79]\n"
    "s_waitcnt lgkmcnt(4)\n"
    "v_mfma_f32_16x16x32_bf16 v[92:95], a[248:251], a[12:15], v[92:95]\n"
    "ds_read_b64_tr_b16 a[232:233], v244 offset:46304\n"
    "v_mfma_f32_16x16x32_bf16 v[92:95], a[252:255], a[28:31], v[92:95]\n"
    "buffer_load_dwordx4 v253, s[24:27], s56 offen offset:0 lds\n"
    "v_mfma_f32_16x16x32_bf16 v[88:91], a[252:255], a[24:27], v[88:91]\n"
    "s_add_u32 m0, s78, 36864\n"
    "ds_read_b64_tr_b16 a[234:235], v244 offset:50912\n"
    "v_mfma_f32_16x16x32_bf16 v[88:91], a[248:251], a[8:11], v[88:91]\n"
    "s_add_u32 s56, s56, 36864\n"
    "v_mfma_f32_16x16x32_bf16 v[84:87], a[248:251], a[4:7], v[84:87]\n"
    "ds_read_b64_tr_b16 a[236:237], v245 offset:224\n"
    "v_mfma_f32_16x16x32_bf16 v[84:87], a[252:255], a[20:23], v[84:87]\n"
    "buffer_load_dwordx4 v251, s[24:27], s56 offen offset:0 lds\n"
    "v_mfma_f32_16x16x32_bf16 v[80:83], a[252:255], a[16:19], v[80:83]\n"
    "ds_read_b64_tr_b16 a[238:239], v245 offset:4832\n"
    "v_mfma_f32_16x16x32_bf16 v[80:83], a[248:251], a[0:3], v[80:83]\n"
    "s_waitcnt lgkmcnt(4)\n"
    "v_mfma_f32_16x16x32_bf16 v[96:99], a[224:227], a[0:3], v[96:99]\n"
    "ds_read_b64_tr_b16 a[240:241], v244 offset:46336\n"
    "v_mfma_f32_16x16x32_bf16 v[96:99], a[228:231], a[16:19], v[96:99]\n"
    "buffer_load_dwordx4 v252, s[24:27], s56 offen offset:1024 lds\n"
    "v_mfma_f32_16x16x32_bf16 v[100:103], a[228:231], a[20:23], v[100:103]\n"
    "s_waitcnt vmcnt(20)\n"
    "ds_read_b64_tr_b16 a[242:243], v244 offset:50944\n"
    "v_cmp_ne_u32 vcc, s66, v248\n"
    "v_mfma_f32_16x16x32_bf16 v[100:103], a[224:227], a[4:7], v[100:103]\n"
    "s_nop 0\n"
    "s_cbranch_vccnz RT10_%=\n"
    "RB10_%=:\n"
    "v_mfma_f32_16x16x32_bf16 v[104:107], a[224:227], a[8:11], v[104:107]\n"
    "s_add_u32 s57, s71, 16384\n"
    "ds_read_b64_tr_b16 a[244:245], v245 offset:256\n"
    "v_mfma_f32_16x16x32_bf16 v[104:107], a[228:231], a[24:27], v[104:107]\n"
    "buffer_load_dwordx4 a[128:131], v254, s[20:23], s57 offen offset:0 sc1\n"
    "v_mfma_f32_16x16x32_bf16 v[108:111], a[228:231], a[28:31], v[108:111]\n"
    "ds_read_b64_tr_b16 a[246:247], v245 offset:4864\n"
    "v_mfma_f32_16x16x32_bf16 v[108:111], a[224:227], a[12:15], v[108:111]\n"
    "s_waitcnt lgkmcnt(4)\n"
    "v_mfma_f32_16x16x32_bf16 v[124:127], a[232:235], a[12:15], v[124:127]\n"
    "ds_read_b64_tr_b16 a[248:249], v245 offset:9216\n"
    "v_mfma_f32_16x16x32_bf16 v[124:127], a[236:239], a[28:31], v[124:127]\n"
    "buffer_load_dwordx4 a[132:135], v254, s[20:23], s57 offen offset:1024 sc1\n"
    "v_mfma_f32_16x16x32_bf16 v[120:123], a[236:239], a[24:27], v[120:123]\n"
    "ds_read_b64_tr_b16 a[250:251], v245 offset:13824\n"
    "v_mfma_f32_16x16x32_bf16 v[120:123], a[232:235], a[8:11], v[120:123]\n"
    "v_mfma_f32_16x16x32_bf16 v[116:119], a[232:235], a[4:7], v[116:119]\n"
    "ds_read_b64_tr_b16 a[252:253], v245 offset:18432\n"
    "v_mfma_f32_16x16x32_bf16 v[116:119], a[236:239], a[20:23], v[116:119]\n"
    "buffer_load_dwordx4 a[136:139], v254, s[20:23], s57 offen offset:2048 sc1\n"
    "v_mfma_f32_16x16x32_bf16 v[112:115], a[236:239], a[16:19], v[112:115]\n"
    "ds_read_b64_tr_b16 a[254:255], v245 offset:23040\n"
    "v_mfma_f32_16x16x32_bf16 v[112:115], a[232:235], a[0:3], v[112:115]\n"
    "s_waitcnt lgkmcnt(4)\n"
    "v_mfma_f32_16x16x32_bf16 v[128:131], a[240:243], a[0:3], v[128:131]\n"
    "ds_read_b64_tr_b16 a[224:225], v245 offset:9248\n"
    "v_mfma_f32_16x16x32_bf16 v[128:131], a[244:247], a[16:19], v[128:131]\n"
    "buffer_load_dwordx4 a[140:143], v254, s[20:23], s57 offen offset:3072 sc1\n"
    "v_mfma_f32_16x16x32_bf16 v[132:135], a[244:247], a[20:23], v[132:135]\n"
    "s_add_u32 s57, s71, 20480\n"
    "ds_read_b64_tr_b16 a[226:227], v245 offset:13856\n"
    "v_mfma_f32_16x16x32_bf16 v[132:135], a[240:243], a[4:7], v[132:135]\n"
    "v_mfma_f32_16x16x32_bf16 v[136:139], a[240:243], a[8:11], v[136:139]\n"
    "ds_read_b64_tr_b16 a[228:229], v245 offset:18464\n"
    "v_mfma_f32_16x16x32_bf16 v[136:139], a[244:247], a[24:27], v[136:139]\n"
    "buffer_load_dwordx4 a[144:147], v254, s[20:23], s57 offen offset:0 sc1\n"
    "v_mfma_f32_16x16x32_bf16 v[140:143], a[244:247], a[28:31], v[140:143]\n"
    "ds_read_b64_tr_b16 a[230:231], v245 offset:23072\n"
    "v_mfma_f32_16x16x32_bf16 v[140:143], a[240:243], a[12:15], v[140:143]\n"
    "s_waitcnt lgkmcnt(4)\n"
    "v_mfma_f32_16x16x32_bf16 v[12:15], a[248:251], a[44:47], v[12:15]\n"
    "ds_read_b64_tr_b16 a[232:233], v245 offset:9280\n"
    "v_mfma_f32_16x16x32_bf16 v[12:15], a[252:255], a[60:63], v[12:15]\n"
    "buffer_load_dwordx4 a[148:151], v254, s[20:23], s57 offen offset:1024 sc1\n"
    "v_mfma_f32_16x16x32_bf16 v[8:11], a[252:255], a[56:59], v[8:11]\n"
    "ds_read_b64_tr_b16 a[234:235], v245 offset:13888\n"
    "v_mfma_f32_16x16x32_bf16 v[8:11], a[248:251], a[40:43], v[8:11]\n"
    "v_mfma_f32_16x16x32_bf16 v[4:7], a[248:251], a[36:39], v[4:7]\n"
    "ds_read_b64_tr_b16 a[236:237], v245 offset:18496\n"
    "v_mfma_f32_16x16x32_bf16 v[4:7], a[252:255], a[52:55], v[4:7]\n"
    "buffer_load_dwordx4 a[152:155], v254, s[20:23], s57 offen offset:2048 sc1\n"
    "v_mfma_f32_16x16x32_bf16 v[0:3], a[252:255], a[48:51], v[0:3]\n"
    "ds_read_b64_tr_b16 a[238:239], v245 offset:23104\n"
    "v_mfma_f32_16x16x32_bf16 v[0:3], a[248:251], a[32:35], v[0:3]\n"
    "s_waitcnt lgkmcnt(4)\n"
    "v_mfma_f32_16x16x32_bf16 v[16:19], a[224:227], a[32:35], v[16:19]\n"
    "ds_read_b64_tr_b16 a[240:241], v245 offset:9312\n"
    "v_mfma_f32_16x16x32_bf16 v[16:19], a[228:231], a[48:51], v[16:19]\n"
    "buffer_load_dwordx4 a[156:159], v254, s[20:23], s57 offen offset:3072 sc1\n"
    "v_mfma_f32_16x16x32_bf16 v[20:23], a[228:231], a[52:55], v[20:23]\n"
    "s_add_u32 s57, s72, 16384\n"
    "ds_read_b64_tr_b16 a[242:243], v245 offset:13920\n"
    "v_mfma_f32_16x16x32_bf16 v[20:23], a[224:227], a[36:39], v[20:23]\n"
    "v_lshlrev_b32 v228, 16, v224\n"
    "v_and_b32 v229, 0xffff0000, v224\n"
    "v_mfma_f32_16x16x32_bf16 v[24:27], a[224:227], a[40:43], v[24:27]\n"
    "ds_read_b64_tr_b16 a[244:245], v245 offset:18528\n"
    "v_lshlrev_b32 v230, 16, v225\n"
    "v_mfma_f32_16x16x32_bf16 v[24:27], a[228:231], a[56:59], v[24:27]\n"
    "buffer_load_dwordx4 a[160:163], v254, s[20:23], s57 offen offset:0 sc1\n"
    "v_mfma_f32_16x16x32_bf16 v[28:31], a[228:231], a[60:63], v[28:31]\n"
    "ds_read_b64_tr_b16 a[246:247], v245 offset:23136\n"
    "v_and_b32 v231, 0xffff0000, v225\n"
    "v_mfma_f32_16x16x32_bf16 v[28:31], a[224:227], a[44:47], v[28:31]\n"
    "s_waitcnt lgkmcnt(4)\n"
    "v_lshlrev_b32 v232, 16, v226\n"
    "v_and_b32 v233, 0xffff0000, v226\n"
    "v_mfma_f32_16x16x32_bf16 v[44:47], a[232:235], a[44:47], v[44:47]\n"
    "ds_read_b64_tr_b16 a[248:249], v245 offset:9344\n"
    "v_lshlrev_b32 v234, 16, v227\n"
    "v_mfma_f32_16x16x32_bf16 v[44:47], a[236:239], a[60:63], v[44:47]\n"
    "buffer_load_dwordx4 a[164:167], v254, s[20:23], s57 offen offset:1024 sc1\n"
    "v_mfma_f32_16x16x32_bf16 v[40:43], a[236:239], a[56:59], v[40:43]\n"
    "ds_read_b64_tr_b16 a[250:251], v245 offset:13952\n"
    "v_and_b32 v235, 0xffff0000, v227\n"
    "v_mfma_f32_16x16x32_bf16 v[40:43], a[232:235], a[40:43], v[40:43]\n"
    "v_rcp_f32 v224, v210\n"
    "v_mfma_f32_16x16x32_bf16 v[36:39], a[232:235], a[36:39], v[36:39]\n"
    "ds_read_b64_tr_b16 a[252:253], v245 offset:18560\n"
    "v_mfma_f32_16x16x32_bf16 v[36:39], a[236:239], a[52:55], v[36:39]\n"
    "buffer_load_dwordx4 a[168:171], v254, s[20:23], s57 offen offset:2048 sc1\n"
    "v_mfma_f32_16x16x32_bf16 v[32:35], a[236:239], a[48:51], v[32:35]\n"
    "ds_read_b64_tr_b16 a[254:255], v245 offset:23168\n"
    "v_mfma_f32_16x16x32_bf16 v[32:35], a[232:235], a[32:35], v[32:35]\n"
    "s_waitcnt lgkmcnt(4)\n"
    "v_rcp_f32 v225, v214\n"
    "v_mfma_f32_16x16x32_bf16 v[48:51], a[240:243], a[32:35], v[48:51]\n"
    "ds_read_b64_tr_b16 a[224:225], v245 offset:9376\n"
    "v_mfma_f32_16x16x32_bf16 v[48:51], a[244:247], a[48:51], v[48:51]\n"
    "buffer_load_dwordx4 a[172:175], v254, s[20:23], s57 offen offset:3072 sc1\n"
    "v_mfma_f32_16x16x32_bf16 v[52:55], a[244:247], a[52:55], v[52:55]\n"
    "s_add_u32 s57, s72, 20480\n"
    "ds_read_b64_tr_b16 a[226:227], v245 offset:13984\n"
    "v_mfma_f32_16x16x32_bf16 v[52:55], a[240:243], a[36:39], v[52:55]\n"
    "v_rcp_f32 v226, v218\n"
    "v_mfma_f32_16x16x32_bf16 v[56:59], a[240:243], a[40:43], v[56:59]\n"
    "ds_read_b64_tr_b16 a[228:229], v245 offset:18592\n"
    "v_mfma_f32_16x16x32_bf16 v[56:59], a[244:247], a[56:59], v[56:59]\n"
    "buffer_load_dwordx4 a[176:179], v254, s[20:23], s57 offen offset:0 sc1\n"
    "v_mfma_f32_16x16x32_bf16 v[60:63], a[244:247], a[60:63], v[60:63]\n"
    "ds_read_b64_tr_b16 a[230:231], v245 offset:23200\n"
    "v_mfma_f32_16x16x32_bf16 v[60:63], a[240:243], a[44:47], v[60:63]\n"
    "s_waitcnt lgkmcnt(4)\n"
    "v_rcp_f32 v227, v222\n"
    "v_mfma_f32_16x16x32_bf16 v[76:79], a[248:251], a[44:47], v[76:79]\n"
    "ds_read_b64_tr_b16 a[232:233], v245 offset:9408\n"
    "v_fmac_f32 v0, v224, v232\n"
    "v_mfma_f32_16x16x32_bf16 v[76:79], a[252:255], a[60:63], v[76:79]\n"
    "buffer_load_dwordx4 a[180:183], v254, s[20:23], s57 offen offset:1024 sc1\n"
    "v_mfma_f32_16x16x32_bf16 v[72:75], a[252:255], a[56:59], v[72:75]\n"
    "ds_read_b64_tr_b16 a[234:235], v245 offset:14016\n"
    "v_fmac_f32 v1, v224, v233\n"
    "v_mfma_f32_16x16x32_bf16 v[72:75], a[248:251], a[40:43], v[72:75]\n"
    "v_fmac_f32 v2, v224, v234\n"
    "v_fmac_f32 v3, v224, v235\n"
    "v_mfma_f32_16x16x32_bf16 v[68:71], a[248:251], a[36:39], v[68:71]\n"
    "ds_read_b64_tr_b16 a[236:237], v245 offset:18624\n"
    "v_mul_f32 v208, v224, v228\n"
    "v_mfma_f32_16x16x32_bf16 v[68:71], a[252:255], a[52:55], v[68:71]\n"
    "buffer_load_dwordx4 a[184:187], v254, s[20:23], s57 offen offset:2048 sc1\n"
    "v_mfma_f32_16x16x32_bf16 v[64:67], a[252:255], a[48:51], v[64:67]\n"
    "ds_read_b64_tr_b16 a[238:239], v245 offset:23232\n"
    "v_mul_f32 v209, v224, v229\n"
    "v_mfma_f32_16x16x32_bf16 v[64:67], a[248:251], a[32:35], v[64:67]\n"
    "s_waitcnt lgkmcnt(4)\n"
    "v_mul_f32 v210, v224, v230\n"
    "v_mul_f32 v211, v224, v231\n"
    "v_mfma_f32_16x16x32_bf16 v[80:83], a[224:227], a[32:35], v[80:83]\n"
    "ds_read_b64_tr_b16 a[240:241], v245 offset:9440\n"
    "v_fmac_f32 v4, v225, v232\n"
    "v_mfma_f32_16x16x32_bf16 v[80:83], a[228:231], a[48:51], v[80:83]\n"
    "buffer_load_dwordx4 a[188:191], v254, s[20:23], s57 offen offset:3072 sc1\n"
    "v_mfma_f32_16x16x32_bf16 v[84:87], a[228:231], a[52:55], v[84:87]\n"
    "s_add_u32 s57, s73, 16384\n"
    "ds_read_b64_tr_b16 a[242:243], v245 offset:14048\n"
    "v_fmac_f32 v5, v225, v233\n"
    "v_mfma_f32_16x16x32_bf16 v[84:87], a[224:227], a[36:39], v[84:87]\n"
    "v_fmac_f32 v6, v225, v234\n"
    "v_fmac_f32 v7, v225, v235\n"
    "v_mfma_f32_16x16x32_bf16 v[88:91], a[224:227], a[40:43], v[88:91]\n"
    "ds_read_b64_tr_b16 a[244:245], v245 offset:18656\n"
    "v_mul_f32 v212, v225, v228\n"
    "v_mfma_f32_16x16x32_bf16 v[88:91], a[228:231], a[56:59], v[88:91]\n"
    "buffer_load_dwordx4 a[192:195], v254, s[20:23], s57 offen offset:0 sc1\n"
    "v_mfma_f32_16x16x32_bf16 v[92:95], a[228:231], a[60:63], v[92:95]\n"
    "ds_read_b64_tr_b16 a[246:247], v245 offset:23264\n"
    "v_mul_f32 v213, v225, v229\n"
    "v_mfma_f32_16x16x32_bf16 v[92:95], a[224:227], a[44:47], v[92:95]\n"
    "s_waitcnt lgkmcnt(4)\n"
    "v_mul_f32 v214, v225, v230\n"
    "v_mul_f32 v215, v225, v231\n"
    "v_mfma_f32_16x16x32_bf16 v[108:111], a[232:235], a[44:47], v[108:111]\n"
    "ds_read_b64_tr_b16 a[248:249], v245 offset:9472\n"
    "v_fmac_f32 v8, v226, v232\n"
    "v_mfma_f32_16x16x32_bf16 v[108:111], a[236:239], a[60:63], v[108:111]\n"
    "buffer_load_dwordx4 a[196:199], v254, s[20:23], s57 offen offset:1024 sc1\n"
    "v_mfma_f32_16x16x32_bf16 v[104:107], a[236:239], a[56:59], v[104:107]\n"
    "ds_read_b64_tr_b16 a[250:251], v245 offset:14080\n"
    "v_fmac_f32 v9, v226, v233\n"
    "v_mfma_f32_16x16x32_bf16 v[104:107], a[232:235], a[40:43], v[104:107]\n"
    "v_fmac_f32 v10, v226, v234\n"
    "v_fmac_f32 v11, v226, v235\n"
    "v_mfma_f32_16x16x32_bf16 v[100:103], a[232:235], a[36:39], v[100:103]\n"
    "ds_read_b64_tr_b16 a[252:253], v245 offset:18688\n"
    "v_mul_f32 v216, v226, v228\n"
    "v_mfma_f32_16x16x32_bf16 v[100:103], a[236:239], a[52:55], v[100:103]\n"
    "buffer_load_dwordx4 a[200:203], v254, s[20:23], s57 offen offset:2048 sc1\n"
    "v_mfma_f32_16x16x32_bf16 v[96:99], a[236:239], a[48:51], v[96:99]\n"
    "ds_read_b64_tr_b16 a[254:255], v245 offset:23296\n"
    "v_mul_f32 v217, v226, v229\n"
    "v_mfma_f32_16x16x32_bf16 v[96:99], a[232:235], a[32:35], v[96:99]\n"
    "s_waitcnt lgkmcnt(4)\n"
    "v_mul_f32 v218, v226, v230\n"
    "v_mul_f32 v219, v226, v231\n"
    "v_mfma_f32_16x16x32_bf16 v[112:115], a[240:243], a[32:35], v[112:115]\n"
    "ds_read_b64_tr_b16 a[224:225], v245 offset:27648\n"
    "v_fmac_f32 v12, v227, v232\n"
    "v_mfma_f32_16x16x32_bf16 v[112:115], a[244:247], a[48:51], v[112:115]\n"
    "buffer_load_dwordx4 a[204:207], v254, s[20:23], s57 offen offset:3072 sc1\n"
    "v_mfma_f32_16x16x32_bf16 v[116:119], a[244:247], a[52:55], v[116:119]\n"
    "s_add_u32 s57, s73, 20480\n"
    "ds_read_b64_tr_b16 a[226:227], v245 offset:32256\n"
    "v_fmac_f32 v13, v227, v233\n"
    "v_mfma_f32_16x16x32_bf16 v[116:119], a[240:243], a[36:39], v[116:119]\n"
    "v_fmac_f32 v14, v227, v234\n"
    "v_fmac_f32 v15, v227, v235\n"
    "v_mfma_f32_16x16x32_bf16 v[120:123], a[240:243], a[40:43], v[120:123]\n"
    "ds_read_b64_tr_b16 a[228:229], v245 offset:36864\n"
    "v_mul_f32 v220, v227, v228\n"
    "v_mfma_f32_16x16x32_bf16 v[120:123], a[244:247], a[56:59], v[120:123]\n"
    "buffer_load_dwordx4 a[208:211], v254, s[20:23], s57 offen offset:0 sc1\n"
    "v_mfma_f32_16x16x32_bf16 v[124:127], a[244:247], a[60:63], v[124:127]\n"
    "ds_read_b64_tr_b16 a[230:231], v245 offset:41472\n"
    "v_mul_f32 v221, v227, v229\n"
    "v_mfma_f32_16x16x32_bf16 v[124:127], a[240:243], a[44:47], v[124:127]\n"
    "s_waitcnt lgkmcnt(4)\n"
    "v_mul_f32 v222, v227, v230\n"
    "v_mul_f32 v223, v227, v231\n"
    "v_mfma_f32_16x16x32_bf16 v[140:143], a[248:251], a[44:47], v[140:143]\n"
    "ds_read_b64_tr_b16 a[232:233], v245 offset:27680\n"
    "v_mfma_f32_16x16x32_bf16 v[140:143], a[252:255], a[60:63], v[140:143]\n"
    "buffer_load_dwordx4 a[212:215], v254, s[20:23], s57 offen offset:1024 sc1\n"
    "v_mfma_f32_16x16x32_bf16 v[136:139], a[252:255], a[56:59], v[136:139]\n"
    "ds_read_b64_tr_b16 a[234:235], v245 offset:32288\n"
    "v_mfma_f32_16x16x32_bf16 v[136:139], a[248:251], a[40:43], v[136:139]\n"
    "v_mfma_f32_16x16x32_bf16 v[132:135], a[248:251], a[36:39], v[132:135]\n"
    "ds_read_b64_tr_b16 a[236:237], v245 offset:36896\n"
    "v_mfma_f32_16x16x32_bf16 v[132:135], a[252:255], a[52:55], v[132:135]\n"
    "buffer_load_dwordx4 a[216:219], v254, s[20:23], s57 offen offset:2048 sc1\n"
    "v_mfma_f32_16x16x32_bf16 v[128:131], a[252:255], a[48:51], v[128:131]\n"
    "ds_read_b64_tr_b16 a[238:239], v245 offset:41504\n"
    "v_mfma_f32_16x16x32_bf16 v[128:131], a[248:251], a[32:35], v[128:131]\n"
    "s_waitcnt lgkmcnt(4)\n"
    "v_mfma_f32_16x16x32_bf16 v[0:3], a[224:227], a[64:67], v[0:3]\n"
    "ds_read_b64_tr_b16 a[240:241], v245 offset:27712\n"
    "v_mfma_f32_16x16x32_bf16 v[0:3], a[228:231], a[80:83], v[0:3]\n"
    "buffer_load_dwordx4 a[220:223], v254, s[20:23], s57 offen offset:3072 sc1\n"
    "v_mfma_f32_16x16x32_bf16 v[4:7], a[228:231], a[84:87], v[4:7]\n"
    "s_add_u32 s57, s75, 0\n"
    "ds_read_b64_tr_b16 a[242:243], v245 offset:32320\n"
    "v_bfe_u32 v224, v255, 4, 1\n"
    "v_mfma_f32_16x16x32_bf16 v[4:7], a[224:227], a[68:71], v[4:7]\n"
    "v_bfe_u32 v225, v255, 2, 2\n"
    "v_mad_u32_u24 v225, v224, 36, v225\n"
    "v_mfma_f32_16x16x32_bf16 v[8:11], a[224:227], a[72:75], v[8:11]\n"
    "ds_read_b64_tr_b16 a[244:245], v245 offset:36928\n"
    "v_mul_u32_u24 v225, 288, v225\n"
    "v_mfma_f32_16x16x32_bf16 v[8:11], a[228:231], a[88:91], v[8:11]\n"
    "buffer_load_dwordx4 v[144:147], v254, s[20:23], s57 offen offset:0 sc1\n"
    "v_mfma_f32_16x16x32_bf16 v[12:15], a[228:231], a[92:95], v[12:15]\n"
    "ds_read_b64_tr_b16 a[246:247], v245 offset:41536\n"
    "v_and_b32 v226, 3, v255\n"
    "v_mfma_f32_16x16x32_bf16 v[12:15], a[224:227], a[76:79], v[12:15]\n"
    "s_waitcnt lgkmcnt(4)\n"
    "v_lshl_add_u32 v225, v226, 3, v225\n"
    "v_add_u32 v247, 4608, v225\n"
    "v_mfma_f32_16x16x32_bf16 v[28:31], a[232:235], a[76:79], v[28:31]\n"
    "ds_read_b64_tr_b16 a[248:249], v245 offset:27744\n"
    "v_and_b32 v224, 31, v255\n"
    "v_mfma_f32_16x16x32_bf16 v[28:31], a[236:239], a[92:95], v[28:31]\n"
    "buffer_load_dwordx4 v[148:151], v254, s[20:23], s57 offen offset:1024 sc1\n"
    "v_mfma_f32_16x16x32_bf16 v[24:27], a[236:239], a[88:91], v[24:27]\n"
    "ds_read_b64_tr_b16 a[250:251], v245 offset:32352\n"
    "v_mul_u32_u24 v224, 1152, v224\n"
    "v_mfma_f32_16x16x32_bf16 v[24:27], a[232:235], a[72:75], v[24:27]\n"
    "v_lshrrev_b32 v225, 5, v255\n"
    "v_lshl_add_u32 v251, v225, 4, v224\n"
    "v_mfma_f32_16x16x32_bf16 v[20:23], a[232:235], a[68:71], v[20:23]\n"
    "ds_read_b64_tr_b16 a[252:253], v245 offset:36960\n"
    "v_lshlrev_b32 v252, 3, v255\n"
    "v_mfma_f32_16x16x32_bf16 v[20:23], a[236:239], a[84:87], v[20:23]\n"
    "buffer_load_dwordx4 v[152:155], v254, s[20:23], s57 offen offset:2048 sc1\n"
    "v_mfma_f32_16x16x32_bf16 v[16:19], a[236:239], a[80:83], v[16:19]\n"
    "ds_read_b64_tr_b16 a[254:255], v245 offset:41568\n"
    "v_and_b32 v250, 0x1fff, v250\n"
    "v_mfma_f32_16x16x32_bf16 v[16:19], a[232:235], a[64:67], v[16:19]\n"
    "s_waitcnt lgkmcnt(4)\n"
    "v_or_b32 v250, 0x4000, v250\n"
    "v_lshlrev_b32 v250, 16, v250\n"
    "v_mfma_f32_16x16x32_bf16 v[32:35], a[240:243], a[64:67], v[32:35]\n"
    "ds_read_b64_tr_b16 a[224:225], v245 offset:27776\n"
    "v_mfma_f32_16x16x32_bf16 v[32:35], a[244:247], a[80:83], v[32:35]\n"
    "buffer_load_dwordx4 v[156:159], v254, s[20:23], s57 offen offset:3072 sc1\n"
    "v_mfma_f32_16x16x32_bf16 v[36:39], a[244:247], a[84:87], v[36:39]\n"
    "s_add_u32 s57, s75, 4096\n"
    "ds_read_b64_tr_b16 a[226:227], v245 offset:32384\n"
    "v_mfma_f32_16x16x32_bf16 v[36:39], a[240:243], a[68:71], v[36:39]\n"
    "v_mfma_f32_16x16x32_bf16 v[40:43], a[240:243], a[72:75], v[40:43]\n"
    "ds_read_b64_tr_b16 a[228:229], v245 offset:36992\n"
    "v_mfma_f32_16x16x32_bf16 v[40:43], a[244:247], a[88:91], v[40:43]\n"
    "buffer_load_dwordx4 v[160:163], v254, s[20:23], s57 offen offset:0 sc1\n"
    "v_mfma_f32_16x16x32_bf16 v[44:47], a[244:247], a[92:95], v[44:47]\n"
    "ds_read_b64_tr_b16 a[230:231], v245 offset:41600\n"
    "v_mfma_f32_16x16x32_bf16 v[44:47], a[240:243], a[76:79], v[44:47]\n"
    "s_waitcnt lgkmcnt(4)\n"
    "v_mfma_f32_16x16x32_bf16 v[60:63], a[248:251], a[76:79], v[60:63]\n"
    "ds_read_b64_tr_b16 a[232:233], v245 offset:27808\n"
    "v_mfma_f32_16x16x32_bf16 v[60:63], a[252:255], a[92:95], v[60:63]\n"
    "buffer_load_dwordx4 v[164:167], v254, s[20:23], s57 offen offset:1024 sc1\n"
    "v_mfma_f32_16x16x32_bf16 v[56:59], a[252:255], a[88:91], v[56:59]\n"
    "ds_read_b64_tr_b16 a[234:235], v245 offset:32416\n"
    "v_mfma_f32_16x16x32_bf16 v[56:59], a[248:251], a[72:75], v[56:59]\n"
    "v_mfma_f32_16x16x32_bf16 v[52:55], a[248:251], a[68:71], v[52:55]\n"
    "ds_read_b64_tr_b16 a[236:237], v245 offset:37024\n"
    "v_mfma_f32_16x16x32_bf16 v[52:55], a[252:255], a[84:87], v[52:55]\n"
    "buffer_load_dwordx4 v[168:171], v254, s[20:23], s57 offen offset:2048 sc1\n"
    "v_mfma_f32_16x16x32_bf16 v[48:51], a[252:255], a[80:83], v[48:51]\n"
    "ds_read_b64_tr_b16 a[238:239], v245 offset:41632\n"
    "v_mfma_f32_16x16x32_bf16 v[48:51], a[248:251], a[64:67], v[48:51]\n"
    "s_waitcnt lgkmcnt(4)\n"
    "v_mfma_f32_16x16x32_bf16 v[64:67], a[224:227], a[64:67], v[64:67]\n"
    "ds_read_b64_tr_b16 a[240:241], v245 offset:27840\n"
    "v_mfma_f32_16x16x32_bf16 v[64:67], a[228:231], a[80:83], v[64:67]\n"
    "buffer_load_dwordx4 v[172:175], v254, s[20:23], s57 offen offset:3072 sc1\n"
    "v_mfma_f32_16x16x32_bf16 v[68:71], a[228:231], a[84:87], v[68:71]\n"
    "s_add_u32 s57, s75, 8192\n"
    "ds_read_b64_tr_b16 a[242:243], v245 offset:32448\n"
    "v_mfma_f32_16x16x32_bf16 v[68:71], a[224:227], a[68:71], v[68:71]\n"
    "v_mfma_f32_16x16x32_bf16 v[72:75], a[224:227], a[72:75], v[72:75]\n"
    "ds_read_b64_tr_b16 a[244:245], v245 offset:37056\n"
    "v_mfma_f32_16x16x32_bf16 v[72:75], a[228:231], a[88:91], v[72:75]\n"
    "buffer_load_dwordx4 v[176:179], v254, s[20:23], s57 offen offset:0 sc1\n"
    "v_mfma_f32_16x16x32_bf16 v[76:79], a[228:231], a[92:95], v[76:79]\n"
    "ds_read_b64_tr_b16 a[246:247], v245 offset:41664\n"
    "v_mfma_f32_16x16x32_bf16 v[76:79], a[224:227], a[76:79], v[76:79]\n"
    "s_waitcnt lgkmcnt(4)\n"
    "v_mfma_f32_16x16x32_bf16 v[92:95], a[232:235], a[76:79], v[92:95]\n"
    "ds_read_b64_tr_b16 a[248:249], v245 offset:27872\n"
    "v_mfma_f32_16x16x32_bf16 v[92:95], a[236:239], a[92:95], v[92:95]\n"
    "buffer_load_dwordx4 v[180:183], v254, s[20:23], s57 offen offset:1024 sc1\n"
    "v_mfma_f32_16x16x32_bf16 v[88:91], a[236:239], a[88:91], v[88:91]\n"
    "ds_read_b64_tr_b16 a[250:251], v245 offset:32480\n"
    "v_mfma_f32_16x16x32_bf16 v[88:91], a[232:235], a[72:75], v[88:91]\n"
    "v_mfma_f32_16x16x32_bf16 v[84:87], a[232:235], a[68:71], v[84:87]\n"
    "ds_read_b64_tr_b16 a[252:253], v245 offset:37088\n"
    "v_mfma_f32_16x16x32_bf16 v[84:87], a[236:239], a[84:87], v[84:87]\n"
    "buffer_load_dwordx4 v[184:187], v254, s[20:23], s57 offen offset:2048 sc1\n"
    "v_mfma_f32_16x16x32_bf16 v[80:83], a[236:239], a[80:83], v[80:83]\n"
    "ds_read_b64_tr_b16 a[254:255], v245 offset:41696\n"
    "v_mfma_f32_16x16x32_bf16 v[80:83], a[232:235], a[64:67], v[80:83]\n"
    "s_waitcnt lgkmcnt(4)\n"
    "v_mfma_f32_16x16x32_bf16 v[96:99], a[240:243], a[64:67], v[96:99]\n"
    "ds_read_b64_tr_b16 a[224:225], v245 offset:27904\n"
    "v_mfma_f32_16x16x32_bf16 v[96:99], a[244:247], a[80:83], v[96:99]\n"
    "buffer_load_dwordx4 v[188:191], v254, s[20:23], s57 offen offset:3072 sc1\n"
    "v_mfma_f32_16x16x32_bf16 v[100:103], a[244:247], a[84:87], v[100:103]\n"
    "ds_read_b64_tr_b16 a[226:227], v245 offset:32512\n"
    "v_mfma_f32_16x16x32_bf16 v[100:103], a[240:243], a[68:71], v[100:103]\n"
    "v_mfma_f32_16x16x32_bf16 v[104:107], a[240:243], a[72:75], v[104:107]\n"
    "ds_read_b64_tr_b16 a[228:229], v245 offset:37120\n"
    "v_mfma_f32_16x16x32_bf16 v[104:107], a[244:247], a[88:91], v[104:107]\n"
    "v_mfma_f32_16x16x32_bf16 v[108:111], a[244:247], a[92:95], v[108:111]\n"
    "s_waitcnt vmcnt(36)\n"
    "ds_read_b64_tr_b16 a[230:231], v245 offset:41728\n"
    "v_mfma_f32_16x16x32_bf16 v[108:111], a[240:243], a[76:79], v[108:111]\n"
    "s_waitcnt lgkmcnt(4)\n"
    "v_mfma_f32_16x16x32_bf16 v[124:127], a[248:251], a[76:79], v[124:127]\n"
    "s_barrier\n"
    "ds_read_b64_tr_b16 a[232:233], v245 offset:46080\n"
    "v_mfma_f32_16x16x32_bf16 v[124:127], a[252:255], a[92:95], v[124:127]\n"
    "v_mfma_f32_16x16x32_bf16 v[120:123], a[252:255], a[88:91], v[120:123]\n"
    "ds_read_b64_tr_b16 a[234:235], v245 offset:50688\n"
    "v_mfma_f32_16x16x32_bf16 v[120:123], a[248:251], a[72:75], v[120:123]\n"
    "v_mfma_f32_16x16x32_bf16 v[116:119], a[248:251], a[68:71], v[116:119]\n"
    "ds_read_b64_tr_b16 a[236:237], v246 offset:0\n"
    "v_mfma_f32_16x16x32_bf16 v[116:119], a[252:255], a[84:87], v[116:119]\n"
    "v_mfma_f32_16x16x32_bf16 v[112:115], a[252:255], a[80:83], v[112:115]\n"
    "ds_read_b64_tr_b16 a[238:239], v246 offset:4608\n"
    "v_mfma_f32_16x16x32_bf16 v[112:115], a[248:251], a[64:67], v[112:115]\n"
    "s_waitcnt lgkmcnt(4)\n"
    "v_mfma_f32_16x16x32_bf16 v[128:131], a[224:227], a[64:67], v[128:131]\n"
    "ds_read_b64_tr_b16 a[240:241], v246 offset:9216\n"
    "v_mfma_f32_16x16x32_bf16 v[128:131], a[228:231], a[80:83], v[128:131]\n"
    "v_mfma_f32_16x16x32_bf16 v[132:135], a[228:231], a[84:87], v[132:135]\n"
    "ds_read_b64_tr_b16 a[242:243], v246 offset:13824\n"
    "v_mfma_f32_16x16x32_bf16 v[132:135], a[224:227], a[68:71], v[132:135]\n"
    "v_mfma_f32_16x16x32_bf16 v[136:139], a[224:227], a[72:75], v[136:139]\n"
    "ds_read_b64_tr_b16 a[244:245], v246 offset:18432\n"
    "v_mfma_f32_16x16x32_bf16 v[136:139], a[228:231], a[88:91], v[136:139]\n"
    "v_mfma_f32_16x16x32_bf16 v[140:143], a[228:231], a[92:95], v[140:143]\n"
    "ds_read_b64_tr_b16 a[246:247], v246 offset:23040\n"
    "v_mfma_f32_16x16x32_bf16 v[140:143], a[224:227], a[76:79], v[140:143]\n"
    "s_waitcnt lgkmcnt(4)\n"
    "s_branch JOIN_%=\n"
    "RT1_%=:\n"
    "s_sleep 1\n"
    "buffer_load_dwordx4 v[204:207], v200, s[20:23], s37 offen sc1\n"
    "s_waitcnt vmcnt(0)\n"
    "v_cmp_ne_u32 vcc, s66, v207\n"
    "s_sub_u32 s67, s67, 1\n"
    "s_cmp_lt_i32 s67, 0\n"
    "s_cbranch_scc1 RB1_%=\n"
    "s_cbranch_vccnz RT1_%=\n"
    "s_branch RB1_%=\n"
    "RT2_%=:\n"
    "s_sleep 1\n"
    "buffer_load_dword v248, v247, s[20:23], s77 offen offset:0 sc1\n"
    "s_waitcnt vmcnt(0)\n"
    "v_cmp_ne_u32 vcc, s66, v248\n"
    "s_sub_u32 s67, s67, 1\n"
    "s_cmp_lt_i32 s67, 0\n"
    "s_cbranch_scc1 RB2_%=\n"
    "s_cbranch_vccnz RT2_%=\n"
    "s_branch RB2_%=\n"
    "RT3_%=:\n"
    "s_sleep 1\n"
    "buffer_load_dword v248, v247, s[20:23], s77 offen offset:256 sc1\n"
    "s_waitcnt vmcnt(0)\n"
    "v_cmp_ne_u32 vcc, s66, v248\n"
    "s_sub_u32 s67, s67, 1\n"
    "s_cmp_lt_i32 s67, 0\n"
    "s_cbranch_scc1 RB3_%=\n"
    "s_cbranch_vccnz RT3_%=\n"
    "s_branch RB3_%=\n"
    "RT4_%=:\n"
    "s_sleep 1\n"
    "buffer_load_dword v248, v247, s[20:23], s77 offen offset:512 sc1\n"
    "s_waitcnt vmcnt(0)\n"
    "v_cmp_ne_u32 vcc, s66, v248\n"
    "s_sub_u32 s67, s67, 1\n"
    "s_cmp_lt_i32 s67, 0\n"
    "s_cbranch_scc1 RB4_%=\n"
    "s_cbranch_vccnz RT4_%=\n"
    "s_branch RB4_%=\n"
    "RT5_%=:\n"
    "s_sleep 1\n"
    "buffer_load_dword v248, off, s[20:23], s80 sc1\n"
    "s_waitcnt vmcnt(0)\n"
    "v_cmp_ne_u32 vcc, s66, v248\n"
    "s_sub_u32 s67, s67, 1\n"
    "s_cmp_lt_i32 s67, 0\n"
    "s_cbranch_scc1 RB5_%=\n"
    "s_cbranch_vccnz RT5_%=\n"
    "s_branch RB5_%=\n"
    "RT6_%=:\n"
    "s_sleep 1\n"
    "buffer_load_dwordx4 v[228:231], v227, s[20:23], s37 offen sc1\n"
    "s_waitcnt vmcnt(0)\n"
    "v_cmp_ne_u32 vcc, s66, v231\n"
    "s_sub_u32 s67, s67, 1\n"
    "s_cmp_lt_i32 s67, 0\n"
    "s_cbranch_scc1 RB6_%=\n"
    "s_cbranch_vccnz RT6_%=\n"
    "s_branch RB6_%=\n"
    "RT7_%=:\n"
    "s_sleep 1\n"
    "buffer_load_dword v248, v247, s[20:23], s77 offen offset:0 sc1\n"
    "s_waitcnt vmcnt(0)\n"
    "v_cmp_ne_u32 vcc, s66, v248\n"
    "s_sub_u32 s67, s67, 1\n"
    "s_cmp_lt_i32 s67, 0\n"
    "s_cbranch_scc1 RB7_%=\n"
    "s_cbranch_vccnz RT7_%=\n"
    "s_branch RB7_%=\n"
    "RT8_%=:\n"
    "s_sleep 1\n"
    "s_add_u32 s86, s39, 0\n"
    "buffer_load_dwordx4 v[0:3], v254, s[20:23], s86 offen offset:0 sc1\n"
    "buffer_load_dwordx4 v[32:35], v254, s[20:23], s86 offen offset:1024 sc1\n"
    "buffer_load_dwordx4 v[4:7], v254, s[20:23], s86 offen offset:2048 sc1\n"
    "buffer_load_dwordx4 v[36:39], v254, s[20:23], s86 offen offset:3072 sc1\n"
    "s_add_u32 s86, s39, 4096\n"
    "buffer_load_dwordx4 v[8:11], v254, s[20:23], s86 offen offset:0 sc1\n"
    "buffer_load_dwordx4 v[40:43], v254, s[20:23], s86 offen offset:1024 sc1\n"
    "buffer_load_dwordx4 v[12:15], v254, s[20:23], s86 offen offset:2048 sc1\n"
    "buffer_load_dwordx4 v[44:47], v254, s[20:23], s86 offen offset:3072 sc1\n"
    "s_add_u32 s86, s39, 8192\n"
    "buffer_load_dwordx4 v[16:19], v254, s[20:23], s86 offen offset:0 sc1\n"
    "buffer_load_dwordx4 v[48:51], v254, s[20:23], s86 offen offset:1024 sc1\n"
    "buffer_load_dwordx4 v[20:23], v254, s[20:23], s86 offen offset:2048 sc1\n"
    "buffer_load_dwordx4 v[52:55], v254, s[20:23], s86 offen offset:3072 sc1\n"
    "s_add_u32 s86, s39, 12288\n"
    "buffer_load_dwordx4 v[24:27], v254, s[20:23], s86 offen offset:0 sc1\n"
    "buffer_load_dwordx4 v[56:59], v254, s[20:23], s86 offen offset:1024 sc1\n"
    "buffer_load_dwordx4 v[28:31], v254, s[20:23], s86 offen offset:2048 sc1\n"
    "buffer_load_dwordx4 v[60:63], v254, s[20:23], s86 offen offset:3072 sc1\n"
    "s_waitcnt vmcnt(0)\n"
    "v_xor_b32 v224, s66, v3\n"
    "v_bitop3_b32 v224, v224, v7, s66 bitop3:0xf6\n"
    "v_bitop3_b32 v224, v224, v11, s66 bitop3:0xf6\n"
    "v_bitop3_b32 v224, v224, v15, s66 bitop3:0xf6\n"
    "v_bitop3_b32 v224, v224, v19, s66 bitop3:0xf6\n"
    "v_bitop3_b32 v224, v224, v23, s66 bitop3:0xf6\n"
    "v_bitop3_b32 v224, v224, v27, s66 bitop3:0xf6\n"
    "v_bitop3_b32 v224, v224, v31, s66 bitop3:0xf6\n"
    "v_bitop3_b32 v224, v224, v35, s66 bitop3:0xf6\n"
    "v_bitop3_b32 v224, v224, v39, s66 bitop3:0xf6\n"
    "v_bitop3_b32 v224, v224, v43, s66 bitop3:0xf6\n"
    "v_bitop3_b32 v224, v224, v47, s66 bitop3:0xf6\n"
    "v_bitop3_b32 v224, v224, v51, s66 bitop3:0xf6\n"
    "v_bitop3_b32 v224, v224, v55, s66 bitop3:0xf6\n"
    "v_bitop3_b32 v224, v224, v59, s66 bitop3:0xf6\n"
    "v_bitop3_b32 v224, v224, v63, s66 bitop3:0xf6\n"
    "v_cmp_ne_u32 vcc, 0, v224\n"
    "s_sub_u32 s67, s67, 1\n"
    "s_cmp_lt_i32 s67, 0\n"
    "s_cbranch_scc1 RB8_%=\n"
    "s_cbranch_vccnz RT8_%=\n"
    "s_branch RB8_%=\n"
    "RT9_%=:\n"
    "s_sleep 1\n"
    "buffer_load_dword v248, v247, s[20:23], s77 offen offset:256 sc1\n"
    "s_waitcnt vmcnt(0)\n"
    "v_cmp_ne_u32 vcc, s66, v248\n"
    "s_sub_u32 s67, s67, 1\n"
    "s_cmp_lt_i32 s67, 0\n"
    "s_cbranch_scc1 RB9_%=\n"
    "s_cbranch_vccnz RT9_%=\n"
    "s_branch RB9_%=\n"
    "RT10_%=:\n"
    "s_sleep 1\n"
    "buffer_load_dword v248, v247, s[20:23], s77 offen offset:512 sc1\n"
    "s_waitcnt vmcnt(0)\n"
    "v_cmp_ne_u32 vcc, s66, v248\n"
    "s_sub_u32 s67, s67, 1\n"
    "s_cmp_lt_i32 s67, 0\n"
    "s_cbranch_scc1 RB10_%=\n"
    "s_cbranch_vccnz RT10_%=\n"
    "s_branch RB10_%=\n"
    "RT11_%=:\n"
    "s_sleep 1\n"
    "s_add_u32 s86, s61, 0\n"
    "buffer_load_dwordx4 v[0:3], v254, s[48:51], s86 offen offset:0 sc1\n"
    "buffer_load_dword v236, v249, s[48:51], s86 offen offset:1024 sc1\n"
    "buffer_load_dwordx4 v[4:7], v254, s[48:51], s86 offen offset:1280 sc1\n"
    "buffer_load_dword v237, v249, s[48:51], s86 offen offset:2304 sc1\n"
    "buffer_load_dwordx4 v[8:11], v254, s[48:51], s86 offen offset:2560 sc1\n"
    "buffer_load_dword v238, v249, s[48:51], s86 offen offset:3584 sc1\n"
    "s_add_u32 s86, s61, 3840\n"
    "buffer_load_dwordx4 v[12:15], v254, s[48:51], s86 offen offset:0 sc1\n"
    "buffer_load_dword v239, v249, s[48:51], s86 offen offset:1024 sc1\n"
    "buffer_load_dwordx4 v[16:19], v254, s[48:51], s86 offen offset:1280 sc1\n"
    "buffer_load_dword v240, v249, s[48:51], s86 offen offset:2304 sc1\n"
    "buffer_load_dwordx4 v[20:23], v254, s[48:51], s86 offen offset:2560 sc1\n"
    "buffer_load_dword v241, v249, s[48:51], s86 offen offset:3584 sc1\n"
    "s_add_u32 s86, s61, 7680\n"
    "buffer_load_dwordx4 v[24:27], v254, s[48:51], s86 offen offset:0 sc1\n"
    "buffer_load_dword v242, v249, s[48:51], s86 offen offset:1024 sc1\n"
    "buffer_load_dwordx4 v[28:31], v254, s[48:51], s86 offen offset:1280 sc1\n"
    "buffer_load_dword v243, v249, s[48:51], s86 offen offset:2304 sc1\n"
    "s_waitcnt vmcnt(0)\n"
    "v_xor_b32 v248, v3, v250\n"
    "v_bitop3_b32 v248, v248, v236, v250 bitop3:0xf6\n"
    "v_bitop3_b32 v248, v248, v7, v250 bitop3:0xf6\n"
    "v_bitop3_b32 v248, v248, v237, v250 bitop3:0xf6\n"
    "v_bitop3_b32 v248, v248, v11, v250 bitop3:0xf6\n"
    "v_bitop3_b32 v248, v248, v238, v250 bitop3:0xf6\n"
    "v_bitop3_b32 v248, v248, v15, v250 bitop3:0xf6\n"
    "v_bitop3_b32 v248, v248, v239, v250 bitop3:0xf6\n"
    "v_bitop3_b32 v248, v248, v19, v250 bitop3:0xf6\n"
    "v_bitop3_b32 v248, v248, v240, v250 bitop3:0xf6\n"
    "v_bitop3_b32 v248, v248, v23, v250 bitop3:0xf6\n"
    "v_bitop3_b32 v248, v248, v241, v250 bitop3:0xf6\n"
    "v_bitop3_b32 v248, v248, v27, v250 bitop3:0xf6\n"
    "v_bitop3_b32 v248, v248, v242, v250 bitop3:0xf6\n"
    "v_bitop3_b32 v248, v248, v31, v250 bitop3:0xf6\n"
    "v_bitop3_b32 v248, v248, v243, v250 bitop3:0xf6\n"
    "v_cmp_lt_u32 vcc, 0xffff, v248\n"
    "s_nop 1\n"
    "s_and_b64 vcc, vcc, s[64:65]\n"
    "s_sub_u32 s67, s67, 1\n"
    "s_cmp_lt_i32 s67, 0\n"
    "s_cbranch_scc1 RB11_%=\n"
    "s_cbranch_vccnz RT11_%=\n"
    "s_branch RB11_%=\n"
    "RT12_%=:\n"
    "s_sleep 1\n"
    "s_add_u32 s86, s59, 0\n"
    "buffer_load_dwordx4 v[192:195], v254, s[20:23], s86 offen sc1\n"
    "buffer_load_dwordx4 v[196:199], v254, s[20:23], s86 offen offset:1024 sc1\n"
    "buffer_load_dwordx2 v[200:201], v252, s[20:23], s86 offen offset:2048 sc1\n"
    "s_waitcnt vmcnt(0)\n"
    "v_xor_b32 v248, v195, v250\n"
    "v_bitop3_b32 v248, v248, v199, v250 bitop3:0xf6\n"
    "v_bitop3_b32 v248, v248, v200, v250 bitop3:0xf6\n"
    "v_bitop3_b32 v248, v248, v201, v250 bitop3:0xf6\n"
    "v_cmp_lt_u32 vcc, 0xffff, v248\n"
    "s_sub_u32 s67, s67, 1\n"
    "s_cmp_lt_i32 s67, 0\n"
    "s_cbranch_scc1 RB12_%=\n"
    "s_cbranch_vccnz RT12_%=\n"
    "s_branch RB12_%=\n"
    "RT13_%=:\n"
    "s_sleep 1\n"
    "s_add_u32 s86, s59, 2560\n"
    "buffer_load_dwordx4 v[32:35], v254, s[20:23], s86 offen sc1\n"
    "buffer_load_dwordx4 v[36:39], v254, s[20:23], s86 offen offset:1024 sc1\n"
    "buffer_load_dwordx2 v[40:41], v252, s[20:23], s86 offen offset:2048 sc1\n"
    "s_waitcnt vmcnt(0)\n"
    "v_xor_b32 v248, v35, v250\n"
    "v_bitop3_b32 v248, v248, v39, v250 bitop3:0xf6\n"
    "v_bitop3_b32 v248, v248, v40, v250 bitop3:0xf6\n"
    "v_bitop3_b32 v248, v248, v41, v250 bitop3:0xf6\n"
    "v_cmp_lt_u32 vcc, 0xffff, v248\n"
    "s_sub_u32 s67, s67, 1\n"
    "s_cmp_lt_i32 s67, 0\n"
    "s_cbranch_scc1 RB13_%=\n"
    "s_cbranch_vccnz RT13_%=\n"
    "s_branch RB13_%=\n"
    "RT14_%=:\n"
    "s_sleep 1\n"
    "s_add_u32 s86, s59, 5120\n"
    "buffer_load_dwordx4 v[48:51], v254, s[20:23], s86 offen sc1\n"
    "buffer_load_dwordx4 v[52:55], v254, s[20:23], s86 offen offset:1024 sc1\n"
    "buffer_load_dwordx2 v[56:57], v252, s[20:23], s86 offen offset:2048 sc1\n"
    "s_waitcnt vmcnt(0)\n"
    "v_xor_b32 v248, v51, v250\n"
    "v_bitop3_b32 v248, v248, v55, v250 bitop3:0xf6\n"
    "v_bitop3_b32 v248, v248, v56, v250 bitop3:0xf6\n"
    "v_bitop3_b32 v248, v248, v57, v250 bitop3:0xf6\n"
    "v_cmp_lt_u32 vcc, 0xffff, v248\n"
    "s_sub_u32 s67, s67, 1\n"
    "s_cmp_lt_i32 s67, 0\n"
    "s_cbranch_scc1 RB14_%=\n"
    "s_cbranch_vccnz RT14_%=\n"
    "s_branch RB14_%=\n"
    "RT15_%=:\n"
    "s_sleep 1\n"
    "s_add_u32 s86, s59, 7680\n"
    "buffer_load_dwordx4 v[64:67], v254, s[20:23], s86 offen sc1\n"
    "buffer_load_dwordx4 v[68:71], v254, s[20:23], s86 offen offset:1024 sc1\n"
    "buffer_load_dwordx2 v[72:73], v252, s[20:23], s86 offen offset:2048 sc1\n"
    "s_waitcnt vmcnt(0)\n"
    "v_xor_b32 v248, v67, v250\n"
    "v_bitop3_b32 v248, v248, v71, v250 bitop3:0xf6\n"
    "v_bitop3_b32 v248, v248, v72, v250 bitop3:0xf6\n"
    "v_bitop3_b32 v248, v248, v73, v250 bitop3:0xf6\n"
    "v_cmp_lt_u32 vcc, 0xffff, v248\n"
    "s_sub_u32 s67, s67, 1\n"
    "s_cmp_lt_i32 s67, 0\n"
    "s_cbranch_scc1 RB15_%=\n"
    "s_cbranch_vccnz RT15_%=\n"
    "s_branch RB15_%=\n"
    "RT16_%=:\n"
    "s_sleep 1\n"
    "s_add_u32 s86, s59, 10240\n"
    "buffer_load_dwordx4 v[80:83], v254, s[20:23], s86 offen sc1\n"
    "buffer_load_dwordx4 v[84:87], v254, s[20:23], s86 offen offset:1024 sc1\n"
    "buffer_load_dwordx2 v[88:89], v252, s[20:23], s86 offen offset:2048 sc1\n"
    "s_waitcnt vmcnt(0)\n"
    "v_xor_b32 v248, v83, v250\n"
    "v_bitop3_b32 v248, v248, v87, v250 bitop3:0xf6\n"
    "v_bitop3_b32 v248, v248, v88, v250 bitop3:0xf6\n"
    "v_bitop3_b32 v248, v248, v89, v250 bitop3:0xf6\n"
    "v_cmp_lt_u32 vcc, 0xffff, v248\n"
    "s_sub_u32 s67, s67, 1\n"
    "s_cmp_lt_i32 s67, 0\n"
    "s_cbranch_scc1 RB16_%=\n"
    "s_cbranch_vccnz RT16_%=\n"
    "s_branch RB16_%=\n"
    "RT17_%=:\n"
    "s_sleep 1\n"
    "s_add_u32 s86, s59, 12800\n"
    "buffer_load_dwordx4 v[96:99], v254, s[20:23], s86 offen sc1\n"
    "buffer_load_dwordx4 v[100:103], v254, s[20:23], s86 offen offset:1024 sc1\n"
    "buffer_load_dwordx2 v[104:105], v252, s[20:23], s86 offen offset:2048 sc1\n"
    "s_waitcnt vmcnt(0)\n"
    "v_xor_b32 v248, v99, v250\n"
    "v_bitop3_b32 v248, v248, v103, v250 bitop3:0xf6\n"
    "v_bitop3_b32 v248, v248, v104, v250 bitop3:0xf6\n"
    "v_bitop3_b32 v248, v248, v105, v250 bitop3:0xf6\n"
    "v_cmp_lt_u32 vcc, 0xffff, v248\n"
    "s_sub_u32 s67, s67, 1\n"
    "s_cmp_lt_i32 s67, 0\n"
    "s_cbranch_scc1 RB17_%=\n"
    "s_cbranch_vccnz RT17_%=\n"
    "s_branch RB17_%=\n"
    "RT18_%=:\n"
    "s_sleep 1\n"
    "s_add_u32 s86, s59, 15360\n"
    "buffer_load_dwordx4 v[0:3], v254, s[20:23], s86 offen sc1\n"
    "buffer_load_dwordx4 v[4:7], v254, s[20:23], s86 offen offset:1024 sc1\n"
    "buffer_load_dwordx2 v[8:9], v252, s[20:23], s86 offen offset:2048 sc1\n"
    "s_waitcnt vmcnt(0)\n"
    "v_xor_b32 v248, v3, v250\n"
    "v_bitop3_b32 v248, v248, v7, v250 bitop3:0xf6\n"
    "v_bitop3_b32 v248, v248, v8, v250 bitop3:0xf6\n"
    "v_bitop3_b32 v248, v248, v9, v250 bitop3:0xf6\n"
    "v_cmp_lt_u32 vcc, 0xffff, v248\n"
    "s_sub_u32 s67, s67, 1\n"
    "s_cmp_lt_i32 s67, 0\n"
    "s_cbranch_scc1 RB18_%=\n"
    "s_cbranch_vccnz RT18_%=\n"
    "s_branch RB18_%=\n"
    "PCE_%=:\n"
    : "+v"(tid)
    : "s"(__builtin_amdgcn_kernarg_segment_ptr()), "s"(blockIdx.x), "s"((u32)(size_t)lds)
    : "v1", "v2", "v3", "v4", "v5", "v6", "v7", "v8", "v9", "v10", "v11", "v12", "v13", "v14", "v15", "v16", "v17", "v18", "v19", "v20", "v21", "v22", "v23", "v24", "v25", "v26", "v27", "v28", "v29", "v30", "v31", "v32", "v33", "v34", "v35", "v36", "v37", "v38", "v39", "v40", "v41", "v42", "v43", "v44", "v45", "v46", "v47", "v48", "v49", "v50", "v51", "v52", "v53", "v54", "v55", "v56", "v57", "v58", "v59", "v60", "v61", "v62", "v63", "v64", "v65", "v66", "v67", "v68", "v69", "v70", "v71", "v72", "v73", "v74", "v75", "v76", "v77", "v78", "v79", "v80", "v81", "v82", "v83", "v84", "v85", "v86", "v87", "v88", "v89", "v90", "v91", "v92", "v93", "v94", "v95", "v96", "v97", "v98", "v99", "v100", "v101", "v102", "v103", "v104", "v105", "v106", "v107", "v108", "v109", "v110", "v111", "v112", "v113", "v114", "v115", "v116", "v117", "v118", "v119", "v120", "v121", "v122", "v123", "v124", "v125", "v126", "v127", "v128", "v129", "v130", "v131", "v132", "v133", "v134", "v135", "v136", "v137", "v138", "v139", "v140", "v141", "v142", "v143", "v144", "v145", "v146", "v147", "v148", "v149", "v150", "v151", "v152", "v153", "v154", "v155", "v156", "v157", "v158", "v159", "v160", "v161", "v162", "v163", "v164", "v165", "v166", "v167", "v168", "v169", "v170", "v171", "v172", "v173", "v174", "v175", "v176", "v177", "v178", "v179", "v180", "v181", "v182", "v183", "v184", "v185", "v186", "v187", "v188", "v189", "v190", "v191", "v192", "v193", "v194", "v195", "v196", "v197", "v198", "v199", "v200", "v201", "v202", "v203", "v204", "v205", "v206", "v207", "v208", "v209", "v210", "v211", "v212", "v213", "v214", "v215", "v216", "v217", "v218", "v219", "v220", "v221", "v222", "v223", "v224", "v225", "v226", "v227", "v228", "v229", "v230", "v231", "v232", "v233", "v234", "v235", "v236", "v237", "v238", "v239", "v240", "v241", "v242", "v243", "v244", "v245", "v246", "v247", "v248", "v249", "v250", "v251", "v252", "v253", "v254", "v255", "a0", "a1", "a2", "a3", "a4", "a5", "a6", "a7", "a8", "a9", "a10", "a11", "a12", "a13", "a14", "a15", "a16", "a17", "a18", "a19", "a20", "a21", "a22", "a23", "a24", "a25", "a26", "a27", "a28", "a29", "a30", "a31", "a32", "a33", "a34", "a35", "a36", "a37", "a38", "a39", "a40", "a41", "a42", "a43", "a44", "a45", "a46", "a47", "a48", "a49", "a50", "a51", "a52", "a53", "a54", "a55", "a56", "a57", "a58", "a59", "a60", "a61", "a62", "a63", "a64", "a65", "a66", "a67", "a68", "a69", "a70", "a71", "a72", "a73", "a74", "a75", "a76", "a77", "a78", "a79", "a80", "a81", "a82", "a83", "a84", "a85", "a86", "a87", "a88", "a89", "a90", "a91", "a92", "a93", "a94", "a95", "a96", "a97", "a98", "a99", "a100", "a101", "a102", "a103", "a104", "a105", "a106", "a107", "a108", "a109", "a110", "a111", "a112", "a113", "a114", "a115", "a116", "a117", "a118", "a119", "a120", "a121", "a122", "a123", "a124", "a125", "a126", "a127", "a128", "a129", "a130", "a131", "a132", "a133", "a134", "a135", "a136", "a137", "a138", "a139", "a140", "a141", "a142", "a143", "a144", "a145", "a146", "a147", "a148", "a149", "a150", "a151", "a152", "a153", "a154", "a155", "a156", "a157", "a158", "a159", "a160", "a161", "a162", "a163", "a164", "a165", "a166", "a167", "a168", "a169", "a170", "a171", "a172", "a173", "a174", "a175", "a176", "a177", "a178", "a179", "a180", "a181", "a182", "a183", "a184", "a185", "a186", "a187", "a188", "a189", "a190", "a191", "a192", "a193", "a194", "a195", "a196", "a197", "a198", "a199", "a200", "a201", "a202", "a203", "a204", "a205", "a206", "a207", "a208", "a209", "a210", "a211", "a212", "a213", "a214", "a215", "a216", "a217", "a218", "a219", "a220", "a221", "a222", "a223", "a224", "a225", "a226", "a227", "a228", "a229", "a230", "a231", "a232", "a233", "a234", "a235", "a236", "a237", "a238", "a239", "a240", "a241", "a242", "a243", "a244", "a245", "a246", "a247", "a248", "a249", "a250", "a251", "a252", "a253", "a254", "a255", "s4", "s5", "s6", "s7", "s8", "s9", "s10", "s11", "s12", "s13", "s14", "s15", "s16", "s17", "s18", "s19", "s20", "s21", "s22", "s23", "s24", "s25", "s26", "s27", "s28", "s29", "s30", "s31", "s32", "s33", "s34", "s35", "s36", "s37", "s38", "s39", "s40", "s41", "s42", "s43", "s44", "s45", "s46", "s47", "s48", "s49", "s50", "s51", "s52", "s53", "s54", "s55", "s56", "s57", "s58", "s59", "s60", "s61", "s62", "s63", "s64", "s65", "s66", "s67", "s68", "s69", "s70", "s71", "s72", "s73", "s74", "s75", "s76", "s77", "s78", "s79", "s80", "s81", "s82", "s83", "s84", "s85", "s86", "s87", "s88", "s89", "s90", "s91", "s92", "s93", "s94", "s95", "s96", "s97", "s98", "s99", "s100", "s101", "vcc", "scc", "memory");
}
extern "C" void kernel_launch(void* const* d_in, const int* in_sizes, int n_in, void* d_out, int out_size, void* d_ws, size_t ws_size, hipStream_t stream) {
  const u32 F = 0x20000u;
  gm_fused<<<256, 256, 0, stream>>>(d_in[0], 2359296u, F, d_in[4], 5308416u, F, d_in[1], 1990656u, F, d_ws, 0x8000000u, F,
                                    d_in[6], 5308416u, F, d_in[2], 663552u, F, d_out, 2359296u, F, d_in[8], 1152u, F,
                                    d_in[5], 9216u, F, d_in[7], 1152u, F);
}
```
